```python
import torch

def _softmax(x, axis=-1):
    return torch.softmax(x, dim=axis)
def _gelu(x):
    return torch.nn.functional.gelu(x, approximate="tanh")
def _silu(x):
    return torch.nn.functional.silu(x)
def _sigmoid(x):
    return torch.sigmoid(x)
def _layernorm(x, axis=-1, eps=1e-5):
    m = x.mean(dim=axis, keepdim=True)
    v = x.var(dim=axis, keepdim=True, unbiased=False)
    return (x - m) / torch.sqrt(v + eps)
def _scatter_add(tgt, idx, src, axis=0):
    return tgt.clone().index_add_(axis, idx, src)
def _segment_sum(src, seg, n):
    out = torch.zeros((n,) + src.shape[1:], dtype=src.dtype)
    return out.index_add_(0, seg, src)
def _pad(x, pw):
    flat = tuple(v for pair in reversed(pw) for v in pair)
    return torch.nn.functional.pad(x, flat)

kernel_name = 'interleave_layers__gqa_block_cached__glu_gated_bilinear__decode'

def setup_inputs():

    _w = (lambda a: (lambda t: t.to(torch.bfloat16) if t.is_floating_point() else t)(torch.as_tensor(a)))
    def _A_setup():
        B, Sq, D = 2, 1, 512
        Hq, Hkv, Sk, Dh = 32, 32, 16384, 128
        assert Hq % Hkv == 0, (Hq, Hkv)
        return {
            "x": _w((lambda *s: torch.randn(*s, dtype=torch.bfloat16) * 0.05)(B, Sq, D)),
            "k_cache": _w((lambda *s: torch.randn(*s, dtype=torch.bfloat16) * 0.8)(B, Hkv, Sk, Dh)),
            "v_cache": _w((lambda *s: torch.randn(*s, dtype=torch.bfloat16) * 1.6)(B, Hkv, Sk, Dh)),
            "wq": _w((lambda *s: torch.randn(*s, dtype=torch.bfloat16) * 0.8)(D, Hq * Dh) * (D ** -0.5)),
            "wo": _w((lambda *s: torch.randn(*s, dtype=torch.bfloat16) * 1.6)(Hq * Dh, D) * ((Hq * Dh) ** -0.5)),
        }
    _A_in = _A_setup()

    def _B_setup():
        B, S, D, F = 2, 1, 512, 1376
        return {
            "x": _w((lambda *s: torch.randn(*s, dtype=torch.bfloat16) * 0.05)(B, S, D)),
            "w_gate": _w((lambda *s: torch.randn(*s, dtype=torch.bfloat16) * 0.4)(D, F) * (D ** -0.5)),
            "w_up": _w((lambda *s: torch.randn(*s, dtype=torch.bfloat16) * 0.4)(D, F) * (D ** -0.5)),
            "w_down": _w((lambda *s: torch.randn(*s, dtype=torch.bfloat16) * 0.4)(F, D) * (F ** -0.5)),
        }
    _B_in = _B_setup()

    return {
        "A_x": _A_in["x"],
        "A_k_cache": _A_in["k_cache"],
        "A_v_cache": _A_in["v_cache"],
        "A_wq": _A_in["wq"],
        "A_wo": _A_in["wo"],
        "B_x": _B_in["x"],
        "B_w_gate": _B_in["w_gate"],
        "B_w_up": _B_in["w_up"],
        "B_w_down": _B_in["w_down"],
    }

def reference(A_x, A_k_cache, A_v_cache, A_wq, A_wo, B_x, B_w_gate, B_w_up, B_w_down):
    def _A_ref(x, k_cache, v_cache, wq, wo):
        B, Sq, D = x.shape
        Hq, Dh = 32, 128
        Hkv = k_cache.shape[1]
        rep = Hq // Hkv
        q = (lambda x, *d: x.permute(*d))((x @ wq).reshape(B, Sq, Hq, Dh), 0, 2, 1, 3)
        k = (lambda x, n, axis: torch.repeat_interleave(x, n, dim=axis))(k_cache, rep, axis=1)
        v = (lambda x, n, axis: torch.repeat_interleave(x, n, dim=axis))(v_cache, rep, axis=1)
        scores = torch.einsum("bhqd,bhkd->bhqk", q, k) * (Dh ** -0.5)
        p = _softmax(scores, axis=-1)
        o = torch.einsum("bhqk,bhkd->bhqd", p, v)
        return (lambda x, *d: x.permute(*d))(o, 0, 2, 1, 3).reshape(B, Sq, Hq * Dh) @ wo

    def _B_ref(x, w_gate, w_up, w_down):
        g = torch.einsum("bsd,df->bsf", x, w_gate)
        u = torch.einsum("bsd,df->bsf", x, w_up)
        h = g * u
        return torch.einsum("bsf,fd->bsd", h, w_down)

    x = A_x
    x = x + _A_ref(_layernorm(x, axis=-1), A_k_cache, A_v_cache, A_wq, A_wo)
    x = x + _B_ref(_layernorm(x, axis=-1), B_w_gate, B_w_up, B_w_down)
    x = x + _A_ref(_layernorm(x, axis=-1), A_k_cache, A_v_cache, A_wq, A_wo)
    x = x + _B_ref(_layernorm(x, axis=-1), B_w_gate, B_w_up, B_w_down)
    return x
```

```cpp
#include <hip/hip_runtime.h>
#include <hip/hip_bf16.h>
#include <stdint.h>

typedef unsigned short u16;
typedef unsigned v4u __attribute__((ext_vector_type(4)));
constexpr int B_=2, D_=512, H_=32, SK=16384, DH=128, F_=1376;
constexpr int NSPLIT=16;

__device__ __forceinline__ float bf2f(u16 x){ return __uint_as_float(((unsigned)x)<<16); }
__device__ __forceinline__ float rbf(float f){ unsigned u=__float_as_uint(f); u += 0x7FFFu + ((u>>16)&1u); return __uint_as_float(u & 0xFFFF0000u); }
__device__ __forceinline__ u16 f2bf(float f){ unsigned u=__float_as_uint(f); u += 0x7FFFu + ((u>>16)&1u); return (u16)(u>>16); }

template<int K, bool LN>
__global__ void __launch_bounds__(256) gemv_k(const float* __restrict__ xin, const u16* __restrict__ W, int N, float* __restrict__ out, const float* __restrict__ resid){
  __shared__ float z[2][K]; __shared__ float red[4][2][64]; __shared__ float st[2][2];
  const int t=threadIdx.x;
  for(int i=t;i<2*K;i+=256) z[i/K][i%K]=xin[i];
  __syncthreads();
  if(LN){
    if(t<128){ int b=t>>6, l=t&63; float s=0; for(int k=l;k<K;k+=64) s+=z[b][k]; for(int o=32;o;o>>=1) s+=__shfl_xor(s,o); float m=s/K; float v=0; for(int k=l;k<K;k+=64){ float d=z[b][k]-m; v+=d*d;} for(int o=32;o;o>>=1) v+=__shfl_xor(v,o); if(l==0){ st[b][0]=m; st[b][1]=1.0f/sqrtf(v/K+1e-5f);} }
    __syncthreads();
    for(int i=t;i<2*K;i+=256){ int b=i/K; z[b][i%K]=rbf((z[b][i%K]-st[b][0])*st[b][1]); }
    __syncthreads();
  }
  const int c=blockIdx.x*64+(t&63), kg=t>>6;
  float a0=0,a1=0;
  if(c<N) for(int k=kg;k<K;k+=4){ float w=bf2f(W[(size_t)k*N+c]); a0+=z[0][k]*w; a1+=z[1][k]*w; }
  red[kg][0][t&63]=a0; red[kg][1][t&63]=a1;
  __syncthreads();
  if(t<128){ int b=t>>6,l=t&63; int cc=blockIdx.x*64+l; if(cc<N){ float s=red[0][b][l]+red[1][b][l]+red[2][b][l]+red[3][b][l]; s=rbf(s); if(resid) s=rbf(resid[b*N+cc]+s); out[b*N+cc]=s; } }
}
__global__ void cvt_in(const u16* x, float* o, int n){ int i=blockIdx.x*256+threadIdx.x; if(i<n) o[i]=bf2f(x[i]); }
__global__ void cvt_out(const float* x, u16* o, int n){ int i=blockIdx.x*256+threadIdx.x; if(i<n) o[i]=f2bf(x[i]); }
__global__ void mul_k(const float* g, const float* u, float* h, int n){ int i=blockIdx.x*256+threadIdx.x; if(i<n) h[i]=rbf(g[i]*u[i]); }

__global__ void __launch_bounds__(256) attn_split(const float* __restrict__ q, const u16* __restrict__ Kc, const u16* __restrict__ Vc, float* __restrict__ pm, float* __restrict__ po){
  const int s=blockIdx.x, bh=blockIdx.y, t=threadIdx.x, lane=t&63, w=t>>6, r=lane>>4, c=lane&15;
  const int b=bh>>5, h=bh&31;
  float qf[8];
  for(int j=0;j<8;j++) qf[j]=q[b*4096+h*128+c*8+j];
  const float sc=0.08838834764831845f*1.4426950408889634f;
  constexpr int KPS=SK/NSPLIT;
  const size_t base=(size_t)bh*SK*DH;
  float m=-INFINITY,l=0,acc[8]; for(int j=0;j<8;j++) acc[j]=0;
  for(int k0=s*KPS+w*4+r; k0<(s+1)*KPS; k0+=16){
    v4u kk=__builtin_nontemporal_load((const v4u*)(Kc+base+(size_t)k0*DH+c*8));
    v4u vv=__builtin_nontemporal_load((const v4u*)(Vc+base+(size_t)k0*DH+c*8));
    float d=0;
    for(int j=0;j<4;j++){ d+=__uint_as_float(kk[j]<<16)*qf[2*j]; d+=__uint_as_float(kk[j]&0xFFFF0000u)*qf[2*j+1]; }
    d+=__shfl_xor(d,1); d+=__shfl_xor(d,2); d+=__shfl_xor(d,4); d+=__shfl_xor(d,8);
    d*=sc;
    float mn=fmaxf(m,d); float al=exp2f(m-mn); float p=exp2f(d-mn); m=mn; l=l*al+p;
    for(int j=0;j<4;j++){ acc[2*j]=acc[2*j]*al+p*__uint_as_float(vv[j]<<16); acc[2*j+1]=acc[2*j+1]*al+p*__uint_as_float(vv[j]&0xFFFF0000u); }
  }
  __shared__ float sm[16], sl[16], so[16][128];
  if(c==0){ sm[w*4+r]=m; sl[w*4+r]=l; }
  for(int j=0;j<8;j++) so[w*4+r][c*8+j]=acc[j];
  __syncthreads();
  if(t<128){
    float M=-INFINITY; for(int i=0;i<16;i++) M=fmaxf(M,sm[i]);
    float L=0,o=0; for(int i=0;i<16;i++){ float a=exp2f(sm[i]-M); L+=a*sl[i]; o+=a*so[i][t]; }
    po[((size_t)bh*NSPLIT+s)*128+t]=o; if(t==0){ pm[(bh*NSPLIT+s)*2]=M; pm[(bh*NSPLIT+s)*2+1]=L; }
  }
}
__global__ void attn_comb(const float* pm, const float* po, float* o){
  const int bh=blockIdx.x,t=threadIdx.x;
  float M=-INFINITY; for(int i=0;i<NSPLIT;i++) M=fmaxf(M,pm[(bh*NSPLIT+i)*2]);
  float L=0,a=0; for(int i=0;i<NSPLIT;i++){ float w=exp2f(pm[(bh*NSPLIT+i)*2]-M); L+=w*pm[(bh*NSPLIT+i)*2+1]; a+=w*po[((size_t)bh*NSPLIT+i)*128+t]; }
  o[bh*128+t]=rbf(a/L);
}

extern "C" void kernel_launch(void* const* d_in, const int* in_sizes, int n_in, void* d_out, int out_size, void* d_ws, size_t ws_size, hipStream_t stream){
  const u16* x=(const u16*)d_in[0]; const u16* Kc=(const u16*)d_in[1]; const u16* Vc=(const u16*)d_in[2];
  const u16* wq=(const u16*)d_in[3]; const u16* wo=(const u16*)d_in[4];
  const u16* wg=(const u16*)d_in[6]; const u16* wu=(const u16*)d_in[7]; const u16* wd=(const u16*)d_in[8];
  float* ws=(float*)d_ws;
  float* xa=ws; float* xb=ws+1024; float* q=ws+2048; float* o=q+8192; float* g=o+8192; float* u=g+2752; float* h=u+2752; float* pm=h+2752+64; float* po=pm+64*NSPLIT*2;
  cvt_in<<<4,256,0,stream>>>(x,xa,1024);
  for(int L=0;L<2;L++){
    gemv_k<512,true><<<64,256,0,stream>>>(xa,wq,4096,q,nullptr);
    attn_split<<<dim3(NSPLIT,64),256,0,stream>>>(q,Kc,Vc,pm,po);
    attn_comb<<<64,128,0,stream>>>(pm,po,o);
    gemv_k<4096,false><<<8,256,0,stream>>>(o,wo,512,xb,xa);
    gemv_k<512,true><<<22,256,0,stream>>>(xb,wg,F_,g,nullptr);
    gemv_k<512,true><<<22,256,0,stream>>>(xb,wu,F_,u,nullptr);
    mul_k<<<11,256,0,stream>>>(g,u,h,2752);
    gemv_k<F_,false><<<8,256,0,stream>>>(h,wd,512,xa,xb);
  }
  cvt_out<<<4,256,0,stream>>>(xa,(u16*)d_out,1024);
}
```

```cpp
#include <hip/hip_runtime.h>
#include <stdint.h>

typedef unsigned short u16;
typedef unsigned v4u __attribute__((ext_vector_type(4)));
typedef unsigned v2u __attribute__((ext_vector_type(2)));
typedef float f4 __attribute__((ext_vector_type(4)));
typedef short bf16x8 __attribute__((ext_vector_type(8)));
#define LDSP __attribute__((address_space(3)))

constexpr int NCHAIN = 32, NSPLIT = 7;
constexpr unsigned WS_Q = 4096;
constexpr unsigned WS_RECO = 0x20000;
constexpr unsigned WS_RECML = 0x110000;
constexpr unsigned WS_Y = 0x120000;
constexpr unsigned WS_XP = 0x140000;
constexpr unsigned WS_X2 = 0x1C0000;
constexpr unsigned WS_BYTES = 0x400000;
constexpr int SPIN_LIMIT = 40000;

constexpr int L_WQ = 0;
constexpr int L_SCR = 131072;
constexpr int L_ZB = L_SCR + 16384;
constexpr int L_XF = L_ZB + 1024;
constexpr int L_ST = L_XF + 2048;
constexpr int L_HB = L_ST + 64;
constexpr int L_LIN = L_HB + 128;
constexpr int L_R3 = L_LIN + 2048;

typedef short v4s __attribute__((ext_vector_type(4)));
__device__ __forceinline__ float bf2f(unsigned x) { return __uint_as_float(x << 16); }
__device__ __forceinline__ unsigned cvtpk(float lo, float hi) { unsigned r; asm("v_cvt_pk_bf16_f32 %0, %1, %2" : "=v"(r) : "v"(lo), "v"(hi)); return r; }
__device__ __forceinline__ float rbf(float f) { return __uint_as_float(cvtpk(f, f) & 0xFFFF0000u); }
template <int CTRL> __device__ __forceinline__ float dpp(float x) {
  return __builtin_bit_cast(float, __builtin_amdgcn_mov_dpp(__builtin_bit_cast(int, x), CTRL, 0xf, 0xf, true));
}
__device__ __forceinline__ float xrow_sum(float x) {
  auto s = __builtin_amdgcn_permlane16_swap(__float_as_uint(x), __float_as_uint(x), false, false);
  x = __uint_as_float(s[0]) + __uint_as_float(s[1]);
  auto t = __builtin_amdgcn_permlane32_swap(__float_as_uint(x), __float_as_uint(x), false, false);
  return __uint_as_float(t[0]) + __uint_as_float(t[1]);
}
__device__ __forceinline__ float wave_sum(float x) {
  x += dpp<0xB1>(x); x += dpp<0x4E>(x); x += dpp<0x124>(x); x += dpp<0x128>(x);
  return xrow_sum(x);
}
__device__ __forceinline__ v4u ldnt(const void* p) { return __builtin_nontemporal_load((const v4u*)p); }
__device__ __forceinline__ v4u ld_sc1(__amdgpu_buffer_rsrc_t rs, unsigned off) { return __builtin_amdgcn_raw_buffer_load_b128(rs, off, 0, 16); }
__device__ __forceinline__ void st_pair(__amdgpu_buffer_rsrc_t rs, unsigned off, float a, float b, unsigned tag) {
  v4u v = {__float_as_uint(a), tag, __float_as_uint(b), tag};
  __builtin_amdgcn_raw_buffer_store_b128(v, rs, off, 0, 16);
}
template <typename T> __device__ __forceinline__ T lds_ld(LDSP char* L, int off) { return *(LDSP T*)(L + off); }
template <typename T> __device__ __forceinline__ void lds_st(LDSP char* L, int off, T v) { *(LDSP T*)(L + off) = v; }
#define WAVE_FENCE() asm volatile("" ::: "memory")

template <bool AG> __device__ __forceinline__ void mfma_w2(f4& a0, const v4u& w0, const v4u& b0, f4& a1, const v4u& w1, const v4u& b1) {
  if constexpr (AG) asm("s_nop 1\n\tv_mfma_f32_16x16x32_bf16 %0, %2, %4, %0\n\tv_mfma_f32_16x16x32_bf16 %1, %3, %5, %1" : "+v"(a0), "+v"(a1) : "a"(w0), "a"(w1), "v"(b0), "v"(b1));
  else asm("s_nop 1\n\tv_mfma_f32_16x16x32_bf16 %0, %2, %4, %0\n\tv_mfma_f32_16x16x32_bf16 %1, %3, %5, %1" : "+v"(a0), "+v"(a1) : "v"(w0), "v"(w1), "v"(b0), "v"(b1));
}
__device__ __forceinline__ void mfma_end(f4& a, f4& b) { asm volatile("s_nop 7\n\ts_nop 1" : "+v"(a), "+v"(b)); }
__device__ __forceinline__ v4u tr_frag(LDSP char* L, int base, int RS, int lane) {
  const int n = lane & 15, g = lane >> 4;
  const int a = base + (8 * g + (n >> 2)) * RS + (n & 3) * 8;
  const v4s lo = __builtin_amdgcn_ds_read_tr16_b64_v4i16((LDSP v4s*)(L + a)), hi = __builtin_amdgcn_ds_read_tr16_b64_v4i16((LDSP v4s*)(L + a + 4 * RS));
  const v2u l2 = __builtin_bit_cast(v2u, lo), h2 = __builtin_bit_cast(v2u, hi);
  return v4u{l2.x, l2.y, h2.x, h2.y};
}
__device__ __forceinline__ void tr_coop(LDSP char* L, int kb, int w, int lane, v4u p0, v4u p1, int c0, int c1, v4u& f0, v4u& f1) {
  const int img = L_SCR + (kb & 1) * 8192;
  lds_st<v4u>(L, img + w * 2048 + lane * 16, p0); lds_st<v4u>(L, img + w * 2048 + 1024 + lane * 16, p1);
  __syncthreads();
  f0 = tr_frag(L, img + c0, 256, lane); f1 = tr_frag(L, img + c1, 256, lane);
  asm volatile("" : "+v"(f0), "+v"(f1) :: "memory");
}
__device__ __forceinline__ void ln_part(LDSP char* L, int w, int lane, float a, float b) {
  const float c = __builtin_bit_cast(float, __builtin_amdgcn_readfirstlane(__builtin_bit_cast(int, a)));
  const float da = a - c, db = b - c;
  float s1 = da + db, s2 = da * da + db * db;
  s1 += dpp<0xB1>(s1); s2 += dpp<0xB1>(s2); s1 += dpp<0x4E>(s1); s2 += dpp<0x4E>(s2);
  s1 += dpp<0x124>(s1); s2 += dpp<0x124>(s2); s1 += dpp<0x128>(s1); s2 += dpp<0x128>(s2);
  s1 = xrow_sum(s1); s2 = xrow_sum(s2);
  const float dm = s1 * (1.0f / 128.0f);
  if (lane == 0) lds_st<v2u>(L, L_ST + w * 8, v2u{__float_as_uint(c + dm), __float_as_uint(fmaxf(s2 - s1 * dm, 0.0f))});
}
__device__ __forceinline__ void ln_apply(LDSP char* L, int t, float a, float b) {
  const f4 s0 = lds_ld<f4>(L, L_ST), s1 = lds_ld<f4>(L, L_ST + 16);
  const float mean = ((s0.x + s0.z) + (s1.x + s1.z)) * 0.25f;
  const float e0 = s0.x - mean, e1 = s0.z - mean, e2 = s1.x - mean, e3 = s1.z - mean;
  const float var = (((s0.y + s0.w) + (s1.y + s1.w)) + 128.0f * ((e0 * e0 + e1 * e1) + (e2 * e2 + e3 * e3))) * (1.0f / 512.0f);
  const float rstd = __builtin_amdgcn_rsqf(var + 1e-5f);
  lds_st<unsigned>(L, L_ZB + t * 4, cvtpk((a - mean) * rstd, (b - mean) * rstd));
}
__device__ __forceinline__ v4u bfrag(LDSP char* L, int base, int kb, int lane) { return lds_ld<v4u>(L, base + (32 * kb + 8 * (lane >> 4)) * 2); }

__device__ __forceinline__ void bfrags16(LDSP char* L, int lane, v4u (&B)[16]) {
#pragma unroll
  for (int kb = 0; kb < 16; ++kb) B[kb] = bfrag(L, L_ZB, kb, lane);
  __builtin_amdgcn_sched_barrier(0);
}
#define POLL_BEGIN() for (int spin_ = 0;; ++spin_) { asm volatile("" ::: "memory");
#define POLL_END(ok_) if (__all(ok_)) break; if (spin_ > SPIN_LIMIT) { dead = true; break; } if (dead) break; }

__device__ __forceinline__ void chain_role(const u16* __restrict__ x, const u16* __restrict__ wq, const u16* __restrict__ wo,
                                           const u16* __restrict__ wg, const u16* __restrict__ wu, const u16* __restrict__ wd, u16* __restrict__ out, char* ws, LDSP char* L, const int w) {
  const int j = blockIdx.x, lane = __builtin_amdgcn_mbcnt_hi(~0u, __builtin_amdgcn_mbcnt_lo(~0u, 0u)), t = w * 64 + lane;
  const int n = lane & 15, g = lane >> 4;
  const __amdgpu_buffer_rsrc_t rs = __builtin_amdgcn_make_buffer_rsrc((void*)ws, 0, WS_BYTES, 0x00020000);
  bool dead = false;
  unsigned E;
  float xc[2][2];
#pragma unroll
  for (int b = 0; b < 2; ++b) { const unsigned v = *(const unsigned*)(x + b * 512 + 2 * t); xc[b][0] = bf2f(v & 0xFFFFu); xc[b][1] = bf2f(v >> 16); }
  {
    v4u P[32];
#pragma unroll
    for (int i = 0; i < 32; ++i) P[i] = ldnt(wq + (size_t)(32 * (i >> 1) + 8 * w + 4 * (i & 1) + g) * 4096 + 128 * j + 8 * n);
    __builtin_amdgcn_sched_barrier(0);
    const unsigned Ev = __hip_atomic_load((unsigned*)ws, __ATOMIC_RELAXED, __HIP_MEMORY_SCOPE_AGENT);
    {
      float dm[2], cc[2], s1[2], s2[2];
#pragma unroll
      for (int b = 0; b < 2; ++b) {
        cc[b] = __builtin_bit_cast(float, __builtin_amdgcn_readfirstlane(__builtin_bit_cast(int, xc[b][0])));
        const float da = xc[b][0] - cc[b], db = xc[b][1] - cc[b];
        s1[b] = wave_sum(da + db); s2[b] = wave_sum(da * da + db * db); dm[b] = s1[b] * (1.0f / 128.0f);
        if (lane == 0) lds_st<v2u>(L, L_ST + (b * 4 + w) * 8, v2u{__float_as_uint(cc[b] + dm[b]), __float_as_uint(fmaxf(s2[b] - s1[b] * dm[b], 0.0f))});
      }
      __syncthreads();
#pragma unroll
      for (int b = 0; b < 2; ++b) {
        const f4 u0 = lds_ld<f4>(L, L_ST + b * 32), u1 = lds_ld<f4>(L, L_ST + b * 32 + 16);
        const float mean = ((u0.x + u0.z) + (u1.x + u1.z)) * 0.25f;
        const float e0 = u0.x - mean, e1 = u0.z - mean, e2 = u1.x - mean, e3 = u1.z - mean;
        const float var = (((u0.y + u0.w) + (u1.y + u1.w)) + 128.0f * ((e0 * e0 + e1 * e1) + (e2 * e2 + e3 * e3))) * (1.0f / 512.0f);
        const float rstd = __builtin_amdgcn_rsqf(var + 1e-5f);
#pragma unroll
        for (int u = 0; u < 2; ++u) {
          const int k = 2 * t + u; const float z = (xc[b][u] - mean) * rstd;
          lds_st<unsigned>(L, L_SCR + (((b * 4 + ((k >> 3) & 3)) * 4 + (k & 3)) * 32 + (k >> 5) * 2 + ((k >> 2) & 1)) * 4, cvtpk(z, z));
        }
      }
      __syncthreads();
      unsigned X[32];
#pragma unroll
      for (int q = 0; q < 8; ++q) { const v4u v = lds_ld<v4u>(L, L_SCR + ((((n >> 3) * 4 + w) * 4 + g) * 32 + 4 * q) * 4); X[4 * q] = v.x; X[4 * q + 1] = v.y; X[4 * q + 2] = v.z; X[4 * q + 3] = v.w; }
      const unsigned half = (n & 1) ? 0xFFFF0000u : 0x0000FFFFu; const int ww = (n & 7) >> 1;
      const unsigned m0 = ww == 0 ? half : 0u, m1 = ww == 1 ? half : 0u, m2 = ww == 2 ? half : 0u, m3 = ww == 3 ? half : 0u;
      f4 a0 = {0.f, 0.f, 0.f, 0.f}, a1 = {0.f, 0.f, 0.f, 0.f};
#pragma unroll
      for (int i = 0; i < 32; i += 2) {
        const v4u b0 = {X[i] & m0, X[i] & m1, X[i] & m2, X[i] & m3}, b1 = {X[i + 1] & m0, X[i + 1] & m1, X[i + 1] & m2, X[i + 1] & m3};
        mfma_w2<false>(a0, P[i], b0, a1, P[i + 1], b1);
      }
      mfma_end(a0, a1);
      const f4 acc = a0 + a1;
#pragma unroll
      for (int e = 0; e < 4; ++e) lds_st<float>(L, L_SCR + 4096 + ((w * 2 + (n >> 3)) * 128 + 8 * (4 * g + e) + (n & 7)) * 4, acc[e]);
      __syncthreads();
      E = __builtin_amdgcn_readfirstlane(Ev);
      if (t < 128) {
        const int b = t >> 6, p = t & 63;
        float q0 = 0.f, q1 = 0.f;
#pragma unroll
        for (int wv = 0; wv < 4; ++wv) { const v2u pr = lds_ld<v2u>(L, L_SCR + 4096 + ((wv * 2 + b) * 128 + 2 * p) * 4); q0 += __uint_as_float(pr.x); q1 += __uint_as_float(pr.y); }
        const v2u gr = {cvtpk(q0, q1), E + 1u + (unsigned)b};
        __builtin_amdgcn_raw_buffer_store_b64(gr, rs, WS_Q + (unsigned)(b * 32 + j) * 512u + (unsigned)p * 8u, 0, 16);
      }
      __syncthreads();
    }
#pragma unroll
    for (int kb = 0; kb < 16; ++kb) {
      v4u f0, f1;
      tr_coop(L, kb, w, lane, P[2 * kb], P[2 * kb + 1], 64 * w, 64 * w + 32, f0, f1);
      lds_st<v4u>(L, L_WQ + w * 32768 + kb * 2048 + lane * 16, f0); lds_st<v4u>(L, L_WQ + w * 32768 + kb * 2048 + 1024 + lane * 16, f1);
    }
    __syncthreads();
  }
  v4u WO[16][2], GG[16], GU[16], WD[2][8];
  const int ks = j >> 2, ns = j & 3, hh = 4 * ks + w;
  const int nj = j < 12 ? 6 : 5, c0 = j < 12 ? 6 * j : 72 + 5 * (j - 12);
  {
    v4u P[32];
#pragma unroll
    for (int i = 0; i < 32; ++i) P[i] = ldnt(wo + (size_t)(512 * ks + 32 * (i >> 1) + 8 * w + 4 * (i & 1) + g) * 512 + 128 * ns + 8 * n);
#pragma unroll
    for (int kb = 0; kb < 16; ++kb) tr_coop(L, kb, w, lane, P[2 * kb], P[2 * kb + 1], 64 * w, 64 * w + 32, WO[kb][0], WO[kb][1]);
    __syncthreads();
  }
  {
    const int rr = n < 6 ? n : (n < 12 ? n - 6 : 0);
    const u16* base = (n < 6 ? wg : wu) + 8 * (c0 + (rr < nj ? rr : 0));
    const int ww = w < 3 ? w : 0;
    v4u P[32];
#pragma unroll
    for (int i = 0; i < 32; ++i) P[i] = ldnt(base + (size_t)(32 * (i >> 1) + 8 * w + 4 * (i & 1) + g) * 1376);
#pragma unroll
    for (int kb = 0; kb < 16; ++kb) tr_coop(L, kb, w, lane, P[2 * kb], P[2 * kb + 1], 32 * ww, 96 + 32 * ww, GG[kb], GU[kb]);
    __syncthreads();
  }
  {
    v4u P[16];
#pragma unroll
    for (int i = 0; i < 16; ++i) { int hid = 8 * c0 + 32 * (i >> 3) + 8 * (i & 3) + (lane >> 3); hid = hid < 1376 ? hid : 1375;
      P[i] = ldnt(wd + (size_t)hid * 512 + 128 * w + 64 * ((i >> 2) & 1) + 8 * (lane & 7)); }
    const int scr = L_SCR + w * 4096;
#pragma unroll
    for (int tl = 0; tl < 4; ++tl) {
      WAVE_FENCE();
#pragma unroll
      for (int i = 0; i < 4; ++i) lds_st<v4u>(L, scr + i * 1024 + lane * 16, P[tl * 4 + i]);
      WAVE_FENCE();
#pragma unroll
      for (int u = 0; u < 4; ++u) { WD[tl >> 1][(tl & 1) * 4 + u] = tr_frag(L, scr + 32 * u, 128, lane); asm volatile("" : "+v"(WD[tl >> 1][(tl & 1) * 4 + u]) :: "memory"); }
    }
    __syncthreads();
  }

#pragma unroll 1
  for (int c = 0; c < 4; ++c) {
    const int b = c & 1; const unsigned tag = E + 1u + (unsigned)c;
    const float xr0 = b ? xc[1][0] : xc[0][0], xr1 = b ? xc[1][1] : xc[0][1];
    {
      const unsigned rec = (unsigned)((c * 32 + hh) * NSPLIT);
      const unsigned mlo = WS_RECML + (rec + (lane < NSPLIT ? lane : 0)) * 64u;
      v4u ml;
      const int need = c == 2 ? NSPLIT : NSPLIT - 2;
      for (int spin_ = 0;; ++spin_) {
        asm volatile("" ::: "memory");
        ml = ld_sc1(rs, mlo);
        const int have = __builtin_popcountll(__builtin_amdgcn_ballot_w64(lane < NSPLIT && ml.y == tag && ml.w == tag));
        if (have >= need) break;
        if (spin_ > SPIN_LIMIT || dead) { dead = true; break; }
        if (c == 2 || have == 0) __builtin_amdgcn_s_sleep(4);
      }
      v4u o[NSPLIT];
      POLL_BEGIN()
        ml = ld_sc1(rs, mlo);
        bool ok = ml.y == tag && ml.w == tag;
#pragma unroll
        for (int s = 0; s < NSPLIT; ++s) o[s] = ld_sc1(rs, WS_RECO + (rec + s) * 1024u + lane * 16u);
#pragma unroll
        for (int s = 0; s < NSPLIT; ++s) ok = ok && o[s].y == tag && o[s].w == tag;
      POLL_END(ok)

      float Ms[NSPLIT], Ls[NSPLIT];
#pragma unroll
      for (int s = 0; s < NSPLIT; ++s) { Ms[s] = __uint_as_float(__builtin_amdgcn_readlane(ml.x, s)); Ls[s] = __uint_as_float(__builtin_amdgcn_readlane(ml.z, s)); }
      float Mx = Ms[0];
#pragma unroll
      for (int s = 1; s < NSPLIT; ++s) Mx = fmaxf(Mx, Ms[s]);
      float Lt = 0.f, o0 = 0.f, o1 = 0.f;
#pragma unroll
      for (int s = 0; s < NSPLIT; ++s) { const float a = __builtin_amdgcn_exp2f(Ms[s] - Mx); Lt = fmaf(a, Ls[s], Lt); o0 = fmaf(a, __uint_as_float(o[s].x), o0); o1 = fmaf(a, __uint_as_float(o[s].z), o1); }
      const float inv = __builtin_amdgcn_rcpf(Lt);
      lds_st<unsigned>(L, L_ZB + (64 * w + lane) * 4, cvtpk(o0 * inv, o1 * inv));

      __syncthreads();

      f4 a0 = {0.f, 0.f, 0.f, 0.f}, a1 = {0.f, 0.f, 0.f, 0.f};
            v4u B[16]; bfrags16(L, lane, B);
#pragma unroll
      for (int kb = 0; kb < 16; ++kb) mfma_w2<true>(a0, WO[kb][0], B[kb], a1, WO[kb][1], B[kb]);
      mfma_end(a0, a1);
      if (n < 4) {
        const float v0 = n == 0 ? a0[0] : n == 1 ? a0[2] : n == 2 ? a1[0] : a1[2], v1 = n == 0 ? a0[1] : n == 1 ? a0[3] : n == 2 ? a1[1] : a1[3];
        st_pair(rs, WS_Y + (unsigned)(c * 8 + ks) * 4096u + (unsigned)(64 * ns + 16 * w + 8 * (n >> 1) + 2 * g + (n & 1)) * 16u, v0, v1, tag);
      }
    }

    {
      v4u y[8];
      POLL_BEGIN()
        bool ok = true;
#pragma unroll
        for (int s = 0; s < 8; ++s) y[s] = ld_sc1(rs, WS_Y + (unsigned)(c * 8 + s) * 4096u + t * 16u);
#pragma unroll
        for (int s = 0; s < 8; ++s) ok = ok && y[s].y == tag && y[s].w == tag;
      POLL_END(ok)

      float a0s = 0.f, a1s = 0.f;
#pragma unroll
      for (int s = 0; s < 8; ++s) { a0s += __uint_as_float(y[s].x); a1s += __uint_as_float(y[s].z); }
      const float x1a = rbf(xr0 + rbf(a0s)), x1b = rbf(xr1 + rbf(a1s));
      ln_part(L, w, lane, x1a, x1b);
      lds_st<v2u>(L, L_XF + t * 8, v2u{__float_as_uint(x1a), __float_as_uint(x1b)});

      __syncthreads();

      ln_apply(L, t, x1a, x1b);

      __syncthreads();

      if (w < 3) {
        f4 ag = {0.f, 0.f, 0.f, 0.f}, au = {0.f, 0.f, 0.f, 0.f};
                v4u B[16]; bfrags16(L, lane, B);
#pragma unroll
        for (int kb = 0; kb < 16; ++kb) mfma_w2<true>(ag, GG[kb], B[kb], au, GU[kb], B[kb]);
        mfma_end(ag, au);
        float h[4];
#pragma unroll
        for (int i = 0; i < 4; ++i) { const float hv = rbf(ag[i]) * rbf(au[i]); h[i] = (16 * w + 4 * g + i < 8 * nj) ? hv : 0.f; }
        if (n == 0) lds_st<v2u>(L, L_HB + (16 * w + 4 * g) * 2, v2u{cvtpk(h[0], h[1]), cvtpk(h[2], h[3])});
      } else if (lane < 8) lds_st<unsigned>(L, L_HB + 96 + lane * 4, 0u);

      __syncthreads();

      f4 d[8];
#pragma unroll
      for (int i = 0; i < 8; ++i) d[i] = f4{0.f, 0.f, 0.f, 0.f};
      const v4u h0 = bfrag(L, L_HB, 0, lane), h1 = bfrag(L, L_HB, 1, lane);
#pragma unroll
      for (int i = 0; i < 8; i += 2) mfma_w2<false>(d[i], WD[0][i], h0, d[i + 1], WD[0][i + 1], h0);
#pragma unroll
      for (int i = 0; i < 8; i += 2) mfma_w2<false>(d[i], WD[1][i], h1, d[i + 1], WD[1][i + 1], h1);
      asm volatile("s_nop 7\n\ts_nop 1" : "+v"(d[0]), "+v"(d[1]), "+v"(d[2]), "+v"(d[3]), "+v"(d[4]), "+v"(d[5]), "+v"(d[6]), "+v"(d[7]));

      {
        const unsigned m1 = 0u - (unsigned)(n & 1), m2 = 0u - (unsigned)((n >> 1) & 1), m4 = 0u - (unsigned)((n >> 2) & 1), m8 = 0u - (unsigned)((n >> 3) & 1);
#define SEL(m, a, b) __uint_as_float((__float_as_uint(b) & (m)) | (__float_as_uint(a) & ~(m)))
#define PICK(e0, e1) SEL(m8, SEL(m4, SEL(m2, SEL(m1, d[0][e0], d[0][e1]), SEL(m1, d[1][e0], d[1][e1])), SEL(m2, SEL(m1, d[2][e0], d[2][e1]), SEL(m1, d[3][e0], d[3][e1]))), \
                             SEL(m4, SEL(m2, SEL(m1, d[4][e0], d[4][e1]), SEL(m1, d[5][e0], d[5][e1])), SEL(m2, SEL(m1, d[6][e0], d[6][e1]), SEL(m1, d[7][e0], d[7][e1]))))
        const float p0 = PICK(0, 2), p1 = PICK(1, 3);
#undef PICK
#undef SEL
        st_pair(rs, WS_XP + (unsigned)(c * 32 + j) * 4096u + (unsigned)(64 * w + 8 * (n >> 1) + 2 * g + (n & 1)) * 16u, p0, p1, tag);
      }
    }

    {
      v4u p;
      POLL_BEGIN()
        p = ld_sc1(rs, WS_XP + (unsigned)(c * 32 + (t >> 3)) * 4096u + (unsigned)(8 * j + (t & 7)) * 16u);
        const bool ok = p.y == tag && p.w == tag;
      POLL_END(ok)

      float s0 = __uint_as_float(p.x), s1 = __uint_as_float(p.z);
      s0 += dpp<0x128>(s0); s1 += dpp<0x128>(s1);
      s0 = xrow_sum(s0); s1 = xrow_sum(s1);
      if (lane < 8) lds_st<v2u>(L, L_R3 + (w * 8 + lane) * 8, v2u{__float_as_uint(s0), __float_as_uint(s1)});
      __syncthreads();
      if (t < 8) {
        float f0 = 0.f, f1 = 0.f;
#pragma unroll
        for (int ww = 0; ww < 4; ++ww) { const v2u q = lds_ld<v2u>(L, L_R3 + (ww * 8 + t) * 8); f0 += __uint_as_float(q.x); f1 += __uint_as_float(q.y); }
        const v2u xo = lds_ld<v2u>(L, L_XF + (8 * j + t) * 8);
        const float u0 = rbf(__uint_as_float(xo.x) + rbf(f0)), u1 = rbf(__uint_as_float(xo.y) + rbf(f1));
        if (c < 2) st_pair(rs, WS_X2 + (unsigned)c * 4096u + (unsigned)(8 * j + t) * 16u, u0, u1, tag);
        else *(unsigned*)(out + b * 512 + 16 * j + 2 * t) = cvtpk(u0, u1);
      }
    }
    if (c < 2) {
      v4u p;
      POLL_BEGIN()
        p = ld_sc1(rs, WS_X2 + (unsigned)c * 4096u + t * 16u);
        const bool ok = p.y == tag && p.w == tag;
      POLL_END(ok)

      const float n0 = __uint_as_float(p.x), n1 = __uint_as_float(p.z);
      if (b) { xc[1][0] = n0; xc[1][1] = n1; } else { xc[0][0] = n0; xc[0][1] = n1; }
      ln_part(L, w, lane, n0, n1);
      __syncthreads();
      ln_apply(L, t, n0, n1);
      __syncthreads();
      f4 a0 = {0.f, 0.f, 0.f, 0.f}, a1 = {0.f, 0.f, 0.f, 0.f};
            v4u B[16]; bfrags16(L, lane, B);
#pragma unroll
      for (int hf = 0; hf < 2; ++hf) {
        v4u Wf[16];
#pragma unroll
        for (int i = 0; i < 16; ++i) Wf[i] = lds_ld<v4u>(L, L_WQ + w * 32768 + (hf * 16 + i) * 1024 + lane * 16);
        __builtin_amdgcn_sched_barrier(0);
#pragma unroll
        for (int i = 0; i < 8; ++i) mfma_w2<false>(a0, Wf[2 * i], B[hf * 8 + i], a1, Wf[2 * i + 1], B[hf * 8 + i]);
        __builtin_amdgcn_sched_barrier(0);
      }
      mfma_end(a0, a1);
      if (n < 4) {
        const float v0 = n == 0 ? a0[0] : n == 1 ? a0[2] : n == 2 ? a1[0] : a1[2], v1 = n == 0 ? a0[1] : n == 1 ? a0[3] : n == 2 ? a1[1] : a1[3];
        const v2u gr = {cvtpk(v0, v1), E + 3u + (unsigned)c};
        __builtin_amdgcn_raw_buffer_store_b64(gr, rs, WS_Q + (unsigned)((c + 2) * 32 + j) * 512u + (unsigned)(16 * w + 8 * (n >> 1) + 2 * g + (n & 1)) * 8u, 0, 16);
      }
    }

    __syncthreads();
  }
  if (j == 0 && t == 0) __hip_atomic_store((unsigned*)ws, E + 8u, __ATOMIC_RELAXED, __HIP_MEMORY_SCOPE_AGENT);
}

__global__ void __launch_bounds__(256, 1) mega_kernel(const u16* __restrict__ x, const u16* __restrict__ Kc, const u16* __restrict__ Vc, const u16* __restrict__ wq,
                                                       const u16* __restrict__ wo, const u16* __restrict__ wg, const u16* __restrict__ wu, const u16* __restrict__ wd,
                                                       u16* __restrict__ out, char* ws) {
  __shared__ __attribute__((aligned(16))) char smem[163840];
  const int w = __builtin_amdgcn_readfirstlane(threadIdx.x >> 6);
  if (blockIdx.x < NCHAIN) {
    chain_role(x, wq, wo, wg, wu, wd, out, ws, (LDSP char*)smem, w);
  } else {
    const int c = blockIdx.x - NCHAIN, h = c / NSPLIT, s = c % NSPLIT;
    const uint64_t kb = (uint64_t)Kc + (uint64_t)h * 0x400000ull, vb = (uint64_t)Vc + (uint64_t)h * 0x400000ull, wb = (uint64_t)ws;
    const unsigned ldsb = (unsigned)(uintptr_t)(LDSP char*)smem;
    asm volatile(R"ASM(
; ==== set-up (what the first loads need)
s_mov_b32 s8, %0
s_and_b32 s9, %1, 0xffff
s_mov_b32 s10, 0x400000
s_mov_b32 s11, 0x20000
s_mov_b32 s12, %2
s_and_b32 s13, %3, 0xffff
s_mov_b32 s14, 0x400000
s_mov_b32 s15, 0x20000
v_mbcnt_lo_u32_b32 v10, -1, 0
v_mbcnt_hi_u32_b32 v10, -1, v10
s_mov_b32 s46, %8
s_mov_b32 s55, %9
s_lshl_b32 s44, %7, 2
s_add_u32 s44, s44, s46
s_lshl_b32 s28, s44, 12
s_mul_i32 s45, s46, 40960
s_add_u32 s45, s45, s55
v_lshlrev_b32 v1, 4, v10
v_and_b32 v12, 15, v10
v_lshrrev_b32 v13, 4, v10
v_lshrrev_b32 v7, 2, v12
v_lshlrev_b32 v0, 8, v7
v_lshl_add_u32 v0, v13, 6, v0
v_and_b32 v7, 3, v12
v_lshl_add_u32 v0, v7, 4, v0
s_mov_b32 s49, %6
; ==== A: first attention layer of batch 0. Fill: 18 tiles (16 keys: K 4 KiB + V 4 KiB) go to the slots they stay in (LDS: LDS-DMA; registers: a0-a255, v96-v255)
s_add_u32 s29, s28, 0
s_add_u32 m0, s45, 0
s_nop 0
buffer_load_dwordx4 v0, s[8:11], s29 offen offset:0 nt lds
buffer_load_dwordx4 v0, s[8:11], s29 offen offset:1024 nt lds
buffer_load_dwordx4 v0, s[8:11], s29 offen offset:2048 nt lds
buffer_load_dwordx4 v0, s[8:11], s29 offen offset:3072 nt lds
s_add_u32 s29, s28, 0
s_add_u32 m0, s45, 4096
s_nop 0
buffer_load_dwordx4 v1, s[12:15], s29 offen offset:0 nt lds
buffer_load_dwordx4 v1, s[12:15], s29 offen offset:1024 nt lds
buffer_load_dwordx4 v1, s[12:15], s29 offen offset:2048 nt lds
buffer_load_dwordx4 v1, s[12:15], s29 offen offset:3072 nt lds
s_add_u32 s29, s28, 114688
s_add_u32 m0, s45, 8192
s_nop 0
buffer_load_dwordx4 v0, s[8:11], s29 offen offset:0 nt lds
buffer_load_dwordx4 v0, s[8:11], s29 offen offset:1024 nt lds
buffer_load_dwordx4 v0, s[8:11], s29 offen offset:2048 nt lds
buffer_load_dwordx4 v0, s[8:11], s29 offen offset:3072 nt lds
s_add_u32 s29, s28, 114688
s_add_u32 m0, s45, 12288
s_nop 0
buffer_load_dwordx4 v1, s[12:15], s29 offen offset:0 nt lds
buffer_load_dwordx4 v1, s[12:15], s29 offen offset:1024 nt lds
buffer_load_dwordx4 v1, s[12:15], s29 offen offset:2048 nt lds
buffer_load_dwordx4 v1, s[12:15], s29 offen offset:3072 nt lds
s_add_u32 s16, %0, 0x8000000
s_addc_u32 s17, %1, 0
s_and_b32 s17, s17, 0xffff
s_mov_b32 s18, 0x400000
s_mov_b32 s19, 0x20000
s_add_u32 s20, %2, 0x8000000
s_addc_u32 s21, %3, 0
s_and_b32 s21, s21, 0xffff
s_mov_b32 s22, 0x400000
s_mov_b32 s23, 0x20000
s_mov_b32 s24, %4
s_and_b32 s25, %5, 0xffff
s_mov_b32 s26, 0x400000
s_mov_b32 s27, 0x20000
s_mul_i32 s47, s49, 7
s_add_u32 s47, s47, %7
s_cmp_lt_u32 s44, 16
s_cselect_b32 s54, 0x400000, 0
s_mov_b32 s36, 0x000f000f
s_mov_b32 s37, 0x000f000f
s_mov_b32 s38, 0x00f000f0
s_mov_b32 s39, 0x00f000f0
s_mov_b32 s40, 0x0f000f00
s_mov_b32 s41, 0x0f000f00
s_mov_b32 s42, 0xf000f000
s_mov_b32 s43, 0xf000f000
s_getpc_b64 s[64:65]
LPC_%=:
s_add_u32 s64, s64, LSLOW_%=-LPC_%=
s_addc_u32 s65, s65, 0
v_add_u32 v11, s45, v1
v_and_b32 v7, 1, v12
v_cmp_eq_u32 vcc, 1, v7
v_mov_b32 v18, 0xffff
v_mov_b32 v10, 0xffff0000
v_cndmask_b32 v18, v18, v10, vcc
v_lshrrev_b32 v7, 1, v12
v_cmp_eq_u32 vcc, 0, v7
s_nop 1
v_cndmask_b32 v3, 0, v18, vcc
v_cmp_eq_u32 vcc, 1, v7
s_nop 1
v_cndmask_b32 v4, 0, v18, vcc
v_cmp_eq_u32 vcc, 2, v7
s_nop 1
v_cndmask_b32 v5, 0, v18, vcc
v_cmp_eq_u32 vcc, 3, v7
s_nop 1
v_cndmask_b32 v6, 0, v18, vcc
v_mov_b32 v2, 0x3e0293ee
s_mov_b32 s58, 0x400000
s_mov_b32 s59, 0x20000
s_mov_b32 s62, 0x400000
s_mov_b32 s63, 0x20000
s_mov_b32 s93, 0
s_waitcnt vmcnt(12)
s_add_u32 s29, s28, 229376
s_add_u32 m0, s45, 16384
s_nop 0
buffer_load_dwordx4 v0, s[8:11], s29 offen offset:0 nt lds
buffer_load_dwordx4 v0, s[8:11], s29 offen offset:1024 nt lds
buffer_load_dwordx4 v0, s[8:11], s29 offen offset:2048 nt lds
buffer_load_dwordx4 v0, s[8:11], s29 offen offset:3072 nt lds
s_waitcnt vmcnt(12)
s_add_u32 s29, s28, 229376
s_add_u32 m0, s45, 20480
s_nop 0
buffer_load_dwordx4 v1, s[12:15], s29 offen offset:0 nt lds
buffer_load_dwordx4 v1, s[12:15], s29 offen offset:1024 nt lds
buffer_load_dwordx4 v1, s[12:15], s29 offen offset:2048 nt lds
buffer_load_dwordx4 v1, s[12:15], s29 offen offset:3072 nt lds
s_waitcnt vmcnt(12)
s_add_u32 s29, s28, 344064
s_add_u32 m0, s45, 24576
s_nop 0
buffer_load_dwordx4 v0, s[8:11], s29 offen offset:0 nt lds
buffer_load_dwordx4 v0, s[8:11], s29 offen offset:1024 nt lds
buffer_load_dwordx4 v0, s[8:11], s29 offen offset:2048 nt lds
buffer_load_dwordx4 v0, s[8:11], s29 offen offset:3072 nt lds
s_waitcnt vmcnt(12)
s_add_u32 s29, s28, 344064
s_add_u32 m0, s45, 28672
s_nop 0
buffer_load_dwordx4 v1, s[12:15], s29 offen offset:0 nt lds
buffer_load_dwordx4 v1, s[12:15], s29 offen offset:1024 nt lds
buffer_load_dwordx4 v1, s[12:15], s29 offen offset:2048 nt lds
buffer_load_dwordx4 v1, s[12:15], s29 offen offset:3072 nt lds
s_waitcnt vmcnt(12)
s_add_u32 s29, s28, 458752
s_add_u32 m0, s45, 32768
s_nop 0
buffer_load_dwordx4 v0, s[8:11], s29 offen offset:0 nt lds
buffer_load_dwordx4 v0, s[8:11], s29 offen offset:1024 nt lds
buffer_load_dwordx4 v0, s[8:11], s29 offen offset:2048 nt lds
buffer_load_dwordx4 v0, s[8:11], s29 offen offset:3072 nt lds
s_waitcnt vmcnt(12)
s_add_u32 s29, s28, 458752
s_add_u32 m0, s45, 36864
s_nop 0
buffer_load_dwordx4 v1, s[12:15], s29 offen offset:0 nt lds
buffer_load_dwordx4 v1, s[12:15], s29 offen offset:1024 nt lds
buffer_load_dwordx4 v1, s[12:15], s29 offen offset:2048 nt lds
buffer_load_dwordx4 v1, s[12:15], s29 offen offset:3072 nt lds
s_waitcnt vmcnt(12)
s_add_u32 s29, s28, 573440
buffer_load_dwordx4 a[0:3], v0, s[8:11], s29 offen offset:0 nt
buffer_load_dwordx4 a[4:7], v0, s[8:11], s29 offen offset:1024 nt
buffer_load_dwordx4 a[8:11], v0, s[8:11], s29 offen offset:2048 nt
buffer_load_dwordx4 a[12:15], v0, s[8:11], s29 offen offset:3072 nt
s_waitcnt vmcnt(12)
s_add_u32 s29, s28, 573440
buffer_load_dwordx4 a[16:19], v1, s[12:15], s29 offen offset:0 nt
buffer_load_dwordx4 a[20:23], v1, s[12:15], s29 offen offset:1024 nt
buffer_load_dwordx4 a[24:27], v1, s[12:15], s29 offen offset:2048 nt
buffer_load_dwordx4 a[28:31], v1, s[12:15], s29 offen offset:3072 nt
s_waitcnt vmcnt(13)
v_mov_b32 v13, 0
buffer_load_dword v10, v13, s[24:27], 0 offen sc1
v_and_b32 v12, 0xff, v0
v_lshlrev_b32 v12, 1, v12
s_lshl_b32 s50, s49, 9
s_add_u32 s50, s50, 4096
v_add_u32 v12, s50, v12
buffer_load_dwordx4 v[40:43], v12, s[24:27], 0 offen sc1
buffer_load_dwordx4 v[44:47], v12, s[24:27], 0 offen offset:16 sc1
s_waitcnt vmcnt(12)
s_add_u32 s29, s28, 688128
buffer_load_dwordx4 a[32:35], v0, s[8:11], s29 offen offset:0 nt
buffer_load_dwordx4 a[36:39], v0, s[8:11], s29 offen offset:1024 nt
buffer_load_dwordx4 a[40:43], v0, s[8:11], s29 offen offset:2048 nt
buffer_load_dwordx4 a[44:47], v0, s[8:11], s29 offen offset:3072 nt
s_waitcnt vmcnt(12)
s_add_u32 s29, s28, 688128
buffer_load_dwordx4 a[48:51], v1, s[12:15], s29 offen offset:0 nt
buffer_load_dwordx4 a[52:55], v1, s[12:15], s29 offen offset:1024 nt
buffer_load_dwordx4 a[56:59], v1, s[12:15], s29 offen offset:2048 nt
buffer_load_dwordx4 a[60:63], v1, s[12:15], s29 offen offset:3072 nt
s_waitcnt vmcnt(12)
s_add_u32 s29, s28, 802816
buffer_load_dwordx4 a[64:67], v0, s[8:11], s29 offen offset:0 nt
buffer_load_dwordx4 a[68:71], v0, s[8:11], s29 offen offset:1024 nt
buffer_load_dwordx4 a[72:75], v0, s[8:11], s29 offen offset:2048 nt
buffer_load_dwordx4 a[76:79], v0, s[8:11], s29 offen offset:3072 nt
s_waitcnt vmcnt(12)
s_add_u32 s29, s28, 802816
buffer_load_dwordx4 a[80:83], v1, s[12:15], s29 offen offset:0 nt
buffer_load_dwordx4 a[84:87], v1, s[12:15], s29 offen offset:1024 nt
buffer_load_dwordx4 a[88:91], v1, s[12:15], s29 offen offset:2048 nt
buffer_load_dwordx4 a[92:95], v1, s[12:15], s29 offen offset:3072 nt
s_waitcnt vmcnt(12)
s_add_u32 s29, s28, 917504
buffer_load_dwordx4 a[96:99], v0, s[8:11], s29 offen offset:0 nt
buffer_load_dwordx4 a[100:103], v0, s[8:11], s29 offen offset:1024 nt
buffer_load_dwordx4 a[104:107], v0, s[8:11], s29 offen offset:2048 nt
buffer_load_dwordx4 a[108:111], v0, s[8:11], s29 offen offset:3072 nt
s_waitcnt vmcnt(12)
s_add_u32 s29, s28, 917504
buffer_load_dwordx4 a[112:115], v1, s[12:15], s29 offen offset:0 nt
buffer_load_dwordx4 a[116:119], v1, s[12:15], s29 offen offset:1024 nt
buffer_load_dwordx4 a[120:123], v1, s[12:15], s29 offen offset:2048 nt
buffer_load_dwordx4 a[124:127], v1, s[12:15], s29 offen offset:3072 nt
s_waitcnt vmcnt(12)
s_add_u32 s29, s28, 1032192
buffer_load_dwordx4 a[128:131], v0, s[8:11], s29 offen offset:0 nt
buffer_load_dwordx4 a[132:135], v0, s[8:11], s29 offen offset:1024 nt
buffer_load_dwordx4 a[136:139], v0, s[8:11], s29 offen offset:2048 nt
buffer_load_dwordx4 a[140:143], v0, s[8:11], s29 offen offset:3072 nt
s_waitcnt vmcnt(12)
s_add_u32 s29, s28, 1032192
buffer_load_dwordx4 a[144:147], v1, s[12:15], s29 offen offset:0 nt
buffer_load_dwordx4 a[148:151], v1, s[12:15], s29 offen offset:1024 nt
buffer_load_dwordx4 a[152:155], v1, s[12:15], s29 offen offset:2048 nt
buffer_load_dwordx4 a[156:159], v1, s[12:15], s29 offen offset:3072 nt
v_readfirstlane_b32 s30, v10
s_nop 1
s_add_u32 s31, s30, 1
v_cmp_eq_u32_e64 s[50:51], s31, v41
v_cmp_eq_u32_e64 s[52:53], s31, v43
s_and_b64 s[50:51], s[50:51], s[52:53]
v_cmp_eq_u32_e64 s[52:53], s31, v45
s_and_b64 s[50:51], s[50:51], s[52:53]
v_cmp_eq_u32_e64 s[52:53], s31, v47
s_and_b64 s[50:51], s[50:51], s[52:53]
s_cmp_eq_u64 s[50:51], -1
s_cbranch_scc1 Lqok_1_%=
s_mov_b32 s48, 0
Lqrt_2_%=:
s_sleep 8
v_and_b32 v12, 0xff, v0
v_lshlrev_b32 v12, 1, v12
s_lshl_b32 s50, s49, 9
s_add_u32 s50, s50, 4096
v_add_u32 v12, s50, v12
buffer_load_dwordx4 v[40:43], v12, s[24:27], 0 offen sc1
buffer_load_dwordx4 v[44:47], v12, s[24:27], 0 offen offset:16 sc1
s_waitcnt vmcnt(0)
v_cmp_eq_u32_e64 s[50:51], s31, v41
v_cmp_eq_u32_e64 s[52:53], s31, v43
s_and_b64 s[50:51], s[50:51], s[52:53]
v_cmp_eq_u32_e64 s[52:53], s31, v45
s_and_b64 s[50:51], s[50:51], s[52:53]
v_cmp_eq_u32_e64 s[52:53], s31, v47
s_and_b64 s[50:51], s[50:51], s[52:53]
s_cmp_eq_u64 s[50:51], -1
s_cbranch_scc1 Lqok_1_%=
s_add_u32 s48, s48, 1
s_cmp_lt_u32 s48, 0x8000
s_cbranch_scc1 Lqrt_2_%=
s_branch LEND_%=
Lqok_1_%=:
v_mov_b32 v14, v40
v_mov_b32 v15, v42
v_mov_b32 v16, v44
v_mov_b32 v17, v46
v_cndmask_b32_e64 v22, 0, v14, s[36:37]
v_cndmask_b32_e64 v23, 0, v15, s[36:37]
v_cndmask_b32_e64 v24, 0, v16, s[36:37]
v_cndmask_b32_e64 v25, 0, v17, s[36:37]
v_cndmask_b32_e64 v26, 0, v14, s[38:39]
v_cndmask_b32_e64 v27, 0, v15, s[38:39]
v_cndmask_b32_e64 v28, 0, v16, s[38:39]
v_cndmask_b32_e64 v29, 0, v17, s[38:39]
v_cndmask_b32_e64 v30, 0, v14, s[40:41]
v_cndmask_b32_e64 v31, 0, v15, s[40:41]
v_cndmask_b32_e64 v32, 0, v16, s[40:41]
v_cndmask_b32_e64 v33, 0, v17, s[40:41]
v_cndmask_b32_e64 v34, 0, v14, s[42:43]
v_cndmask_b32_e64 v35, 0, v15, s[42:43]
v_cndmask_b32_e64 v36, 0, v16, s[42:43]
v_cndmask_b32_e64 v37, 0, v17, s[42:43]
s_nop 1
s_nop 7
v_mov_b32 v18, 0
v_mov_b32 v19, 0
v_mov_b32 v20, 0
v_mov_b32 v21, 0
v_mov_b32 v9, 0
v_mov_b32 v8, 0
s_mov_b32 s81, 1
s_nop 1
ds_read_b128 v[48:51], v11 offset:0
ds_read_b128 v[52:55], v11 offset:1024
ds_read_b128 v[56:59], v11 offset:2048
ds_read_b128 v[60:63], v11 offset:3072
ds_read_b128 v[64:67], v11 offset:4096
ds_read_b128 v[68:71], v11 offset:5120
ds_read_b128 v[72:75], v11 offset:6144
ds_read_b128 v[76:79], v11 offset:7168
s_waitcnt lgkmcnt(0)
v_mfma_f32_16x16x32_bf16 v[14:17], v[48:51], v[22:25], 0
v_mfma_f32_16x16x32_bf16 v[14:17], v[52:55], v[26:29], v[14:17]
v_mfma_f32_16x16x32_bf16 v[14:17], v[56:59], v[30:33], v[14:17]
v_mfma_f32_16x16x32_bf16 v[14:17], v[60:63], v[34:37], v[14:17]
s_nop 7
v_mul_f32_dpp v14, v14, v2 quad_perm:[0,0,0,0] row_mask:0xf bank_mask:0xf
v_mul_f32_dpp v15, v15, v2 quad_perm:[1,1,1,1] row_mask:0xf bank_mask:0xf
v_mul_f32_dpp v16, v16, v2 quad_perm:[2,2,2,2] row_mask:0xf bank_mask:0xf
v_mul_f32_dpp v17, v17, v2 quad_perm:[3,3,3,3] row_mask:0xf bank_mask:0xf
v_add_f32 v14, v14, v15
v_add_f32 v16, v16, v17
v_add_f32 v14, v14, v8
v_add_f32 v7, v14, v16
v_cmp_lt_f32 vcc, 0, v7
s_cbranch_vccnz Lnc_4_%=
s_cmp_lg_u32 s81, 0
s_cbranch_scc1 Lnc_4_%=
Lns_3_%=:
v_exp_f32 v7, v7
s_nop 0
v_add_f32 v9, v9, v7
v_cvt_pk_bf16_f32 v10, v7, v7
s_nop 1
v_and_b32_dpp v14, v10, v3 row_newbcast:0 row_mask:0xf bank_mask:0xf
v_and_b32_dpp v15, v10, v4 row_newbcast:0 row_mask:0xf bank_mask:0xf
v_and_b32_dpp v16, v10, v5 row_newbcast:0 row_mask:0xf bank_mask:0xf
v_and_b32_dpp v17, v10, v6 row_newbcast:0 row_mask:0xf bank_mask:0xf
s_nop 1
v_mfma_f32_16x16x32_bf16 v[18:21], v[64:67], v[14:17], v[18:21]
v_and_b32_dpp v14, v10, v3 row_newbcast:4 row_mask:0xf bank_mask:0xf
v_and_b32_dpp v15, v10, v4 row_newbcast:4 row_mask:0xf bank_mask:0xf
v_and_b32_dpp v16, v10, v5 row_newbcast:4 row_mask:0xf bank_mask:0xf
v_and_b32_dpp v17, v10, v6 row_newbcast:4 row_mask:0xf bank_mask:0xf
s_nop 1
v_mfma_f32_16x16x32_bf16 v[18:21], v[68:71], v[14:17], v[18:21]
v_and_b32_dpp v14, v10, v3 row_newbcast:8 row_mask:0xf bank_mask:0xf
v_and_b32_dpp v15, v10, v4 row_newbcast:8 row_mask:0xf bank_mask:0xf
v_and_b32_dpp v16, v10, v5 row_newbcast:8 row_mask:0xf bank_mask:0xf
v_and_b32_dpp v17, v10, v6 row_newbcast:8 row_mask:0xf bank_mask:0xf
s_nop 1
v_mfma_f32_16x16x32_bf16 v[18:21], v[72:75], v[14:17], v[18:21]
v_and_b32_dpp v14, v10, v3 row_newbcast:12 row_mask:0xf bank_mask:0xf
v_and_b32_dpp v15, v10, v4 row_newbcast:12 row_mask:0xf bank_mask:0xf
v_and_b32_dpp v16, v10, v5 row_newbcast:12 row_mask:0xf bank_mask:0xf
v_and_b32_dpp v17, v10, v6 row_newbcast:12 row_mask:0xf bank_mask:0xf
s_nop 1
v_mfma_f32_16x16x32_bf16 v[18:21], v[76:79], v[14:17], v[18:21]
s_waitcnt vmcnt(12)
s_add_u32 s29, s28, 1146880
buffer_load_dwordx4 a[160:163], v0, s[8:11], s29 offen offset:0 nt
buffer_load_dwordx4 a[164:167], v0, s[8:11], s29 offen offset:1024 nt
buffer_load_dwordx4 a[168:171], v0, s[8:11], s29 offen offset:2048 nt
buffer_load_dwordx4 a[172:175], v0, s[8:11], s29 offen offset:3072 nt
s_waitcnt vmcnt(12)
s_add_u32 s29, s28, 1146880
buffer_load_dwordx4 a[176:179], v1, s[12:15], s29 offen offset:0 nt
buffer_load_dwordx4 a[180:183], v1, s[12:15], s29 offen offset:1024 nt
buffer_load_dwordx4 a[184:187], v1, s[12:15], s29 offen offset:2048 nt
buffer_load_dwordx4 a[188:191], v1, s[12:15], s29 offen offset:3072 nt
ds_read_b128 v[48:51], v11 offset:8192
ds_read_b128 v[52:55], v11 offset:9216
ds_read_b128 v[56:59], v11 offset:10240
ds_read_b128 v[60:63], v11 offset:11264
ds_read_b128 v[64:67], v11 offset:12288
ds_read_b128 v[68:71], v11 offset:13312
ds_read_b128 v[72:75], v11 offset:14336
ds_read_b128 v[76:79], v11 offset:15360
s_waitcnt lgkmcnt(0)
v_mfma_f32_16x16x32_bf16 v[14:17], v[48:51], v[22:25], 0
v_mfma_f32_16x16x32_bf16 v[14:17], v[52:55], v[26:29], v[14:17]
v_mfma_f32_16x16x32_bf16 v[14:17], v[56:59], v[30:33], v[14:17]
v_mfma_f32_16x16x32_bf16 v[14:17], v[60:63], v[34:37], v[14:17]
s_nop 7
v_mul_f32_dpp v14, v14, v2 quad_perm:[0,0,0,0] row_mask:0xf bank_mask:0xf
v_mul_f32_dpp v15, v15, v2 quad_perm:[1,1,1,1] row_mask:0xf bank_mask:0xf
v_mul_f32_dpp v16, v16, v2 quad_perm:[2,2,2,2] row_mask:0xf bank_mask:0xf
v_mul_f32_dpp v17, v17, v2 quad_perm:[3,3,3,3] row_mask:0xf bank_mask:0xf
v_add_f32 v14, v14, v15
v_add_f32 v16, v16, v17
v_add_f32 v14, v14, v8
v_add_f32 v7, v14, v16
v_cmp_lt_f32 vcc, 0, v7
s_cbranch_vccnz Lnc_6_%=
s_cmp_lg_u32 s81, 0
s_cbranch_scc1 Lnc_6_%=
Lns_5_%=:
v_exp_f32 v7, v7
s_nop 0
v_add_f32 v9, v9, v7
v_cvt_pk_bf16_f32 v10, v7, v7
s_nop 1
v_and_b32_dpp v14, v10, v3 row_newbcast:0 row_mask:0xf bank_mask:0xf
v_and_b32_dpp v15, v10, v4 row_newbcast:0 row_mask:0xf bank_mask:0xf
v_and_b32_dpp v16, v10, v5 row_newbcast:0 row_mask:0xf bank_mask:0xf
v_and_b32_dpp v17, v10, v6 row_newbcast:0 row_mask:0xf bank_mask:0xf
s_nop 1
v_mfma_f32_16x16x32_bf16 v[18:21], v[64:67], v[14:17], v[18:21]
v_and_b32_dpp v14, v10, v3 row_newbcast:4 row_mask:0xf bank_mask:0xf
v_and_b32_dpp v15, v10, v4 row_newbcast:4 row_mask:0xf bank_mask:0xf
v_and_b32_dpp v16, v10, v5 row_newbcast:4 row_mask:0xf bank_mask:0xf
v_and_b32_dpp v17, v10, v6 row_newbcast:4 row_mask:0xf bank_mask:0xf
s_nop 1
v_mfma_f32_16x16x32_bf16 v[18:21], v[68:71], v[14:17], v[18:21]
v_and_b32_dpp v14, v10, v3 row_newbcast:8 row_mask:0xf bank_mask:0xf
v_and_b32_dpp v15, v10, v4 row_newbcast:8 row_mask:0xf bank_mask:0xf
v_and_b32_dpp v16, v10, v5 row_newbcast:8 row_mask:0xf bank_mask:0xf
v_and_b32_dpp v17, v10, v6 row_newbcast:8 row_mask:0xf bank_mask:0xf
s_nop 1
v_mfma_f32_16x16x32_bf16 v[18:21], v[72:75], v[14:17], v[18:21]
v_and_b32_dpp v14, v10, v3 row_newbcast:12 row_mask:0xf bank_mask:0xf
v_and_b32_dpp v15, v10, v4 row_newbcast:12 row_mask:0xf bank_mask:0xf
v_and_b32_dpp v16, v10, v5 row_newbcast:12 row_mask:0xf bank_mask:0xf
v_and_b32_dpp v17, v10, v6 row_newbcast:12 row_mask:0xf bank_mask:0xf
s_nop 1
v_mfma_f32_16x16x32_bf16 v[18:21], v[76:79], v[14:17], v[18:21]
s_waitcnt vmcnt(12)
s_add_u32 s29, s28, 1261568
buffer_load_dwordx4 a[192:195], v0, s[8:11], s29 offen offset:0 nt
buffer_load_dwordx4 a[196:199], v0, s[8:11], s29 offen offset:1024 nt
buffer_load_dwordx4 a[200:203], v0, s[8:11], s29 offen offset:2048 nt
buffer_load_dwordx4 a[204:207], v0, s[8:11], s29 offen offset:3072 nt
s_waitcnt vmcnt(12)
s_add_u32 s29, s28, 1261568
buffer_load_dwordx4 a[208:211], v1, s[12:15], s29 offen offset:0 nt
buffer_load_dwordx4 a[212:215], v1, s[12:15], s29 offen offset:1024 nt
buffer_load_dwordx4 a[216:219], v1, s[12:15], s29 offen offset:2048 nt
buffer_load_dwordx4 a[220:223], v1, s[12:15], s29 offen offset:3072 nt
ds_read_b128 v[48:51], v11 offset:16384
ds_read_b128 v[52:55], v11 offset:17408
ds_read_b128 v[56:59], v11 offset:18432
ds_read_b128 v[60:63], v11 offset:19456
ds_read_b128 v[64:67], v11 offset:20480
ds_read_b128 v[68:71], v11 offset:21504
ds_read_b128 v[72:75], v11 offset:22528
ds_read_b128 v[76:79], v11 offset:23552
s_waitcnt lgkmcnt(0)
v_mfma_f32_16x16x32_bf16 v[14:17], v[48:51], v[22:25], 0
v_mfma_f32_16x16x32_bf16 v[14:17], v[52:55], v[26:29], v[14:17]
v_mfma_f32_16x16x32_bf16 v[14:17], v[56:59], v[30:33], v[14:17]
v_mfma_f32_16x16x32_bf16 v[14:17], v[60:63], v[34:37], v[14:17]
s_nop 7
v_mul_f32_dpp v14, v14, v2 quad_perm:[0,0,0,0] row_mask:0xf bank_mask:0xf
v_mul_f32_dpp v15, v15, v2 quad_perm:[1,1,1,1] row_mask:0xf bank_mask:0xf
v_mul_f32_dpp v16, v16, v2 quad_perm:[2,2,2,2] row_mask:0xf bank_mask:0xf
v_mul_f32_dpp v17, v17, v2 quad_perm:[3,3,3,3] row_mask:0xf bank_mask:0xf
v_add_f32 v14, v14, v15
v_add_f32 v16, v16, v17
v_add_f32 v14, v14, v8
v_add_f32 v7, v14, v16
v_cmp_lt_f32 vcc, 0, v7
s_cbranch_vccnz Lnc_8_%=
s_cmp_lg_u32 s81, 0
s_cbranch_scc1 Lnc_8_%=
Lns_7_%=:
v_exp_f32 v7, v7
s_nop 0
v_add_f32 v9, v9, v7
v_cvt_pk_bf16_f32 v10, v7, v7
s_nop 1
v_and_b32_dpp v14, v10, v3 row_newbcast:0 row_mask:0xf bank_mask:0xf
v_and_b32_dpp v15, v10, v4 row_newbcast:0 row_mask:0xf bank_mask:0xf
v_and_b32_dpp v16, v10, v5 row_newbcast:0 row_mask:0xf bank_mask:0xf
v_and_b32_dpp v17, v10, v6 row_newbcast:0 row_mask:0xf bank_mask:0xf
s_nop 1
v_mfma_f32_16x16x32_bf16 v[18:21], v[64:67], v[14:17], v[18:21]
v_and_b32_dpp v14, v10, v3 row_newbcast:4 row_mask:0xf bank_mask:0xf
v_and_b32_dpp v15, v10, v4 row_newbcast:4 row_mask:0xf bank_mask:0xf
v_and_b32_dpp v16, v10, v5 row_newbcast:4 row_mask:0xf bank_mask:0xf
v_and_b32_dpp v17, v10, v6 row_newbcast:4 row_mask:0xf bank_mask:0xf
s_nop 1
v_mfma_f32_16x16x32_bf16 v[18:21], v[68:71], v[14:17], v[18:21]
v_and_b32_dpp v14, v10, v3 row_newbcast:8 row_mask:0xf bank_mask:0xf
v_and_b32_dpp v15, v10, v4 row_newbcast:8 row_mask:0xf bank_mask:0xf
v_and_b32_dpp v16, v10, v5 row_newbcast:8 row_mask:0xf bank_mask:0xf
v_and_b32_dpp v17, v10, v6 row_newbcast:8 row_mask:0xf bank_mask:0xf
s_nop 1
v_mfma_f32_16x16x32_bf16 v[18:21], v[72:75], v[14:17], v[18:21]
v_and_b32_dpp v14, v10, v3 row_newbcast:12 row_mask:0xf bank_mask:0xf
v_and_b32_dpp v15, v10, v4 row_newbcast:12 row_mask:0xf bank_mask:0xf
v_and_b32_dpp v16, v10, v5 row_newbcast:12 row_mask:0xf bank_mask:0xf
v_and_b32_dpp v17, v10, v6 row_newbcast:12 row_mask:0xf bank_mask:0xf
s_nop 1
v_mfma_f32_16x16x32_bf16 v[18:21], v[76:79], v[14:17], v[18:21]
s_waitcnt vmcnt(12)
s_add_u32 s29, s28, 1376256
buffer_load_dwordx4 a[224:227], v0, s[8:11], s29 offen offset:0 nt
buffer_load_dwordx4 a[228:231], v0, s[8:11], s29 offen offset:1024 nt
buffer_load_dwordx4 a[232:235], v0, s[8:11], s29 offen offset:2048 nt
buffer_load_dwordx4 a[236:239], v0, s[8:11], s29 offen offset:3072 nt
s_waitcnt vmcnt(12)
s_add_u32 s29, s28, 1376256
buffer_load_dwordx4 a[240:243], v1, s[12:15], s29 offen offset:0 nt
buffer_load_dwordx4 a[244:247], v1, s[12:15], s29 offen offset:1024 nt
buffer_load_dwordx4 a[248:251], v1, s[12:15], s29 offen offset:2048 nt
buffer_load_dwordx4 a[252:255], v1, s[12:15], s29 offen offset:3072 nt
ds_read_b128 v[48:51], v11 offset:24576
ds_read_b128 v[52:55], v11 offset:25600
ds_read_b128 v[56:59], v11 offset:26624
ds_read_b128 v[60:63], v11 offset:27648
ds_read_b128 v[64:67], v11 offset:28672
ds_read_b128 v[68:71], v11 offset:29696
ds_read_b128 v[72:75], v11 offset:30720
ds_read_b128 v[76:79], v11 offset:31744
s_waitcnt lgkmcnt(0)
v_mfma_f32_16x16x32_bf16 v[14:17], v[48:51], v[22:25], 0
v_mfma_f32_16x16x32_bf16 v[14:17], v[52:55], v[26:29], v[14:17]
v_mfma_f32_16x16x32_bf16 v[14:17], v[56:59], v[30:33], v[14:17]
v_mfma_f32_16x16x32_bf16 v[14:17], v[60:63], v[34:37], v[14:17]
s_nop 7
v_mul_f32_dpp v14, v14, v2 quad_perm:[0,0,0,0] row_mask:0xf bank_mask:0xf
v_mul_f32_dpp v15, v15, v2 quad_perm:[1,1,1,1] row_mask:0xf bank_mask:0xf
v_mul_f32_dpp v16, v16, v2 quad_perm:[2,2,2,2] row_mask:0xf bank_mask:0xf
v_mul_f32_dpp v17, v17, v2 quad_perm:[3,3,3,3] row_mask:0xf bank_mask:0xf
v_add_f32 v14, v14, v15
v_add_f32 v16, v16, v17
v_add_f32 v14, v14, v8
v_add_f32 v7, v14, v16
v_cmp_lt_f32 vcc, 0, v7
s_cbranch_vccnz Lnc_10_%=
s_cmp_lg_u32 s81, 0
s_cbranch_scc1 Lnc_10_%=
Lns_9_%=:
v_exp_f32 v7, v7
s_nop 0
v_add_f32 v9, v9, v7
v_cvt_pk_bf16_f32 v10, v7, v7
s_nop 1
v_and_b32_dpp v14, v10, v3 row_newbcast:0 row_mask:0xf bank_mask:0xf
v_and_b32_dpp v15, v10, v4 row_newbcast:0 row_mask:0xf bank_mask:0xf
v_and_b32_dpp v16, v10, v5 row_newbcast:0 row_mask:0xf bank_mask:0xf
v_and_b32_dpp v17, v10, v6 row_newbcast:0 row_mask:0xf bank_mask:0xf
s_nop 1
v_mfma_f32_16x16x32_bf16 v[18:21], v[64:67], v[14:17], v[18:21]
v_and_b32_dpp v14, v10, v3 row_newbcast:4 row_mask:0xf bank_mask:0xf
v_and_b32_dpp v15, v10, v4 row_newbcast:4 row_mask:0xf bank_mask:0xf
v_and_b32_dpp v16, v10, v5 row_newbcast:4 row_mask:0xf bank_mask:0xf
v_and_b32_dpp v17, v10, v6 row_newbcast:4 row_mask:0xf bank_mask:0xf
s_nop 1
v_mfma_f32_16x16x32_bf16 v[18:21], v[68:71], v[14:17], v[18:21]
v_and_b32_dpp v14, v10, v3 row_newbcast:8 row_mask:0xf bank_mask:0xf
v_and_b32_dpp v15, v10, v4 row_newbcast:8 row_mask:0xf bank_mask:0xf
v_and_b32_dpp v16, v10, v5 row_newbcast:8 row_mask:0xf bank_mask:0xf
v_and_b32_dpp v17, v10, v6 row_newbcast:8 row_mask:0xf bank_mask:0xf
s_nop 1
v_mfma_f32_16x16x32_bf16 v[18:21], v[72:75], v[14:17], v[18:21]
v_and_b32_dpp v14, v10, v3 row_newbcast:12 row_mask:0xf bank_mask:0xf
v_and_b32_dpp v15, v10, v4 row_newbcast:12 row_mask:0xf bank_mask:0xf
v_and_b32_dpp v16, v10, v5 row_newbcast:12 row_mask:0xf bank_mask:0xf
v_and_b32_dpp v17, v10, v6 row_newbcast:12 row_mask:0xf bank_mask:0xf
s_nop 1
v_mfma_f32_16x16x32_bf16 v[18:21], v[76:79], v[14:17], v[18:21]
s_waitcnt vmcnt(12)
s_add_u32 s29, s28, 1490944
buffer_load_dwordx4 v[96:99], v0, s[8:11], s29 offen offset:0 nt
buffer_load_dwordx4 v[100:103], v0, s[8:11], s29 offen offset:1024 nt
buffer_load_dwordx4 v[104:107], v0, s[8:11], s29 offen offset:2048 nt
buffer_load_dwordx4 v[108:111], v0, s[8:11], s29 offen offset:3072 nt
s_waitcnt vmcnt(12)
s_add_u32 s29, s28, 1490944
buffer_load_dwordx4 v[112:115], v1, s[12:15], s29 offen offset:0 nt
buffer_load_dwordx4 v[116:119], v1, s[12:15], s29 offen offset:1024 nt
buffer_load_dwordx4 v[120:123], v1, s[12:15], s29 offen offset:2048 nt
buffer_load_dwordx4 v[124:127], v1, s[12:15], s29 offen offset:3072 nt
ds_read_b128 v[48:51], v11 offset:32768
ds_read_b128 v[52:55], v11 offset:33792
ds_read_b128 v[56:59], v11 offset:34816
ds_read_b128 v[60:63], v11 offset:35840
ds_read_b128 v[64:67], v11 offset:36864
ds_read_b128 v[68:71], v11 offset:37888
ds_read_b128 v[72:75], v11 offset:38912
ds_read_b128 v[76:79], v11 offset:39936
s_waitcnt lgkmcnt(0)
v_mfma_f32_16x16x32_bf16 v[14:17], v[48:51], v[22:25], 0
v_mfma_f32_16x16x32_bf16 v[14:17], v[52:55], v[26:29], v[14:17]
v_mfma_f32_16x16x32_bf16 v[14:17], v[56:59], v[30:33], v[14:17]
v_mfma_f32_16x16x32_bf16 v[14:17], v[60:63], v[34:37], v[14:17]
s_nop 7
v_mul_f32_dpp v14, v14, v2 quad_perm:[0,0,0,0] row_mask:0xf bank_mask:0xf
v_mul_f32_dpp v15, v15, v2 quad_perm:[1,1,1,1] row_mask:0xf bank_mask:0xf
v_mul_f32_dpp v16, v16, v2 quad_perm:[2,2,2,2] row_mask:0xf bank_mask:0xf
v_mul_f32_dpp v17, v17, v2 quad_perm:[3,3,3,3] row_mask:0xf bank_mask:0xf
v_add_f32 v14, v14, v15
v_add_f32 v16, v16, v17
v_add_f32 v14, v14, v8
v_add_f32 v7, v14, v16
v_cmp_lt_f32 vcc, 0, v7
s_cbranch_vccnz Lnc_12_%=
s_cmp_lg_u32 s81, 0
s_cbranch_scc1 Lnc_12_%=
Lns_11_%=:
v_exp_f32 v7, v7
s_nop 0
v_add_f32 v9, v9, v7
v_cvt_pk_bf16_f32 v10, v7, v7
s_nop 1
v_and_b32_dpp v14, v10, v3 row_newbcast:0 row_mask:0xf bank_mask:0xf
v_and_b32_dpp v15, v10, v4 row_newbcast:0 row_mask:0xf bank_mask:0xf
v_and_b32_dpp v16, v10, v5 row_newbcast:0 row_mask:0xf bank_mask:0xf
v_and_b32_dpp v17, v10, v6 row_newbcast:0 row_mask:0xf bank_mask:0xf
s_nop 1
v_mfma_f32_16x16x32_bf16 v[18:21], v[64:67], v[14:17], v[18:21]
v_and_b32_dpp v14, v10, v3 row_newbcast:4 row_mask:0xf bank_mask:0xf
v_and_b32_dpp v15, v10, v4 row_newbcast:4 row_mask:0xf bank_mask:0xf
v_and_b32_dpp v16, v10, v5 row_newbcast:4 row_mask:0xf bank_mask:0xf
v_and_b32_dpp v17, v10, v6 row_newbcast:4 row_mask:0xf bank_mask:0xf
s_nop 1
v_mfma_f32_16x16x32_bf16 v[18:21], v[68:71], v[14:17], v[18:21]
v_and_b32_dpp v14, v10, v3 row_newbcast:8 row_mask:0xf bank_mask:0xf
v_and_b32_dpp v15, v10, v4 row_newbcast:8 row_mask:0xf bank_mask:0xf
v_and_b32_dpp v16, v10, v5 row_newbcast:8 row_mask:0xf bank_mask:0xf
v_and_b32_dpp v17, v10, v6 row_newbcast:8 row_mask:0xf bank_mask:0xf
s_nop 1
v_mfma_f32_16x16x32_bf16 v[18:21], v[72:75], v[14:17], v[18:21]
v_and_b32_dpp v14, v10, v3 row_newbcast:12 row_mask:0xf bank_mask:0xf
v_and_b32_dpp v15, v10, v4 row_newbcast:12 row_mask:0xf bank_mask:0xf
v_and_b32_dpp v16, v10, v5 row_newbcast:12 row_mask:0xf bank_mask:0xf
v_and_b32_dpp v17, v10, v6 row_newbcast:12 row_mask:0xf bank_mask:0xf
s_nop 1
v_mfma_f32_16x16x32_bf16 v[18:21], v[76:79], v[14:17], v[18:21]
s_waitcnt vmcnt(12)
s_add_u32 s29, s28, 1605632
buffer_load_dwordx4 v[128:131], v0, s[8:11], s29 offen offset:0 nt
buffer_load_dwordx4 v[132:135], v0, s[8:11], s29 offen offset:1024 nt
buffer_load_dwordx4 v[136:139], v0, s[8:11], s29 offen offset:2048 nt
buffer_load_dwordx4 v[140:143], v0, s[8:11], s29 offen offset:3072 nt
s_waitcnt vmcnt(12)
s_add_u32 s29, s28, 1605632
buffer_load_dwordx4 v[144:147], v1, s[12:15], s29 offen offset:0 nt
buffer_load_dwordx4 v[148:151], v1, s[12:15], s29 offen offset:1024 nt
buffer_load_dwordx4 v[152:155], v1, s[12:15], s29 offen offset:2048 nt
buffer_load_dwordx4 v[156:159], v1, s[12:15], s29 offen offset:3072 nt
v_mfma_f32_16x16x32_bf16 v[14:17], a[0:3], v[22:25], 0
v_mfma_f32_16x16x32_bf16 v[14:17], a[4:7], v[26:29], v[14:17]
v_mfma_f32_16x16x32_bf16 v[14:17], a[8:11], v[30:33], v[14:17]
v_mfma_f32_16x16x32_bf16 v[14:17], a[12:15], v[34:37], v[14:17]
s_nop 7
v_mul_f32_dpp v14, v14, v2 quad_perm:[0,0,0,0] row_mask:0xf bank_mask:0xf
v_mul_f32_dpp v15, v15, v2 quad_perm:[1,1,1,1] row_mask:0xf bank_mask:0xf
v_mul_f32_dpp v16, v16, v2 quad_perm:[2,2,2,2] row_mask:0xf bank_mask:0xf
v_mul_f32_dpp v17, v17, v2 quad_perm:[3,3,3,3] row_mask:0xf bank_mask:0xf
v_add_f32 v14, v14, v15
v_add_f32 v16, v16, v17
v_add_f32 v14, v14, v8
v_add_f32 v7, v14, v16
v_cmp_lt_f32 vcc, 0, v7
s_cbranch_vccnz Lnc_14_%=
s_cmp_lg_u32 s81, 0
s_cbranch_scc1 Lnc_14_%=
Lns_13_%=:
v_exp_f32 v7, v7
s_nop 0
v_add_f32 v9, v9, v7
v_cvt_pk_bf16_f32 v10, v7, v7
s_nop 1
v_and_b32_dpp v14, v10, v3 row_newbcast:0 row_mask:0xf bank_mask:0xf
v_and_b32_dpp v15, v10, v4 row_newbcast:0 row_mask:0xf bank_mask:0xf
v_and_b32_dpp v16, v10, v5 row_newbcast:0 row_mask:0xf bank_mask:0xf
v_and_b32_dpp v17, v10, v6 row_newbcast:0 row_mask:0xf bank_mask:0xf
s_nop 1
v_mfma_f32_16x16x32_bf16 v[18:21], a[16:19], v[14:17], v[18:21]
v_and_b32_dpp v14, v10, v3 row_newbcast:4 row_mask:0xf bank_mask:0xf
v_and_b32_dpp v15, v10, v4 row_newbcast:4 row_mask:0xf bank_mask:0xf
v_and_b32_dpp v16, v10, v5 row_newbcast:4 row_mask:0xf bank_mask:0xf
v_and_b32_dpp v17, v10, v6 row_newbcast:4 row_mask:0xf bank_mask:0xf
s_nop 1
v_mfma_f32_16x16x32_bf16 v[18:21], a[20:23], v[14:17], v[18:21]
v_and_b32_dpp v14, v10, v3 row_newbcast:8 row_mask:0xf bank_mask:0xf
v_and_b32_dpp v15, v10, v4 row_newbcast:8 row_mask:0xf bank_mask:0xf
v_and_b32_dpp v16, v10, v5 row_newbcast:8 row_mask:0xf bank_mask:0xf
v_and_b32_dpp v17, v10, v6 row_newbcast:8 row_mask:0xf bank_mask:0xf
s_nop 1
v_mfma_f32_16x16x32_bf16 v[18:21], a[24:27], v[14:17], v[18:21]
v_and_b32_dpp v14, v10, v3 row_newbcast:12 row_mask:0xf bank_mask:0xf
v_and_b32_dpp v15, v10, v4 row_newbcast:12 row_mask:0xf bank_mask:0xf
v_and_b32_dpp v16, v10, v5 row_newbcast:12 row_mask:0xf bank_mask:0xf
v_and_b32_dpp v17, v10, v6 row_newbcast:12 row_mask:0xf bank_mask:0xf
s_nop 1
v_mfma_f32_16x16x32_bf16 v[18:21], a[28:31], v[14:17], v[18:21]
s_waitcnt vmcnt(12)
s_add_u32 s29, s28, 1720320
buffer_load_dwordx4 v[160:163], v0, s[8:11], s29 offen offset:0 nt
buffer_load_dwordx4 v[164:167], v0, s[8:11], s29 offen offset:1024 nt
buffer_load_dwordx4 v[168:171], v0, s[8:11], s29 offen offset:2048 nt
buffer_load_dwordx4 v[172:175], v0, s[8:11], s29 offen offset:3072 nt
s_waitcnt vmcnt(12)
s_add_u32 s29, s28, 1720320
buffer_load_dwordx4 v[176:179], v1, s[12:15], s29 offen offset:0 nt
buffer_load_dwordx4 v[180:183], v1, s[12:15], s29 offen offset:1024 nt
buffer_load_dwordx4 v[184:187], v1, s[12:15], s29 offen offset:2048 nt
buffer_load_dwordx4 v[188:191], v1, s[12:15], s29 offen offset:3072 nt
v_mfma_f32_16x16x32_bf16 v[14:17], a[32:35], v[22:25], 0
v_mfma_f32_16x16x32_bf16 v[14:17], a[36:39], v[26:29], v[14:17]
v_mfma_f32_16x16x32_bf16 v[14:17], a[40:43], v[30:33], v[14:17]
v_mfma_f32_16x16x32_bf16 v[14:17], a[44:47], v[34:37], v[14:17]
s_nop 7
v_mul_f32_dpp v14, v14, v2 quad_perm:[0,0,0,0] row_mask:0xf bank_mask:0xf
v_mul_f32_dpp v15, v15, v2 quad_perm:[1,1,1,1] row_mask:0xf bank_mask:0xf
v_mul_f32_dpp v16, v16, v2 quad_perm:[2,2,2,2] row_mask:0xf bank_mask:0xf
v_mul_f32_dpp v17, v17, v2 quad_perm:[3,3,3,3] row_mask:0xf bank_mask:0xf
v_add_f32 v14, v14, v15
v_add_f32 v16, v16, v17
v_add_f32 v14, v14, v8
v_add_f32 v7, v14, v16
v_cmp_lt_f32 vcc, 0, v7
s_cbranch_vccnz Lnc_16_%=
s_cmp_lg_u32 s81, 0
s_cbranch_scc1 Lnc_16_%=
Lns_15_%=:
v_exp_f32 v7, v7
s_nop 0
v_add_f32 v9, v9, v7
v_cvt_pk_bf16_f32 v10, v7, v7
s_nop 1
v_and_b32_dpp v14, v10, v3 row_newbcast:0 row_mask:0xf bank_mask:0xf
v_and_b32_dpp v15, v10, v4 row_newbcast:0 row_mask:0xf bank_mask:0xf
v_and_b32_dpp v16, v10, v5 row_newbcast:0 row_mask:0xf bank_mask:0xf
v_and_b32_dpp v17, v10, v6 row_newbcast:0 row_mask:0xf bank_mask:0xf
s_nop 1
v_mfma_f32_16x16x32_bf16 v[18:21], a[48:51], v[14:17], v[18:21]
v_and_b32_dpp v14, v10, v3 row_newbcast:4 row_mask:0xf bank_mask:0xf
v_and_b32_dpp v15, v10, v4 row_newbcast:4 row_mask:0xf bank_mask:0xf
v_and_b32_dpp v16, v10, v5 row_newbcast:4 row_mask:0xf bank_mask:0xf
v_and_b32_dpp v17, v10, v6 row_newbcast:4 row_mask:0xf bank_mask:0xf
s_nop 1
v_mfma_f32_16x16x32_bf16 v[18:21], a[52:55], v[14:17], v[18:21]
v_and_b32_dpp v14, v10, v3 row_newbcast:8 row_mask:0xf bank_mask:0xf
v_and_b32_dpp v15, v10, v4 row_newbcast:8 row_mask:0xf bank_mask:0xf
v_and_b32_dpp v16, v10, v5 row_newbcast:8 row_mask:0xf bank_mask:0xf
v_and_b32_dpp v17, v10, v6 row_newbcast:8 row_mask:0xf bank_mask:0xf
s_nop 1
v_mfma_f32_16x16x32_bf16 v[18:21], a[56:59], v[14:17], v[18:21]
v_and_b32_dpp v14, v10, v3 row_newbcast:12 row_mask:0xf bank_mask:0xf
v_and_b32_dpp v15, v10, v4 row_newbcast:12 row_mask:0xf bank_mask:0xf
v_and_b32_dpp v16, v10, v5 row_newbcast:12 row_mask:0xf bank_mask:0xf
v_and_b32_dpp v17, v10, v6 row_newbcast:12 row_mask:0xf bank_mask:0xf
s_nop 1
v_mfma_f32_16x16x32_bf16 v[18:21], a[60:63], v[14:17], v[18:21]
s_waitcnt vmcnt(12)
s_add_u32 s29, s28, 1835008
buffer_load_dwordx4 v[192:195], v0, s[8:11], s29 offen offset:0 nt
buffer_load_dwordx4 v[196:199], v0, s[8:11], s29 offen offset:1024 nt
buffer_load_dwordx4 v[200:203], v0, s[8:11], s29 offen offset:2048 nt
buffer_load_dwordx4 v[204:207], v0, s[8:11], s29 offen offset:3072 nt
s_waitcnt vmcnt(12)
s_add_u32 s29, s28, 1835008
buffer_load_dwordx4 v[208:211], v1, s[12:15], s29 offen offset:0 nt
buffer_load_dwordx4 v[212:215], v1, s[12:15], s29 offen offset:1024 nt
buffer_load_dwordx4 v[216:219], v1, s[12:15], s29 offen offset:2048 nt
buffer_load_dwordx4 v[220:223], v1, s[12:15], s29 offen offset:3072 nt
v_mfma_f32_16x16x32_bf16 v[14:17], a[64:67], v[22:25], 0
v_mfma_f32_16x16x32_bf16 v[14:17], a[68:71], v[26:29], v[14:17]
v_mfma_f32_16x16x32_bf16 v[14:17], a[72:75], v[30:33], v[14:17]
v_mfma_f32_16x16x32_bf16 v[14:17], a[76:79], v[34:37], v[14:17]
s_nop 7
v_mul_f32_dpp v14, v14, v2 quad_perm:[0,0,0,0] row_mask:0xf bank_mask:0xf
v_mul_f32_dpp v15, v15, v2 quad_perm:[1,1,1,1] row_mask:0xf bank_mask:0xf
v_mul_f32_dpp v16, v16, v2 quad_perm:[2,2,2,2] row_mask:0xf bank_mask:0xf
v_mul_f32_dpp v17, v17, v2 quad_perm:[3,3,3,3] row_mask:0xf bank_mask:0xf
v_add_f32 v14, v14, v15
v_add_f32 v16, v16, v17
v_add_f32 v14, v14, v8
v_add_f32 v7, v14, v16
v_cmp_lt_f32 vcc, 0, v7
s_cbranch_vccnz Lnc_18_%=
s_cmp_lg_u32 s81, 0
s_cbranch_scc1 Lnc_18_%=
Lns_17_%=:
v_exp_f32 v7, v7
s_nop 0
v_add_f32 v9, v9, v7
v_cvt_pk_bf16_f32 v10, v7, v7
s_nop 1
v_and_b32_dpp v14, v10, v3 row_newbcast:0 row_mask:0xf bank_mask:0xf
v_and_b32_dpp v15, v10, v4 row_newbcast:0 row_mask:0xf bank_mask:0xf
v_and_b32_dpp v16, v10, v5 row_newbcast:0 row_mask:0xf bank_mask:0xf
v_and_b32_dpp v17, v10, v6 row_newbcast:0 row_mask:0xf bank_mask:0xf
s_nop 1
v_mfma_f32_16x16x32_bf16 v[18:21], a[80:83], v[14:17], v[18:21]
v_and_b32_dpp v14, v10, v3 row_newbcast:4 row_mask:0xf bank_mask:0xf
v_and_b32_dpp v15, v10, v4 row_newbcast:4 row_mask:0xf bank_mask:0xf
v_and_b32_dpp v16, v10, v5 row_newbcast:4 row_mask:0xf bank_mask:0xf
v_and_b32_dpp v17, v10, v6 row_newbcast:4 row_mask:0xf bank_mask:0xf
s_nop 1
v_mfma_f32_16x16x32_bf16 v[18:21], a[84:87], v[14:17], v[18:21]
v_and_b32_dpp v14, v10, v3 row_newbcast:8 row_mask:0xf bank_mask:0xf
v_and_b32_dpp v15, v10, v4 row_newbcast:8 row_mask:0xf bank_mask:0xf
v_and_b32_dpp v16, v10, v5 row_newbcast:8 row_mask:0xf bank_mask:0xf
v_and_b32_dpp v17, v10, v6 row_newbcast:8 row_mask:0xf bank_mask:0xf
s_nop 1
v_mfma_f32_16x16x32_bf16 v[18:21], a[88:91], v[14:17], v[18:21]
v_and_b32_dpp v14, v10, v3 row_newbcast:12 row_mask:0xf bank_mask:0xf
v_and_b32_dpp v15, v10, v4 row_newbcast:12 row_mask:0xf bank_mask:0xf
v_and_b32_dpp v16, v10, v5 row_newbcast:12 row_mask:0xf bank_mask:0xf
v_and_b32_dpp v17, v10, v6 row_newbcast:12 row_mask:0xf bank_mask:0xf
s_nop 1
v_mfma_f32_16x16x32_bf16 v[18:21], a[92:95], v[14:17], v[18:21]
s_waitcnt vmcnt(12)
s_add_u32 s29, s28, 1949696
buffer_load_dwordx4 v[224:227], v0, s[8:11], s29 offen offset:0 nt
buffer_load_dwordx4 v[228:231], v0, s[8:11], s29 offen offset:1024 nt
buffer_load_dwordx4 v[232:235], v0, s[8:11], s29 offen offset:2048 nt
buffer_load_dwordx4 v[236:239], v0, s[8:11], s29 offen offset:3072 nt
s_waitcnt vmcnt(12)
s_add_u32 s29, s28, 1949696
buffer_load_dwordx4 v[240:243], v1, s[12:15], s29 offen offset:0 nt
buffer_load_dwordx4 v[244:247], v1, s[12:15], s29 offen offset:1024 nt
buffer_load_dwordx4 v[248:251], v1, s[12:15], s29 offen offset:2048 nt
buffer_load_dwordx4 v[252:255], v1, s[12:15], s29 offen offset:3072 nt
s_mov_b32 s76, s44
s_add_u32 s70, s28, 2064384
s_mov_b32 s77, 0
s_add_u32 s71, s28, 2179072
s_mov_b32 s78, 0
s_mov_b32 s80, 0
s_mov_b32 s82, 0
v_and_b32 v12, 0xff, v0
v_lshlrev_b32 v12, 1, v12
s_lshl_b32 s50, s49, 9
s_add_u32 s50, s50, 20480
v_add_u32 v12, s50, v12
v_mov_b32 v39, v12
s_waitcnt vmcnt(12)
s_cmp_eq_u32 s77, 1
s_cselect_b32 s56, s16, s8
s_cselect_b32 s57, s17, s9
s_mov_b32 s58, 0x400000
buffer_load_dwordx4 v[48:51], v0, s[56:59], s70 offen offset:0 nt
buffer_load_dwordx4 v[52:55], v0, s[56:59], s70 offen offset:1024 nt
buffer_load_dwordx4 v[56:59], v0, s[56:59], s70 offen offset:2048 nt
buffer_load_dwordx4 v[60:63], v0, s[56:59], s70 offen offset:3072 nt
s_waitcnt vmcnt(12)
s_cmp_eq_u32 s77, 1
s_cselect_b32 s60, s20, s12
s_cselect_b32 s61, s21, s13
s_mov_b32 s62, 0x400000
s_cmp_ge_u32 s70, 2359296
s_cbranch_scc1 Lpo_20_%=
buffer_load_dwordx4 v[64:67], v1, s[60:63], s70 offen offset:0 nt
buffer_load_dwordx4 v[68:71], v1, s[60:63], s70 offen offset:1024 nt
buffer_load_dwordx4 v[72:75], v1, s[60:63], s70 offen offset:2048 nt
buffer_load_dwordx4 v[76:79], v1, s[60:63], s70 offen offset:3072 nt
Lpn_19_%=:
s_waitcnt vmcnt(12)
s_cmp_eq_u32 s78, 1
s_cselect_b32 s56, s16, s8
s_cselect_b32 s57, s17, s9
s_mov_b32 s58, 0x400000
buffer_load_dwordx4 v[80:83], v0, s[56:59], s71 offen offset:0 nt
buffer_load_dwordx4 v[84:87], v0, s[56:59], s71 offen offset:1024 nt
buffer_load_dwordx4 v[88:91], v0, s[56:59], s71 offen offset:2048 nt
buffer_load_dwordx4 v[92:95], v0, s[56:59], s71 offen offset:3072 nt
v_mfma_f32_16x16x32_bf16 v[14:17], a[96:99], v[22:25], 0
v_mfma_f32_16x16x32_bf16 v[14:17], a[100:103], v[26:29], v[14:17]
v_mfma_f32_16x16x32_bf16 v[14:17], a[104:107], v[30:33], v[14:17]
v_mfma_f32_16x16x32_bf16 v[14:17], a[108:111], v[34:37], v[14:17]
s_nop 7
v_mul_f32_dpp v14, v14, v2 quad_perm:[0,0,0,0] row_mask:0xf bank_mask:0xf
v_mul_f32_dpp v15, v15, v2 quad_perm:[1,1,1,1] row_mask:0xf bank_mask:0xf
v_mul_f32_dpp v16, v16, v2 quad_perm:[2,2,2,2] row_mask:0xf bank_mask:0xf
v_mul_f32_dpp v17, v17, v2 quad_perm:[3,3,3,3] row_mask:0xf bank_mask:0xf
v_add_f32 v14, v14, v15
v_add_f32 v16, v16, v17
v_add_f32 v14, v14, v8
v_add_f32 v7, v14, v16
v_cmp_lt_f32 vcc, 0, v7
s_cbranch_vccnz Lnc_22_%=
s_cmp_lg_u32 s81, 0
s_cbranch_scc1 Lnc_22_%=
Lns_21_%=:
v_exp_f32 v7, v7
s_nop 0
v_add_f32 v9, v9, v7
v_cvt_pk_bf16_f32 v10, v7, v7
s_nop 1
v_and_b32_dpp v14, v10, v3 row_newbcast:0 row_mask:0xf bank_mask:0xf
v_and_b32_dpp v15, v10, v4 row_newbcast:0 row_mask:0xf bank_mask:0xf
v_and_b32_dpp v16, v10, v5 row_newbcast:0 row_mask:0xf bank_mask:0xf
v_and_b32_dpp v17, v10, v6 row_newbcast:0 row_mask:0xf bank_mask:0xf
s_nop 1
v_mfma_f32_16x16x32_bf16 v[18:21], a[112:115], v[14:17], v[18:21]
v_and_b32_dpp v14, v10, v3 row_newbcast:4 row_mask:0xf bank_mask:0xf
v_and_b32_dpp v15, v10, v4 row_newbcast:4 row_mask:0xf bank_mask:0xf
v_and_b32_dpp v16, v10, v5 row_newbcast:4 row_mask:0xf bank_mask:0xf
v_and_b32_dpp v17, v10, v6 row_newbcast:4 row_mask:0xf bank_mask:0xf
s_nop 1
v_mfma_f32_16x16x32_bf16 v[18:21], a[116:119], v[14:17], v[18:21]
v_and_b32_dpp v14, v10, v3 row_newbcast:8 row_mask:0xf bank_mask:0xf
v_and_b32_dpp v15, v10, v4 row_newbcast:8 row_mask:0xf bank_mask:0xf
v_and_b32_dpp v16, v10, v5 row_newbcast:8 row_mask:0xf bank_mask:0xf
v_and_b32_dpp v17, v10, v6 row_newbcast:8 row_mask:0xf bank_mask:0xf
s_nop 1
v_mfma_f32_16x16x32_bf16 v[18:21], a[120:123], v[14:17], v[18:21]
v_and_b32_dpp v14, v10, v3 row_newbcast:12 row_mask:0xf bank_mask:0xf
v_and_b32_dpp v15, v10, v4 row_newbcast:12 row_mask:0xf bank_mask:0xf
v_and_b32_dpp v16, v10, v5 row_newbcast:12 row_mask:0xf bank_mask:0xf
v_and_b32_dpp v17, v10, v6 row_newbcast:12 row_mask:0xf bank_mask:0xf
s_nop 1
v_mfma_f32_16x16x32_bf16 v[18:21], a[124:127], v[14:17], v[18:21]
v_mfma_f32_16x16x32_bf16 v[14:17], a[128:131], v[22:25], 0
v_mfma_f32_16x16x32_bf16 v[14:17], a[132:135], v[26:29], v[14:17]
v_mfma_f32_16x16x32_bf16 v[14:17], a[136:139], v[30:33], v[14:17]
v_mfma_f32_16x16x32_bf16 v[14:17], a[140:143], v[34:37], v[14:17]
s_nop 7
v_mul_f32_dpp v14, v14, v2 quad_perm:[0,0,0,0] row_mask:0xf bank_mask:0xf
v_mul_f32_dpp v15, v15, v2 quad_perm:[1,1,1,1] row_mask:0xf bank_mask:0xf
v_mul_f32_dpp v16, v16, v2 quad_perm:[2,2,2,2] row_mask:0xf bank_mask:0xf
v_mul_f32_dpp v17, v17, v2 quad_perm:[3,3,3,3] row_mask:0xf bank_mask:0xf
v_add_f32 v14, v14, v15
v_add_f32 v16, v16, v17
v_add_f32 v14, v14, v8
v_add_f32 v7, v14, v16
v_cmp_lt_f32 vcc, 0, v7
s_cbranch_vccnz Lnc_24_%=
s_cmp_lg_u32 s81, 0
s_cbranch_scc1 Lnc_24_%=
Lns_23_%=:
v_exp_f32 v7, v7
s_nop 0
v_add_f32 v9, v9, v7
v_cvt_pk_bf16_f32 v10, v7, v7
s_nop 1
v_and_b32_dpp v14, v10, v3 row_newbcast:0 row_mask:0xf bank_mask:0xf
v_and_b32_dpp v15, v10, v4 row_newbcast:0 row_mask:0xf bank_mask:0xf
v_and_b32_dpp v16, v10, v5 row_newbcast:0 row_mask:0xf bank_mask:0xf
v_and_b32_dpp v17, v10, v6 row_newbcast:0 row_mask:0xf bank_mask:0xf
s_nop 1
v_mfma_f32_16x16x32_bf16 v[18:21], a[144:147], v[14:17], v[18:21]
v_and_b32_dpp v14, v10, v3 row_newbcast:4 row_mask:0xf bank_mask:0xf
v_and_b32_dpp v15, v10, v4 row_newbcast:4 row_mask:0xf bank_mask:0xf
v_and_b32_dpp v16, v10, v5 row_newbcast:4 row_mask:0xf bank_mask:0xf
v_and_b32_dpp v17, v10, v6 row_newbcast:4 row_mask:0xf bank_mask:0xf
s_nop 1
v_mfma_f32_16x16x32_bf16 v[18:21], a[148:151], v[14:17], v[18:21]
v_and_b32_dpp v14, v10, v3 row_newbcast:8 row_mask:0xf bank_mask:0xf
v_and_b32_dpp v15, v10, v4 row_newbcast:8 row_mask:0xf bank_mask:0xf
v_and_b32_dpp v16, v10, v5 row_newbcast:8 row_mask:0xf bank_mask:0xf
v_and_b32_dpp v17, v10, v6 row_newbcast:8 row_mask:0xf bank_mask:0xf
s_nop 1
v_mfma_f32_16x16x32_bf16 v[18:21], a[152:155], v[14:17], v[18:21]
v_and_b32_dpp v14, v10, v3 row_newbcast:12 row_mask:0xf bank_mask:0xf
v_and_b32_dpp v15, v10, v4 row_newbcast:12 row_mask:0xf bank_mask:0xf
v_and_b32_dpp v16, v10, v5 row_newbcast:12 row_mask:0xf bank_mask:0xf
v_and_b32_dpp v17, v10, v6 row_newbcast:12 row_mask:0xf bank_mask:0xf
s_nop 1
v_mfma_f32_16x16x32_bf16 v[18:21], a[156:159], v[14:17], v[18:21]
v_mfma_f32_16x16x32_bf16 v[14:17], a[160:163], v[22:25], 0
v_mfma_f32_16x16x32_bf16 v[14:17], a[164:167], v[26:29], v[14:17]
v_mfma_f32_16x16x32_bf16 v[14:17], a[168:171], v[30:33], v[14:17]
v_mfma_f32_16x16x32_bf16 v[14:17], a[172:175], v[34:37], v[14:17]
s_nop 7
v_mul_f32_dpp v14, v14, v2 quad_perm:[0,0,0,0] row_mask:0xf bank_mask:0xf
v_mul_f32_dpp v15, v15, v2 quad_perm:[1,1,1,1] row_mask:0xf bank_mask:0xf
v_mul_f32_dpp v16, v16, v2 quad_perm:[2,2,2,2] row_mask:0xf bank_mask:0xf
v_mul_f32_dpp v17, v17, v2 quad_perm:[3,3,3,3] row_mask:0xf bank_mask:0xf
v_add_f32 v14, v14, v15
v_add_f32 v16, v16, v17
v_add_f32 v14, v14, v8
v_add_f32 v7, v14, v16
v_cmp_lt_f32 vcc, 0, v7
s_cbranch_vccnz Lnc_26_%=
s_cmp_lg_u32 s81, 0
s_cbranch_scc1 Lnc_26_%=
Lns_25_%=:
v_exp_f32 v7, v7
s_nop 0
v_add_f32 v9, v9, v7
v_cvt_pk_bf16_f32 v10, v7, v7
s_nop 1
v_and_b32_dpp v14, v10, v3 row_newbcast:0 row_mask:0xf bank_mask:0xf
v_and_b32_dpp v15, v10, v4 row_newbcast:0 row_mask:0xf bank_mask:0xf
v_and_b32_dpp v16, v10, v5 row_newbcast:0 row_mask:0xf bank_mask:0xf
v_and_b32_dpp v17, v10, v6 row_newbcast:0 row_mask:0xf bank_mask:0xf
s_nop 1
v_mfma_f32_16x16x32_bf16 v[18:21], a[176:179], v[14:17], v[18:21]
v_and_b32_dpp v14, v10, v3 row_newbcast:4 row_mask:0xf bank_mask:0xf
v_and_b32_dpp v15, v10, v4 row_newbcast:4 row_mask:0xf bank_mask:0xf
v_and_b32_dpp v16, v10, v5 row_newbcast:4 row_mask:0xf bank_mask:0xf
v_and_b32_dpp v17, v10, v6 row_newbcast:4 row_mask:0xf bank_mask:0xf
s_nop 1
v_mfma_f32_16x16x32_bf16 v[18:21], a[180:183], v[14:17], v[18:21]
v_and_b32_dpp v14, v10, v3 row_newbcast:8 row_mask:0xf bank_mask:0xf
v_and_b32_dpp v15, v10, v4 row_newbcast:8 row_mask:0xf bank_mask:0xf
v_and_b32_dpp v16, v10, v5 row_newbcast:8 row_mask:0xf bank_mask:0xf
v_and_b32_dpp v17, v10, v6 row_newbcast:8 row_mask:0xf bank_mask:0xf
s_nop 1
v_mfma_f32_16x16x32_bf16 v[18:21], a[184:187], v[14:17], v[18:21]
v_and_b32_dpp v14, v10, v3 row_newbcast:12 row_mask:0xf bank_mask:0xf
v_and_b32_dpp v15, v10, v4 row_newbcast:12 row_mask:0xf bank_mask:0xf
v_and_b32_dpp v16, v10, v5 row_newbcast:12 row_mask:0xf bank_mask:0xf
v_and_b32_dpp v17, v10, v6 row_newbcast:12 row_mask:0xf bank_mask:0xf
s_nop 1
v_mfma_f32_16x16x32_bf16 v[18:21], a[188:191], v[14:17], v[18:21]
v_mfma_f32_16x16x32_bf16 v[14:17], a[192:195], v[22:25], 0
v_mfma_f32_16x16x32_bf16 v[14:17], a[196:199], v[26:29], v[14:17]
v_mfma_f32_16x16x32_bf16 v[14:17], a[200:203], v[30:33], v[14:17]
v_mfma_f32_16x16x32_bf16 v[14:17], a[204:207], v[34:37], v[14:17]
s_nop 7
v_mul_f32_dpp v14, v14, v2 quad_perm:[0,0,0,0] row_mask:0xf bank_mask:0xf
v_mul_f32_dpp v15, v15, v2 quad_perm:[1,1,1,1] row_mask:0xf bank_mask:0xf
v_mul_f32_dpp v16, v16, v2 quad_perm:[2,2,2,2] row_mask:0xf bank_mask:0xf
v_mul_f32_dpp v17, v17, v2 quad_perm:[3,3,3,3] row_mask:0xf bank_mask:0xf
v_add_f32 v14, v14, v15
v_add_f32 v16, v16, v17
v_add_f32 v14, v14, v8
v_add_f32 v7, v14, v16
v_cmp_lt_f32 vcc, 0, v7
s_cbranch_vccnz Lnc_28_%=
s_cmp_lg_u32 s81, 0
s_cbranch_scc1 Lnc_28_%=
Lns_27_%=:
v_exp_f32 v7, v7
s_nop 0
v_add_f32 v9, v9, v7
v_cvt_pk_bf16_f32 v10, v7, v7
s_nop 1
v_and_b32_dpp v14, v10, v3 row_newbcast:0 row_mask:0xf bank_mask:0xf
v_and_b32_dpp v15, v10, v4 row_newbcast:0 row_mask:0xf bank_mask:0xf
v_and_b32_dpp v16, v10, v5 row_newbcast:0 row_mask:0xf bank_mask:0xf
v_and_b32_dpp v17, v10, v6 row_newbcast:0 row_mask:0xf bank_mask:0xf
s_nop 1
v_mfma_f32_16x16x32_bf16 v[18:21], a[208:211], v[14:17], v[18:21]
v_and_b32_dpp v14, v10, v3 row_newbcast:4 row_mask:0xf bank_mask:0xf
v_and_b32_dpp v15, v10, v4 row_newbcast:4 row_mask:0xf bank_mask:0xf
v_and_b32_dpp v16, v10, v5 row_newbcast:4 row_mask:0xf bank_mask:0xf
v_and_b32_dpp v17, v10, v6 row_newbcast:4 row_mask:0xf bank_mask:0xf
s_nop 1
v_mfma_f32_16x16x32_bf16 v[18:21], a[212:215], v[14:17], v[18:21]
v_and_b32_dpp v14, v10, v3 row_newbcast:8 row_mask:0xf bank_mask:0xf
v_and_b32_dpp v15, v10, v4 row_newbcast:8 row_mask:0xf bank_mask:0xf
v_and_b32_dpp v16, v10, v5 row_newbcast:8 row_mask:0xf bank_mask:0xf
v_and_b32_dpp v17, v10, v6 row_newbcast:8 row_mask:0xf bank_mask:0xf
s_nop 1
v_mfma_f32_16x16x32_bf16 v[18:21], a[216:219], v[14:17], v[18:21]
v_and_b32_dpp v14, v10, v3 row_newbcast:12 row_mask:0xf bank_mask:0xf
v_and_b32_dpp v15, v10, v4 row_newbcast:12 row_mask:0xf bank_mask:0xf
v_and_b32_dpp v16, v10, v5 row_newbcast:12 row_mask:0xf bank_mask:0xf
v_and_b32_dpp v17, v10, v6 row_newbcast:12 row_mask:0xf bank_mask:0xf
s_nop 1
v_mfma_f32_16x16x32_bf16 v[18:21], a[220:223], v[14:17], v[18:21]
v_mfma_f32_16x16x32_bf16 v[14:17], a[224:227], v[22:25], 0
v_mfma_f32_16x16x32_bf16 v[14:17], a[228:231], v[26:29], v[14:17]
v_mfma_f32_16x16x32_bf16 v[14:17], a[232:235], v[30:33], v[14:17]
v_mfma_f32_16x16x32_bf16 v[14:17], a[236:239], v[34:37], v[14:17]
s_nop 7
v_mul_f32_dpp v14, v14, v2 quad_perm:[0,0,0,0] row_mask:0xf bank_mask:0xf
v_mul_f32_dpp v15, v15, v2 quad_perm:[1,1,1,1] row_mask:0xf bank_mask:0xf
v_mul_f32_dpp v16, v16, v2 quad_perm:[2,2,2,2] row_mask:0xf bank_mask:0xf
v_mul_f32_dpp v17, v17, v2 quad_perm:[3,3,3,3] row_mask:0xf bank_mask:0xf
v_add_f32 v14, v14, v15
v_add_f32 v16, v16, v17
v_add_f32 v14, v14, v8
v_add_f32 v7, v14, v16
v_cmp_lt_f32 vcc, 0, v7
s_cbranch_vccnz Lnc_30_%=
s_cmp_lg_u32 s81, 0
s_cbranch_scc1 Lnc_30_%=
Lns_29_%=:
v_exp_f32 v7, v7
s_nop 0
v_add_f32 v9, v9, v7
v_cvt_pk_bf16_f32 v10, v7, v7
s_nop 1
v_and_b32_dpp v14, v10, v3 row_newbcast:0 row_mask:0xf bank_mask:0xf
v_and_b32_dpp v15, v10, v4 row_newbcast:0 row_mask:0xf bank_mask:0xf
v_and_b32_dpp v16, v10, v5 row_newbcast:0 row_mask:0xf bank_mask:0xf
v_and_b32_dpp v17, v10, v6 row_newbcast:0 row_mask:0xf bank_mask:0xf
s_nop 1
v_mfma_f32_16x16x32_bf16 v[18:21], a[240:243], v[14:17], v[18:21]
v_and_b32_dpp v14, v10, v3 row_newbcast:4 row_mask:0xf bank_mask:0xf
v_and_b32_dpp v15, v10, v4 row_newbcast:4 row_mask:0xf bank_mask:0xf
v_and_b32_dpp v16, v10, v5 row_newbcast:4 row_mask:0xf bank_mask:0xf
v_and_b32_dpp v17, v10, v6 row_newbcast:4 row_mask:0xf bank_mask:0xf
s_nop 1
v_mfma_f32_16x16x32_bf16 v[18:21], a[244:247], v[14:17], v[18:21]
v_and_b32_dpp v14, v10, v3 row_newbcast:8 row_mask:0xf bank_mask:0xf
v_and_b32_dpp v15, v10, v4 row_newbcast:8 row_mask:0xf bank_mask:0xf
v_and_b32_dpp v16, v10, v5 row_newbcast:8 row_mask:0xf bank_mask:0xf
v_and_b32_dpp v17, v10, v6 row_newbcast:8 row_mask:0xf bank_mask:0xf
s_nop 1
v_mfma_f32_16x16x32_bf16 v[18:21], a[248:251], v[14:17], v[18:21]
v_and_b32_dpp v14, v10, v3 row_newbcast:12 row_mask:0xf bank_mask:0xf
v_and_b32_dpp v15, v10, v4 row_newbcast:12 row_mask:0xf bank_mask:0xf
v_and_b32_dpp v16, v10, v5 row_newbcast:12 row_mask:0xf bank_mask:0xf
v_and_b32_dpp v17, v10, v6 row_newbcast:12 row_mask:0xf bank_mask:0xf
s_nop 1
v_mfma_f32_16x16x32_bf16 v[18:21], a[252:255], v[14:17], v[18:21]
v_mfma_f32_16x16x32_bf16 v[14:17], v[96:99], v[22:25], 0
v_mfma_f32_16x16x32_bf16 v[14:17], v[100:103], v[26:29], v[14:17]
v_mfma_f32_16x16x32_bf16 v[14:17], v[104:107], v[30:33], v[14:17]
v_mfma_f32_16x16x32_bf16 v[14:17], v[108:111], v[34:37], v[14:17]
s_nop 7
v_mul_f32_dpp v14, v14, v2 quad_perm:[0,0,0,0] row_mask:0xf bank_mask:0xf
v_mul_f32_dpp v15, v15, v2 quad_perm:[1,1,1,1] row_mask:0xf bank_mask:0xf
v_mul_f32_dpp v16, v16, v2 quad_perm:[2,2,2,2] row_mask:0xf bank_mask:0xf
v_mul_f32_dpp v17, v17, v2 quad_perm:[3,3,3,3] row_mask:0xf bank_mask:0xf
v_add_f32 v14, v14, v15
v_add_f32 v16, v16, v17
v_add_f32 v14, v14, v8
v_add_f32 v7, v14, v16
v_cmp_lt_f32 vcc, 0, v7
s_cbranch_vccnz Lnc_32_%=
s_cmp_lg_u32 s81, 0
s_cbranch_scc1 Lnc_32_%=
Lns_31_%=:
v_exp_f32 v7, v7
s_nop 0
v_add_f32 v9, v9, v7
v_cvt_pk_bf16_f32 v10, v7, v7
s_nop 1
v_and_b32_dpp v14, v10, v3 row_newbcast:0 row_mask:0xf bank_mask:0xf
v_and_b32_dpp v15, v10, v4 row_newbcast:0 row_mask:0xf bank_mask:0xf
v_and_b32_dpp v16, v10, v5 row_newbcast:0 row_mask:0xf bank_mask:0xf
v_and_b32_dpp v17, v10, v6 row_newbcast:0 row_mask:0xf bank_mask:0xf
s_nop 1
v_mfma_f32_16x16x32_bf16 v[18:21], v[112:115], v[14:17], v[18:21]
v_and_b32_dpp v14, v10, v3 row_newbcast:4 row_mask:0xf bank_mask:0xf
v_and_b32_dpp v15, v10, v4 row_newbcast:4 row_mask:0xf bank_mask:0xf
v_and_b32_dpp v16, v10, v5 row_newbcast:4 row_mask:0xf bank_mask:0xf
v_and_b32_dpp v17, v10, v6 row_newbcast:4 row_mask:0xf bank_mask:0xf
s_nop 1
v_mfma_f32_16x16x32_bf16 v[18:21], v[116:119], v[14:17], v[18:21]
v_and_b32_dpp v14, v10, v3 row_newbcast:8 row_mask:0xf bank_mask:0xf
v_and_b32_dpp v15, v10, v4 row_newbcast:8 row_mask:0xf bank_mask:0xf
v_and_b32_dpp v16, v10, v5 row_newbcast:8 row_mask:0xf bank_mask:0xf
v_and_b32_dpp v17, v10, v6 row_newbcast:8 row_mask:0xf bank_mask:0xf
s_nop 1
v_mfma_f32_16x16x32_bf16 v[18:21], v[120:123], v[14:17], v[18:21]
v_and_b32_dpp v14, v10, v3 row_newbcast:12 row_mask:0xf bank_mask:0xf
v_and_b32_dpp v15, v10, v4 row_newbcast:12 row_mask:0xf bank_mask:0xf
v_and_b32_dpp v16, v10, v5 row_newbcast:12 row_mask:0xf bank_mask:0xf
v_and_b32_dpp v17, v10, v6 row_newbcast:12 row_mask:0xf bank_mask:0xf
s_nop 1
v_mfma_f32_16x16x32_bf16 v[18:21], v[124:127], v[14:17], v[18:21]
v_mfma_f32_16x16x32_bf16 v[14:17], v[128:131], v[22:25], 0
v_mfma_f32_16x16x32_bf16 v[14:17], v[132:135], v[26:29], v[14:17]
v_mfma_f32_16x16x32_bf16 v[14:17], v[136:139], v[30:33], v[14:17]
v_mfma_f32_16x16x32_bf16 v[14:17], v[140:143], v[34:37], v[14:17]
s_nop 7
v_mul_f32_dpp v14, v14, v2 quad_perm:[0,0,0,0] row_mask:0xf bank_mask:0xf
v_mul_f32_dpp v15, v15, v2 quad_perm:[1,1,1,1] row_mask:0xf bank_mask:0xf
v_mul_f32_dpp v16, v16, v2 quad_perm:[2,2,2,2] row_mask:0xf bank_mask:0xf
v_mul_f32_dpp v17, v17, v2 quad_perm:[3,3,3,3] row_mask:0xf bank_mask:0xf
v_add_f32 v14, v14, v15
v_add_f32 v16, v16, v17
v_add_f32 v14, v14, v8
v_add_f32 v7, v14, v16
v_cmp_lt_f32 vcc, 0, v7
s_cbranch_vccnz Lnc_34_%=
s_cmp_lg_u32 s81, 0
s_cbranch_scc1 Lnc_34_%=
Lns_33_%=:
v_exp_f32 v7, v7
s_nop 0
v_add_f32 v9, v9, v7
v_cvt_pk_bf16_f32 v10, v7, v7
s_nop 1
v_and_b32_dpp v14, v10, v3 row_newbcast:0 row_mask:0xf bank_mask:0xf
v_and_b32_dpp v15, v10, v4 row_newbcast:0 row_mask:0xf bank_mask:0xf
v_and_b32_dpp v16, v10, v5 row_newbcast:0 row_mask:0xf bank_mask:0xf
v_and_b32_dpp v17, v10, v6 row_newbcast:0 row_mask:0xf bank_mask:0xf
s_nop 1
v_mfma_f32_16x16x32_bf16 v[18:21], v[144:147], v[14:17], v[18:21]
v_and_b32_dpp v14, v10, v3 row_newbcast:4 row_mask:0xf bank_mask:0xf
v_and_b32_dpp v15, v10, v4 row_newbcast:4 row_mask:0xf bank_mask:0xf
v_and_b32_dpp v16, v10, v5 row_newbcast:4 row_mask:0xf bank_mask:0xf
v_and_b32_dpp v17, v10, v6 row_newbcast:4 row_mask:0xf bank_mask:0xf
s_nop 1
v_mfma_f32_16x16x32_bf16 v[18:21], v[148:151], v[14:17], v[18:21]
v_and_b32_dpp v14, v10, v3 row_newbcast:8 row_mask:0xf bank_mask:0xf
v_and_b32_dpp v15, v10, v4 row_newbcast:8 row_mask:0xf bank_mask:0xf
v_and_b32_dpp v16, v10, v5 row_newbcast:8 row_mask:0xf bank_mask:0xf
v_and_b32_dpp v17, v10, v6 row_newbcast:8 row_mask:0xf bank_mask:0xf
s_nop 1
v_mfma_f32_16x16x32_bf16 v[18:21], v[152:155], v[14:17], v[18:21]
v_and_b32_dpp v14, v10, v3 row_newbcast:12 row_mask:0xf bank_mask:0xf
v_and_b32_dpp v15, v10, v4 row_newbcast:12 row_mask:0xf bank_mask:0xf
v_and_b32_dpp v16, v10, v5 row_newbcast:12 row_mask:0xf bank_mask:0xf
v_and_b32_dpp v17, v10, v6 row_newbcast:12 row_mask:0xf bank_mask:0xf
s_nop 1
v_mfma_f32_16x16x32_bf16 v[18:21], v[156:159], v[14:17], v[18:21]
v_mfma_f32_16x16x32_bf16 v[14:17], v[160:163], v[22:25], 0
v_mfma_f32_16x16x32_bf16 v[14:17], v[164:167], v[26:29], v[14:17]
v_mfma_f32_16x16x32_bf16 v[14:17], v[168:171], v[30:33], v[14:17]
v_mfma_f32_16x16x32_bf16 v[14:17], v[172:175], v[34:37], v[14:17]
s_nop 7
v_mul_f32_dpp v14, v14, v2 quad_perm:[0,0,0,0] row_mask:0xf bank_mask:0xf
v_mul_f32_dpp v15, v15, v2 quad_perm:[1,1,1,1] row_mask:0xf bank_mask:0xf
v_mul_f32_dpp v16, v16, v2 quad_perm:[2,2,2,2] row_mask:0xf bank_mask:0xf
v_mul_f32_dpp v17, v17, v2 quad_perm:[3,3,3,3] row_mask:0xf bank_mask:0xf
v_add_f32 v14, v14, v15
v_add_f32 v16, v16, v17
v_add_f32 v14, v14, v8
v_add_f32 v7, v14, v16
v_cmp_lt_f32 vcc, 0, v7
s_cbranch_vccnz Lnc_36_%=
s_cmp_lg_u32 s81, 0
s_cbranch_scc1 Lnc_36_%=
Lns_35_%=:
v_exp_f32 v7, v7
s_nop 0
v_add_f32 v9, v9, v7
v_cvt_pk_bf16_f32 v10, v7, v7
s_nop 1
v_and_b32_dpp v14, v10, v3 row_newbcast:0 row_mask:0xf bank_mask:0xf
v_and_b32_dpp v15, v10, v4 row_newbcast:0 row_mask:0xf bank_mask:0xf
v_and_b32_dpp v16, v10, v5 row_newbcast:0 row_mask:0xf bank_mask:0xf
v_and_b32_dpp v17, v10, v6 row_newbcast:0 row_mask:0xf bank_mask:0xf
s_nop 1
v_mfma_f32_16x16x32_bf16 v[18:21], v[176:179], v[14:17], v[18:21]
v_and_b32_dpp v14, v10, v3 row_newbcast:4 row_mask:0xf bank_mask:0xf
v_and_b32_dpp v15, v10, v4 row_newbcast:4 row_mask:0xf bank_mask:0xf
v_and_b32_dpp v16, v10, v5 row_newbcast:4 row_mask:0xf bank_mask:0xf
v_and_b32_dpp v17, v10, v6 row_newbcast:4 row_mask:0xf bank_mask:0xf
s_nop 1
v_mfma_f32_16x16x32_bf16 v[18:21], v[180:183], v[14:17], v[18:21]
v_and_b32_dpp v14, v10, v3 row_newbcast:8 row_mask:0xf bank_mask:0xf
v_and_b32_dpp v15, v10, v4 row_newbcast:8 row_mask:0xf bank_mask:0xf
v_and_b32_dpp v16, v10, v5 row_newbcast:8 row_mask:0xf bank_mask:0xf
v_and_b32_dpp v17, v10, v6 row_newbcast:8 row_mask:0xf bank_mask:0xf
s_nop 1
v_mfma_f32_16x16x32_bf16 v[18:21], v[184:187], v[14:17], v[18:21]
v_and_b32_dpp v14, v10, v3 row_newbcast:12 row_mask:0xf bank_mask:0xf
v_and_b32_dpp v15, v10, v4 row_newbcast:12 row_mask:0xf bank_mask:0xf
v_and_b32_dpp v16, v10, v5 row_newbcast:12 row_mask:0xf bank_mask:0xf
v_and_b32_dpp v17, v10, v6 row_newbcast:12 row_mask:0xf bank_mask:0xf
s_nop 1
v_mfma_f32_16x16x32_bf16 v[18:21], v[188:191], v[14:17], v[18:21]
v_mfma_f32_16x16x32_bf16 v[14:17], v[192:195], v[22:25], 0
v_mfma_f32_16x16x32_bf16 v[14:17], v[196:199], v[26:29], v[14:17]
v_mfma_f32_16x16x32_bf16 v[14:17], v[200:203], v[30:33], v[14:17]
v_mfma_f32_16x16x32_bf16 v[14:17], v[204:207], v[34:37], v[14:17]
s_nop 7
v_mul_f32_dpp v14, v14, v2 quad_perm:[0,0,0,0] row_mask:0xf bank_mask:0xf
v_mul_f32_dpp v15, v15, v2 quad_perm:[1,1,1,1] row_mask:0xf bank_mask:0xf
v_mul_f32_dpp v16, v16, v2 quad_perm:[2,2,2,2] row_mask:0xf bank_mask:0xf
v_mul_f32_dpp v17, v17, v2 quad_perm:[3,3,3,3] row_mask:0xf bank_mask:0xf
v_add_f32 v14, v14, v15
v_add_f32 v16, v16, v17
v_add_f32 v14, v14, v8
v_add_f32 v7, v14, v16
v_cmp_lt_f32 vcc, 0, v7
s_cbranch_vccnz Lnc_38_%=
s_cmp_lg_u32 s81, 0
s_cbranch_scc1 Lnc_38_%=
Lns_37_%=:
v_exp_f32 v7, v7
s_nop 0
v_add_f32 v9, v9, v7
v_cvt_pk_bf16_f32 v10, v7, v7
s_nop 1
v_and_b32_dpp v14, v10, v3 row_newbcast:0 row_mask:0xf bank_mask:0xf
v_and_b32_dpp v15, v10, v4 row_newbcast:0 row_mask:0xf bank_mask:0xf
v_and_b32_dpp v16, v10, v5 row_newbcast:0 row_mask:0xf bank_mask:0xf
v_and_b32_dpp v17, v10, v6 row_newbcast:0 row_mask:0xf bank_mask:0xf
s_nop 1
v_mfma_f32_16x16x32_bf16 v[18:21], v[208:211], v[14:17], v[18:21]
v_and_b32_dpp v14, v10, v3 row_newbcast:4 row_mask:0xf bank_mask:0xf
v_and_b32_dpp v15, v10, v4 row_newbcast:4 row_mask:0xf bank_mask:0xf
v_and_b32_dpp v16, v10, v5 row_newbcast:4 row_mask:0xf bank_mask:0xf
v_and_b32_dpp v17, v10, v6 row_newbcast:4 row_mask:0xf bank_mask:0xf
s_nop 1
v_mfma_f32_16x16x32_bf16 v[18:21], v[212:215], v[14:17], v[18:21]
v_and_b32_dpp v14, v10, v3 row_newbcast:8 row_mask:0xf bank_mask:0xf
v_and_b32_dpp v15, v10, v4 row_newbcast:8 row_mask:0xf bank_mask:0xf
v_and_b32_dpp v16, v10, v5 row_newbcast:8 row_mask:0xf bank_mask:0xf
v_and_b32_dpp v17, v10, v6 row_newbcast:8 row_mask:0xf bank_mask:0xf
s_nop 1
v_mfma_f32_16x16x32_bf16 v[18:21], v[216:219], v[14:17], v[18:21]
v_and_b32_dpp v14, v10, v3 row_newbcast:12 row_mask:0xf bank_mask:0xf
v_and_b32_dpp v15, v10, v4 row_newbcast:12 row_mask:0xf bank_mask:0xf
v_and_b32_dpp v16, v10, v5 row_newbcast:12 row_mask:0xf bank_mask:0xf
v_and_b32_dpp v17, v10, v6 row_newbcast:12 row_mask:0xf bank_mask:0xf
s_nop 1
v_mfma_f32_16x16x32_bf16 v[18:21], v[220:223], v[14:17], v[18:21]
v_mfma_f32_16x16x32_bf16 v[14:17], v[224:227], v[22:25], 0
v_mfma_f32_16x16x32_bf16 v[14:17], v[228:231], v[26:29], v[14:17]
v_mfma_f32_16x16x32_bf16 v[14:17], v[232:235], v[30:33], v[14:17]
v_mfma_f32_16x16x32_bf16 v[14:17], v[236:239], v[34:37], v[14:17]
s_nop 7
v_mul_f32_dpp v14, v14, v2 quad_perm:[0,0,0,0] row_mask:0xf bank_mask:0xf
v_mul_f32_dpp v15, v15, v2 quad_perm:[1,1,1,1] row_mask:0xf bank_mask:0xf
v_mul_f32_dpp v16, v16, v2 quad_perm:[2,2,2,2] row_mask:0xf bank_mask:0xf
v_mul_f32_dpp v17, v17, v2 quad_perm:[3,3,3,3] row_mask:0xf bank_mask:0xf
v_add_f32 v14, v14, v15
v_add_f32 v16, v16, v17
v_add_f32 v14, v14, v8
v_add_f32 v7, v14, v16
v_cmp_lt_f32 vcc, 0, v7
s_cbranch_vccnz Lnc_40_%=
s_cmp_lg_u32 s81, 0
s_cbranch_scc1 Lnc_40_%=
Lns_39_%=:
v_exp_f32 v7, v7
s_nop 0
v_add_f32 v9, v9, v7
v_cvt_pk_bf16_f32 v10, v7, v7
s_waitcnt vmcnt(12)
s_nop 1
v_and_b32_dpp v14, v10, v3 row_newbcast:0 row_mask:0xf bank_mask:0xf
v_and_b32_dpp v15, v10, v4 row_newbcast:0 row_mask:0xf bank_mask:0xf
v_and_b32_dpp v16, v10, v5 row_newbcast:0 row_mask:0xf bank_mask:0xf
v_and_b32_dpp v17, v10, v6 row_newbcast:0 row_mask:0xf bank_mask:0xf
s_nop 1
v_mfma_f32_16x16x32_bf16 v[18:21], v[240:243], v[14:17], v[18:21]
v_and_b32_dpp v14, v10, v3 row_newbcast:4 row_mask:0xf bank_mask:0xf
v_and_b32_dpp v15, v10, v4 row_newbcast:4 row_mask:0xf bank_mask:0xf
v_and_b32_dpp v16, v10, v5 row_newbcast:4 row_mask:0xf bank_mask:0xf
v_and_b32_dpp v17, v10, v6 row_newbcast:4 row_mask:0xf bank_mask:0xf
s_nop 1
v_mfma_f32_16x16x32_bf16 v[18:21], v[244:247], v[14:17], v[18:21]
v_and_b32_dpp v14, v10, v3 row_newbcast:8 row_mask:0xf bank_mask:0xf
v_and_b32_dpp v15, v10, v4 row_newbcast:8 row_mask:0xf bank_mask:0xf
v_and_b32_dpp v16, v10, v5 row_newbcast:8 row_mask:0xf bank_mask:0xf
v_and_b32_dpp v17, v10, v6 row_newbcast:8 row_mask:0xf bank_mask:0xf
s_nop 1
v_mfma_f32_16x16x32_bf16 v[18:21], v[248:251], v[14:17], v[18:21]
v_and_b32_dpp v14, v10, v3 row_newbcast:12 row_mask:0xf bank_mask:0xf
v_and_b32_dpp v15, v10, v4 row_newbcast:12 row_mask:0xf bank_mask:0xf
v_and_b32_dpp v16, v10, v5 row_newbcast:12 row_mask:0xf bank_mask:0xf
v_and_b32_dpp v17, v10, v6 row_newbcast:12 row_mask:0xf bank_mask:0xf
s_nop 1
v_mfma_f32_16x16x32_bf16 v[18:21], v[252:255], v[14:17], v[18:21]
; ==== ring AB: streamed tiles of batch 0, then of batch 1, through a window of three 4 KiB halves (v48-v95); first layer, q of that batch
Lab_loop_%=:
s_waitcnt vmcnt(8)
s_cmp_lg_u32 s77, s80
s_cbranch_scc1 Losw_42_%=
Lnsw_41_%=:
v_mfma_f32_16x16x32_bf16 v[14:17], v[48:51], v[22:25], 0
v_mfma_f32_16x16x32_bf16 v[14:17], v[52:55], v[26:29], v[14:17]
v_mfma_f32_16x16x32_bf16 v[14:17], v[56:59], v[30:33], v[14:17]
v_mfma_f32_16x16x32_bf16 v[14:17], v[60:63], v[34:37], v[14:17]
s_cmp_eq_u32 s78, 1
s_cselect_b32 s60, s20, s12
s_cselect_b32 s61, s21, s13
s_mov_b32 s62, 0x400000
s_cmp_ge_u32 s71, 2359296
s_cbranch_scc1 Lpo_45_%=
buffer_load_dwordx4 v[48:51], v1, s[60:63], s71 offen offset:0 nt
buffer_load_dwordx4 v[52:55], v1, s[60:63], s71 offen offset:1024 nt
buffer_load_dwordx4 v[56:59], v1, s[60:63], s71 offen offset:2048 nt
buffer_load_dwordx4 v[60:63], v1, s[60:63], s71 offen offset:3072 nt
Lpn_44_%=:
s_cmp_lt_u32 s76, 464
s_cselect_b32 s79, 0, 1
s_cselect_b32 s50, 560, 40
s_add_u32 s50, s50, s76
s_lshl_b32 s72, s50, 12
s_cmp_lt_u32 s76, 984
s_cselect_b32 s79, s79, 2
s_add_u32 s76, s76, 28
s_cmp_lg_u32 s79, s78
s_cbranch_scc1 Lso_47_%=
Lsb_46_%=:
s_cmp_ge_u32 s76, 984
s_cbranch_scc1 Lso_50_%=
Lsb_49_%=:
v_mul_f32_dpp v14, v14, v2 quad_perm:[0,0,0,0] row_mask:0xf bank_mask:0xf
v_mul_f32_dpp v15, v15, v2 quad_perm:[1,1,1,1] row_mask:0xf bank_mask:0xf
v_mul_f32_dpp v16, v16, v2 quad_perm:[2,2,2,2] row_mask:0xf bank_mask:0xf
v_mul_f32_dpp v17, v17, v2 quad_perm:[3,3,3,3] row_mask:0xf bank_mask:0xf
v_add_f32 v14, v14, v15
v_add_f32 v16, v16, v17
v_add_f32 v14, v14, v8
v_add_f32 v7, v14, v16
v_cmp_lt_f32 vcc, 0, v7
s_cbranch_vccnz Lnc_53_%=
s_cmp_lg_u32 s81, 0
s_cbranch_scc1 Lnc_53_%=
Lns_52_%=:
v_exp_f32 v7, v7
s_nop 0
v_add_f32 v9, v9, v7
v_cvt_pk_bf16_f32 v10, v7, v7
s_waitcnt vmcnt(8)
s_nop 1
v_and_b32_dpp v14, v10, v3 row_newbcast:0 row_mask:0xf bank_mask:0xf
v_and_b32_dpp v15, v10, v4 row_newbcast:0 row_mask:0xf bank_mask:0xf
v_and_b32_dpp v16, v10, v5 row_newbcast:0 row_mask:0xf bank_mask:0xf
v_and_b32_dpp v17, v10, v6 row_newbcast:0 row_mask:0xf bank_mask:0xf
s_nop 1
v_mfma_f32_16x16x32_bf16 v[18:21], v[64:67], v[14:17], v[18:21]
v_and_b32_dpp v14, v10, v3 row_newbcast:4 row_mask:0xf bank_mask:0xf
v_and_b32_dpp v15, v10, v4 row_newbcast:4 row_mask:0xf bank_mask:0xf
v_and_b32_dpp v16, v10, v5 row_newbcast:4 row_mask:0xf bank_mask:0xf
v_and_b32_dpp v17, v10, v6 row_newbcast:4 row_mask:0xf bank_mask:0xf
s_nop 1
v_mfma_f32_16x16x32_bf16 v[18:21], v[68:71], v[14:17], v[18:21]
v_and_b32_dpp v14, v10, v3 row_newbcast:8 row_mask:0xf bank_mask:0xf
v_and_b32_dpp v15, v10, v4 row_newbcast:8 row_mask:0xf bank_mask:0xf
v_and_b32_dpp v16, v10, v5 row_newbcast:8 row_mask:0xf bank_mask:0xf
v_and_b32_dpp v17, v10, v6 row_newbcast:8 row_mask:0xf bank_mask:0xf
s_nop 1
v_mfma_f32_16x16x32_bf16 v[18:21], v[72:75], v[14:17], v[18:21]
v_and_b32_dpp v14, v10, v3 row_newbcast:12 row_mask:0xf bank_mask:0xf
v_and_b32_dpp v15, v10, v4 row_newbcast:12 row_mask:0xf bank_mask:0xf
v_and_b32_dpp v16, v10, v5 row_newbcast:12 row_mask:0xf bank_mask:0xf
v_and_b32_dpp v17, v10, v6 row_newbcast:12 row_mask:0xf bank_mask:0xf
s_nop 1
v_mfma_f32_16x16x32_bf16 v[18:21], v[76:79], v[14:17], v[18:21]
s_cmp_eq_u32 s79, 2
s_cbranch_scc1 Lab_x1_%=
s_cmp_eq_u32 s79, 1
s_cselect_b32 s56, s16, s8
s_cselect_b32 s57, s17, s9
s_mov_b32 s58, 0x400000
buffer_load_dwordx4 v[64:67], v0, s[56:59], s72 offen offset:0 nt
buffer_load_dwordx4 v[68:71], v0, s[56:59], s72 offen offset:1024 nt
buffer_load_dwordx4 v[72:75], v0, s[56:59], s72 offen offset:2048 nt
buffer_load_dwordx4 v[76:79], v0, s[56:59], s72 offen offset:3072 nt
s_waitcnt vmcnt(8)
s_cmp_lg_u32 s78, s80
s_cbranch_scc1 Losw_55_%=
Lnsw_54_%=:
v_mfma_f32_16x16x32_bf16 v[14:17], v[80:83], v[22:25], 0
v_mfma_f32_16x16x32_bf16 v[14:17], v[84:87], v[26:29], v[14:17]
v_mfma_f32_16x16x32_bf16 v[14:17], v[88:91], v[30:33], v[14:17]
v_mfma_f32_16x16x32_bf16 v[14:17], v[92:95], v[34:37], v[14:17]
s_cmp_eq_u32 s79, 1
s_cselect_b32 s60, s20, s12
s_cselect_b32 s61, s21, s13
s_mov_b32 s62, 0x400000
s_cmp_ge_u32 s72, 2359296
s_cbranch_scc1 Lpo_58_%=
buffer_load_dwordx4 v[80:83], v1, s[60:63], s72 offen offset:0 nt
buffer_load_dwordx4 v[84:87], v1, s[60:63], s72 offen offset:1024 nt
buffer_load_dwordx4 v[88:91], v1, s[60:63], s72 offen offset:2048 nt
buffer_load_dwordx4 v[92:95], v1, s[60:63], s72 offen offset:3072 nt
Lpn_57_%=:
s_cmp_lt_u32 s76, 464
s_cselect_b32 s77, 0, 1
s_cselect_b32 s50, 560, 40
s_add_u32 s50, s50, s76
s_lshl_b32 s70, s50, 12
s_cmp_lt_u32 s76, 984
s_cselect_b32 s77, s77, 2
s_add_u32 s76, s76, 28
s_cmp_lg_u32 s77, s79
s_cbranch_scc1 Lso_60_%=
Lsb_59_%=:
s_cmp_ge_u32 s76, 984
s_cbranch_scc1 Lso_63_%=
Lsb_62_%=:
v_mul_f32_dpp v14, v14, v2 quad_perm:[0,0,0,0] row_mask:0xf bank_mask:0xf
v_mul_f32_dpp v15, v15, v2 quad_perm:[1,1,1,1] row_mask:0xf bank_mask:0xf
v_mul_f32_dpp v16, v16, v2 quad_perm:[2,2,2,2] row_mask:0xf bank_mask:0xf
v_mul_f32_dpp v17, v17, v2 quad_perm:[3,3,3,3] row_mask:0xf bank_mask:0xf
v_add_f32 v14, v14, v15
v_add_f32 v16, v16, v17
v_add_f32 v14, v14, v8
v_add_f32 v7, v14, v16
v_cmp_lt_f32 vcc, 0, v7
s_cbranch_vccnz Lnc_66_%=
s_cmp_lg_u32 s81, 0
s_cbranch_scc1 Lnc_66_%=
Lns_65_%=:
v_exp_f32 v7, v7
s_nop 0
v_add_f32 v9, v9, v7
v_cvt_pk_bf16_f32 v10, v7, v7
s_waitcnt vmcnt(8)
s_nop 1
v_and_b32_dpp v14, v10, v3 row_newbcast:0 row_mask:0xf bank_mask:0xf
v_and_b32_dpp v15, v10, v4 row_newbcast:0 row_mask:0xf bank_mask:0xf
v_and_b32_dpp v16, v10, v5 row_newbcast:0 row_mask:0xf bank_mask:0xf
v_and_b32_dpp v17, v10, v6 row_newbcast:0 row_mask:0xf bank_mask:0xf
s_nop 1
v_mfma_f32_16x16x32_bf16 v[18:21], v[48:51], v[14:17], v[18:21]
v_and_b32_dpp v14, v10, v3 row_newbcast:4 row_mask:0xf bank_mask:0xf
v_and_b32_dpp v15, v10, v4 row_newbcast:4 row_mask:0xf bank_mask:0xf
v_and_b32_dpp v16, v10, v5 row_newbcast:4 row_mask:0xf bank_mask:0xf
v_and_b32_dpp v17, v10, v6 row_newbcast:4 row_mask:0xf bank_mask:0xf
s_nop 1
v_mfma_f32_16x16x32_bf16 v[18:21], v[52:55], v[14:17], v[18:21]
v_and_b32_dpp v14, v10, v3 row_newbcast:8 row_mask:0xf bank_mask:0xf
v_and_b32_dpp v15, v10, v4 row_newbcast:8 row_mask:0xf bank_mask:0xf
v_and_b32_dpp v16, v10, v5 row_newbcast:8 row_mask:0xf bank_mask:0xf
v_and_b32_dpp v17, v10, v6 row_newbcast:8 row_mask:0xf bank_mask:0xf
s_nop 1
v_mfma_f32_16x16x32_bf16 v[18:21], v[56:59], v[14:17], v[18:21]
v_and_b32_dpp v14, v10, v3 row_newbcast:12 row_mask:0xf bank_mask:0xf
v_and_b32_dpp v15, v10, v4 row_newbcast:12 row_mask:0xf bank_mask:0xf
v_and_b32_dpp v16, v10, v5 row_newbcast:12 row_mask:0xf bank_mask:0xf
v_and_b32_dpp v17, v10, v6 row_newbcast:12 row_mask:0xf bank_mask:0xf
s_nop 1
v_mfma_f32_16x16x32_bf16 v[18:21], v[60:63], v[14:17], v[18:21]
s_cmp_eq_u32 s77, 2
s_cbranch_scc1 Lab_x2_%=
s_cmp_eq_u32 s77, 1
s_cselect_b32 s56, s16, s8
s_cselect_b32 s57, s17, s9
s_mov_b32 s58, 0x400000
buffer_load_dwordx4 v[48:51], v0, s[56:59], s70 offen offset:0 nt
buffer_load_dwordx4 v[52:55], v0, s[56:59], s70 offen offset:1024 nt
buffer_load_dwordx4 v[56:59], v0, s[56:59], s70 offen offset:2048 nt
buffer_load_dwordx4 v[60:63], v0, s[56:59], s70 offen offset:3072 nt
s_waitcnt vmcnt(8)
s_cmp_lg_u32 s79, s80
s_cbranch_scc1 Losw_68_%=
Lnsw_67_%=:
v_mfma_f32_16x16x32_bf16 v[14:17], v[64:67], v[22:25], 0
v_mfma_f32_16x16x32_bf16 v[14:17], v[68:71], v[26:29], v[14:17]
v_mfma_f32_16x16x32_bf16 v[14:17], v[72:75], v[30:33], v[14:17]
v_mfma_f32_16x16x32_bf16 v[14:17], v[76:79], v[34:37], v[14:17]
s_cmp_eq_u32 s77, 1
s_cselect_b32 s60, s20, s12
s_cselect_b32 s61, s21, s13
s_mov_b32 s62, 0x400000
s_cmp_ge_u32 s70, 2359296
s_cbranch_scc1 Lpo_71_%=
buffer_load_dwordx4 v[64:67], v1, s[60:63], s70 offen offset:0 nt
buffer_load_dwordx4 v[68:71], v1, s[60:63], s70 offen offset:1024 nt
buffer_load_dwordx4 v[72:75], v1, s[60:63], s70 offen offset:2048 nt
buffer_load_dwordx4 v[76:79], v1, s[60:63], s70 offen offset:3072 nt
Lpn_70_%=:
s_cmp_lt_u32 s76, 464
s_cselect_b32 s78, 0, 1
s_cselect_b32 s50, 560, 40
s_add_u32 s50, s50, s76
s_lshl_b32 s71, s50, 12
s_cmp_lt_u32 s76, 984
s_cselect_b32 s78, s78, 2
s_add_u32 s76, s76, 28
s_cmp_lg_u32 s78, s77
s_cbranch_scc1 Lso_73_%=
Lsb_72_%=:
s_cmp_ge_u32 s76, 984
s_cbranch_scc1 Lso_76_%=
Lsb_75_%=:
v_mul_f32_dpp v14, v14, v2 quad_perm:[0,0,0,0] row_mask:0xf bank_mask:0xf
v_mul_f32_dpp v15, v15, v2 quad_perm:[1,1,1,1] row_mask:0xf bank_mask:0xf
v_mul_f32_dpp v16, v16, v2 quad_perm:[2,2,2,2] row_mask:0xf bank_mask:0xf
v_mul_f32_dpp v17, v17, v2 quad_perm:[3,3,3,3] row_mask:0xf bank_mask:0xf
v_add_f32 v14, v14, v15
v_add_f32 v16, v16, v17
v_add_f32 v14, v14, v8
v_add_f32 v7, v14, v16
v_cmp_lt_f32 vcc, 0, v7
s_cbranch_vccnz Lnc_79_%=
s_cmp_lg_u32 s81, 0
s_cbranch_scc1 Lnc_79_%=
Lns_78_%=:
v_exp_f32 v7, v7
s_nop 0
v_add_f32 v9, v9, v7
v_cvt_pk_bf16_f32 v10, v7, v7
s_waitcnt vmcnt(8)
s_nop 1
v_and_b32_dpp v14, v10, v3 row_newbcast:0 row_mask:0xf bank_mask:0xf
v_and_b32_dpp v15, v10, v4 row_newbcast:0 row_mask:0xf bank_mask:0xf
v_and_b32_dpp v16, v10, v5 row_newbcast:0 row_mask:0xf bank_mask:0xf
v_and_b32_dpp v17, v10, v6 row_newbcast:0 row_mask:0xf bank_mask:0xf
s_nop 1
v_mfma_f32_16x16x32_bf16 v[18:21], v[80:83], v[14:17], v[18:21]
v_and_b32_dpp v14, v10, v3 row_newbcast:4 row_mask:0xf bank_mask:0xf
v_and_b32_dpp v15, v10, v4 row_newbcast:4 row_mask:0xf bank_mask:0xf
v_and_b32_dpp v16, v10, v5 row_newbcast:4 row_mask:0xf bank_mask:0xf
v_and_b32_dpp v17, v10, v6 row_newbcast:4 row_mask:0xf bank_mask:0xf
s_nop 1
v_mfma_f32_16x16x32_bf16 v[18:21], v[84:87], v[14:17], v[18:21]
v_and_b32_dpp v14, v10, v3 row_newbcast:8 row_mask:0xf bank_mask:0xf
v_and_b32_dpp v15, v10, v4 row_newbcast:8 row_mask:0xf bank_mask:0xf
v_and_b32_dpp v16, v10, v5 row_newbcast:8 row_mask:0xf bank_mask:0xf
v_and_b32_dpp v17, v10, v6 row_newbcast:8 row_mask:0xf bank_mask:0xf
s_nop 1
v_mfma_f32_16x16x32_bf16 v[18:21], v[88:91], v[14:17], v[18:21]
v_and_b32_dpp v14, v10, v3 row_newbcast:12 row_mask:0xf bank_mask:0xf
v_and_b32_dpp v15, v10, v4 row_newbcast:12 row_mask:0xf bank_mask:0xf
v_and_b32_dpp v16, v10, v5 row_newbcast:12 row_mask:0xf bank_mask:0xf
v_and_b32_dpp v17, v10, v6 row_newbcast:12 row_mask:0xf bank_mask:0xf
s_nop 1
v_mfma_f32_16x16x32_bf16 v[18:21], v[92:95], v[14:17], v[18:21]
s_cmp_eq_u32 s78, 2
s_cbranch_scc1 Lab_x0_%=
s_cmp_eq_u32 s78, 1
s_cselect_b32 s56, s16, s8
s_cselect_b32 s57, s17, s9
s_mov_b32 s58, 0x400000
buffer_load_dwordx4 v[80:83], v0, s[56:59], s71 offen offset:0 nt
buffer_load_dwordx4 v[84:87], v0, s[56:59], s71 offen offset:1024 nt
buffer_load_dwordx4 v[88:91], v0, s[56:59], s71 offen offset:2048 nt
buffer_load_dwordx4 v[92:95], v0, s[56:59], s71 offen offset:3072 nt
s_branch Lab_loop_%=
Lab_x0_%=:
s_mov_b32 s83, s77
s_getpc_b64 s[88:89]
Lpc_80_%=:
s_add_u32 s88, s88, LPEND0_%=-Lpc_80_%=
s_addc_u32 s89, s89, 0
s_branch Lab_exit_%=
Lab_x1_%=:
s_mov_b32 s83, s78
s_getpc_b64 s[88:89]
Lpc_81_%=:
s_add_u32 s88, s88, LPEND1_%=-Lpc_81_%=
s_addc_u32 s89, s89, 0
s_branch Lab_exit_%=
Lab_x2_%=:
s_mov_b32 s83, s79
s_getpc_b64 s[88:89]
Lpc_82_%=:
s_add_u32 s88, s88, LPEND2_%=-Lpc_82_%=
s_addc_u32 s89, s89, 0
s_branch Lab_exit_%=
Lab_exit_%=:
s_waitcnt vmcnt(8)
; ==== B2: retained tiles of batch 0 meet its second-layer q; each freed slot is refilled with a tile of batch 1 (first layer), processed a few slots later
s_add_u32 s31, s30, 3
v_cmp_eq_u32_e64 s[50:51], s31, v41
v_cmp_eq_u32_e64 s[52:53], s31, v43
s_and_b64 s[50:51], s[50:51], s[52:53]
v_cmp_eq_u32_e64 s[52:53], s31, v45
s_and_b64 s[50:51], s[50:51], s[52:53]
v_cmp_eq_u32_e64 s[52:53], s31, v47
s_and_b64 s[50:51], s[50:51], s[52:53]
s_cmp_eq_u64 s[50:51], -1
s_cbranch_scc1 Lqok_83_%=
s_mov_b32 s48, 0
Lqrt_84_%=:
s_sleep 8
v_and_b32 v12, 0xff, v0
v_lshlrev_b32 v12, 1, v12
s_lshl_b32 s50, s49, 9
s_add_u32 s50, s50, 36864
v_add_u32 v12, s50, v12
buffer_load_dwordx4 v[40:43], v12, s[24:27], 0 offen sc1
buffer_load_dwordx4 v[44:47], v12, s[24:27], 0 offen offset:16 sc1
s_waitcnt vmcnt(0)
v_cmp_eq_u32_e64 s[50:51], s31, v41
v_cmp_eq_u32_e64 s[52:53], s31, v43
s_and_b64 s[50:51], s[50:51], s[52:53]
v_cmp_eq_u32_e64 s[52:53], s31, v45
s_and_b64 s[50:51], s[50:51], s[52:53]
v_cmp_eq_u32_e64 s[52:53], s31, v47
s_and_b64 s[50:51], s[50:51], s[52:53]
s_cmp_eq_u64 s[50:51], -1
s_cbranch_scc1 Lqok_83_%=
s_add_u32 s48, s48, 1
s_cmp_lt_u32 s48, 0x8000
s_cbranch_scc1 Lqrt_84_%=
s_branch LEND_%=
Lqok_83_%=:
v_mov_b32 v7, v40
v_mov_b32 v10, v42
v_mov_b32 v12, v44
v_mov_b32 v13, v46
s_nop 7
v_mov_b32 v40, v18
v_mov_b32 v41, v19
v_mov_b32 v42, v20
v_mov_b32 v43, v21
v_mov_b32 v44, v9
v_mov_b32 v45, v8
s_mov_b32 s92, s81
v_or3_b32 v38, v22, v26, v30
v_or_b32 v38, v38, v34
v_or3_b32 v39, v23, v27, v31
v_or_b32 v39, v39, v35
v_or3_b32 v46, v24, v28, v32
v_or_b32 v46, v46, v36
v_or3_b32 v47, v25, v29, v33
v_or_b32 v47, v47, v37
v_cndmask_b32_e64 v22, 0, v7, s[36:37]
v_cndmask_b32_e64 v23, 0, v10, s[36:37]
v_cndmask_b32_e64 v24, 0, v12, s[36:37]
v_cndmask_b32_e64 v25, 0, v13, s[36:37]
v_cndmask_b32_e64 v26, 0, v7, s[38:39]
v_cndmask_b32_e64 v27, 0, v10, s[38:39]
v_cndmask_b32_e64 v28, 0, v12, s[38:39]
v_cndmask_b32_e64 v29, 0, v13, s[38:39]
v_cndmask_b32_e64 v30, 0, v7, s[40:41]
v_cndmask_b32_e64 v31, 0, v10, s[40:41]
v_cndmask_b32_e64 v32, 0, v12, s[40:41]
v_cndmask_b32_e64 v33, 0, v13, s[40:41]
v_cndmask_b32_e64 v34, 0, v7, s[42:43]
v_cndmask_b32_e64 v35, 0, v10, s[42:43]
v_cndmask_b32_e64 v36, 0, v12, s[42:43]
v_cndmask_b32_e64 v37, 0, v13, s[42:43]
s_nop 1
s_nop 7
v_mov_b32 v18, 0
v_mov_b32 v19, 0
v_mov_b32 v20, 0
v_mov_b32 v21, 0
v_mov_b32 v9, 0
v_mov_b32 v8, 0
s_mov_b32 s81, 1
s_nop 1
v_mfma_f32_16x16x32_bf16 v[14:17], a[0:3], v[22:25], 0
v_mfma_f32_16x16x32_bf16 v[14:17], a[4:7], v[26:29], v[14:17]
v_mfma_f32_16x16x32_bf16 v[14:17], a[8:11], v[30:33], v[14:17]
v_mfma_f32_16x16x32_bf16 v[14:17], a[12:15], v[34:37], v[14:17]
s_nop 7
v_mul_f32_dpp v14, v14, v2 quad_perm:[0,0,0,0] row_mask:0xf bank_mask:0xf
v_mul_f32_dpp v15, v15, v2 quad_perm:[1,1,1,1] row_mask:0xf bank_mask:0xf
v_mul_f32_dpp v16, v16, v2 quad_perm:[2,2,2,2] row_mask:0xf bank_mask:0xf
v_mul_f32_dpp v17, v17, v2 quad_perm:[3,3,3,3] row_mask:0xf bank_mask:0xf
v_add_f32 v14, v14, v15
v_add_f32 v16, v16, v17
v_add_f32 v14, v14, v8
v_add_f32 v7, v14, v16
v_cmp_lt_f32 vcc, 0, v7
s_cbranch_vccnz Lnc_86_%=
s_cmp_lg_u32 s81, 0
s_cbranch_scc1 Lnc_86_%=
Lns_85_%=:
v_exp_f32 v7, v7
s_nop 0
v_add_f32 v9, v9, v7
v_cvt_pk_bf16_f32 v10, v7, v7
s_nop 1
v_and_b32_dpp v14, v10, v3 row_newbcast:0 row_mask:0xf bank_mask:0xf
v_and_b32_dpp v15, v10, v4 row_newbcast:0 row_mask:0xf bank_mask:0xf
v_and_b32_dpp v16, v10, v5 row_newbcast:0 row_mask:0xf bank_mask:0xf
v_and_b32_dpp v17, v10, v6 row_newbcast:0 row_mask:0xf bank_mask:0xf
s_nop 1
v_mfma_f32_16x16x32_bf16 v[18:21], a[16:19], v[14:17], v[18:21]
v_and_b32_dpp v14, v10, v3 row_newbcast:4 row_mask:0xf bank_mask:0xf
v_and_b32_dpp v15, v10, v4 row_newbcast:4 row_mask:0xf bank_mask:0xf
v_and_b32_dpp v16, v10, v5 row_newbcast:4 row_mask:0xf bank_mask:0xf
v_and_b32_dpp v17, v10, v6 row_newbcast:4 row_mask:0xf bank_mask:0xf
s_nop 1
v_mfma_f32_16x16x32_bf16 v[18:21], a[20:23], v[14:17], v[18:21]
v_and_b32_dpp v14, v10, v3 row_newbcast:8 row_mask:0xf bank_mask:0xf
v_and_b32_dpp v15, v10, v4 row_newbcast:8 row_mask:0xf bank_mask:0xf
v_and_b32_dpp v16, v10, v5 row_newbcast:8 row_mask:0xf bank_mask:0xf
v_and_b32_dpp v17, v10, v6 row_newbcast:8 row_mask:0xf bank_mask:0xf
s_nop 1
v_mfma_f32_16x16x32_bf16 v[18:21], a[24:27], v[14:17], v[18:21]
v_and_b32_dpp v14, v10, v3 row_newbcast:12 row_mask:0xf bank_mask:0xf
v_and_b32_dpp v15, v10, v4 row_newbcast:12 row_mask:0xf bank_mask:0xf
v_and_b32_dpp v16, v10, v5 row_newbcast:12 row_mask:0xf bank_mask:0xf
v_and_b32_dpp v17, v10, v6 row_newbcast:12 row_mask:0xf bank_mask:0xf
s_nop 1
v_mfma_f32_16x16x32_bf16 v[18:21], a[28:31], v[14:17], v[18:21]
ds_read_b128 a[0:3], v11 offset:0
ds_read_b128 a[4:7], v11 offset:1024
ds_read_b128 a[8:11], v11 offset:2048
ds_read_b128 a[12:15], v11 offset:3072
ds_read_b128 a[16:19], v11 offset:4096
ds_read_b128 a[20:23], v11 offset:5120
ds_read_b128 a[24:27], v11 offset:6144
ds_read_b128 a[28:31], v11 offset:7168
s_waitcnt lgkmcnt(0)
s_add_u32 s29, s28, 0
s_add_u32 m0, s45, 0
s_nop 0
buffer_load_dwordx4 v0, s[16:19], s29 offen offset:0 nt lds
buffer_load_dwordx4 v0, s[16:19], s29 offen offset:1024 nt lds
buffer_load_dwordx4 v0, s[16:19], s29 offen offset:2048 nt lds
buffer_load_dwordx4 v0, s[16:19], s29 offen offset:3072 nt lds
s_add_u32 s29, s28, 0
s_add_u32 m0, s45, 4096
s_nop 0
buffer_load_dwordx4 v1, s[20:23], s29 offen offset:0 nt lds
buffer_load_dwordx4 v1, s[20:23], s29 offen offset:1024 nt lds
buffer_load_dwordx4 v1, s[20:23], s29 offen offset:2048 nt lds
buffer_load_dwordx4 v1, s[20:23], s29 offen offset:3072 nt lds
v_mfma_f32_16x16x32_bf16 v[14:17], a[0:3], v[22:25], 0
v_mfma_f32_16x16x32_bf16 v[14:17], a[4:7], v[26:29], v[14:17]
v_mfma_f32_16x16x32_bf16 v[14:17], a[8:11], v[30:33], v[14:17]
v_mfma_f32_16x16x32_bf16 v[14:17], a[12:15], v[34:37], v[14:17]
s_nop 7
v_mul_f32_dpp v14, v14, v2 quad_perm:[0,0,0,0] row_mask:0xf bank_mask:0xf
v_mul_f32_dpp v15, v15, v2 quad_perm:[1,1,1,1] row_mask:0xf bank_mask:0xf
v_mul_f32_dpp v16, v16, v2 quad_perm:[2,2,2,2] row_mask:0xf bank_mask:0xf
v_mul_f32_dpp v17, v17, v2 quad_perm:[3,3,3,3] row_mask:0xf bank_mask:0xf
v_add_f32 v14, v14, v15
v_add_f32 v16, v16, v17
v_add_f32 v14, v14, v8
v_add_f32 v7, v14, v16
v_cmp_lt_f32 vcc, 0, v7
s_cbranch_vccnz Lnc_88_%=
s_cmp_lg_u32 s81, 0
s_cbranch_scc1 Lnc_88_%=
Lns_87_%=:
v_exp_f32 v7, v7
s_nop 0
v_add_f32 v9, v9, v7
v_cvt_pk_bf16_f32 v10, v7, v7
s_nop 1
v_and_b32_dpp v14, v10, v3 row_newbcast:0 row_mask:0xf bank_mask:0xf
v_and_b32_dpp v15, v10, v4 row_newbcast:0 row_mask:0xf bank_mask:0xf
v_and_b32_dpp v16, v10, v5 row_newbcast:0 row_mask:0xf bank_mask:0xf
v_and_b32_dpp v17, v10, v6 row_newbcast:0 row_mask:0xf bank_mask:0xf
s_nop 1
v_mfma_f32_16x16x32_bf16 v[18:21], a[16:19], v[14:17], v[18:21]
v_and_b32_dpp v14, v10, v3 row_newbcast:4 row_mask:0xf bank_mask:0xf
v_and_b32_dpp v15, v10, v4 row_newbcast:4 row_mask:0xf bank_mask:0xf
v_and_b32_dpp v16, v10, v5 row_newbcast:4 row_mask:0xf bank_mask:0xf
v_and_b32_dpp v17, v10, v6 row_newbcast:4 row_mask:0xf bank_mask:0xf
s_nop 1
v_mfma_f32_16x16x32_bf16 v[18:21], a[20:23], v[14:17], v[18:21]
v_and_b32_dpp v14, v10, v3 row_newbcast:8 row_mask:0xf bank_mask:0xf
v_and_b32_dpp v15, v10, v4 row_newbcast:8 row_mask:0xf bank_mask:0xf
v_and_b32_dpp v16, v10, v5 row_newbcast:8 row_mask:0xf bank_mask:0xf
v_and_b32_dpp v17, v10, v6 row_newbcast:8 row_mask:0xf bank_mask:0xf
s_nop 1
v_mfma_f32_16x16x32_bf16 v[18:21], a[24:27], v[14:17], v[18:21]
v_and_b32_dpp v14, v10, v3 row_newbcast:12 row_mask:0xf bank_mask:0xf
v_and_b32_dpp v15, v10, v4 row_newbcast:12 row_mask:0xf bank_mask:0xf
v_and_b32_dpp v16, v10, v5 row_newbcast:12 row_mask:0xf bank_mask:0xf
v_and_b32_dpp v17, v10, v6 row_newbcast:12 row_mask:0xf bank_mask:0xf
s_nop 1
v_mfma_f32_16x16x32_bf16 v[18:21], a[28:31], v[14:17], v[18:21]
ds_read_b128 a[0:3], v11 offset:8192
ds_read_b128 a[4:7], v11 offset:9216
ds_read_b128 a[8:11], v11 offset:10240
ds_read_b128 a[12:15], v11 offset:11264
ds_read_b128 a[16:19], v11 offset:12288
ds_read_b128 a[20:23], v11 offset:13312
ds_read_b128 a[24:27], v11 offset:14336
ds_read_b128 a[28:31], v11 offset:15360
s_waitcnt lgkmcnt(0)
s_add_u32 s29, s28, 114688
s_add_u32 m0, s45, 8192
s_nop 0
buffer_load_dwordx4 v0, s[16:19], s29 offen offset:0 nt lds
buffer_load_dwordx4 v0, s[16:19], s29 offen offset:1024 nt lds
buffer_load_dwordx4 v0, s[16:19], s29 offen offset:2048 nt lds
buffer_load_dwordx4 v0, s[16:19], s29 offen offset:3072 nt lds
s_add_u32 s29, s28, 114688
s_add_u32 m0, s45, 12288
s_nop 0
buffer_load_dwordx4 v1, s[20:23], s29 offen offset:0 nt lds
buffer_load_dwordx4 v1, s[20:23], s29 offen offset:1024 nt lds
buffer_load_dwordx4 v1, s[20:23], s29 offen offset:2048 nt lds
buffer_load_dwordx4 v1, s[20:23], s29 offen offset:3072 nt lds
v_mfma_f32_16x16x32_bf16 v[14:17], a[0:3], v[22:25], 0
v_mfma_f32_16x16x32_bf16 v[14:17], a[4:7], v[26:29], v[14:17]
v_mfma_f32_16x16x32_bf16 v[14:17], a[8:11], v[30:33], v[14:17]
v_mfma_f32_16x16x32_bf16 v[14:17], a[12:15], v[34:37], v[14:17]
s_nop 7
v_mul_f32_dpp v14, v14, v2 quad_perm:[0,0,0,0] row_mask:0xf bank_mask:0xf
v_mul_f32_dpp v15, v15, v2 quad_perm:[1,1,1,1] row_mask:0xf bank_mask:0xf
v_mul_f32_dpp v16, v16, v2 quad_perm:[2,2,2,2] row_mask:0xf bank_mask:0xf
v_mul_f32_dpp v17, v17, v2 quad_perm:[3,3,3,3] row_mask:0xf bank_mask:0xf
v_add_f32 v14, v14, v15
v_add_f32 v16, v16, v17
v_add_f32 v14, v14, v8
v_add_f32 v7, v14, v16
v_cmp_lt_f32 vcc, 0, v7
s_cbranch_vccnz Lnc_90_%=
s_cmp_lg_u32 s81, 0
s_cbranch_scc1 Lnc_90_%=
Lns_89_%=:
v_exp_f32 v7, v7
s_nop 0
v_add_f32 v9, v9, v7
v_cvt_pk_bf16_f32 v10, v7, v7
s_nop 1
v_and_b32_dpp v14, v10, v3 row_newbcast:0 row_mask:0xf bank_mask:0xf
v_and_b32_dpp v15, v10, v4 row_newbcast:0 row_mask:0xf bank_mask:0xf
v_and_b32_dpp v16, v10, v5 row_newbcast:0 row_mask:0xf bank_mask:0xf
v_and_b32_dpp v17, v10, v6 row_newbcast:0 row_mask:0xf bank_mask:0xf
s_nop 1
v_mfma_f32_16x16x32_bf16 v[18:21], a[16:19], v[14:17], v[18:21]
v_and_b32_dpp v14, v10, v3 row_newbcast:4 row_mask:0xf bank_mask:0xf
v_and_b32_dpp v15, v10, v4 row_newbcast:4 row_mask:0xf bank_mask:0xf
v_and_b32_dpp v16, v10, v5 row_newbcast:4 row_mask:0xf bank_mask:0xf
v_and_b32_dpp v17, v10, v6 row_newbcast:4 row_mask:0xf bank_mask:0xf
s_nop 1
v_mfma_f32_16x16x32_bf16 v[18:21], a[20:23], v[14:17], v[18:21]
v_and_b32_dpp v14, v10, v3 row_newbcast:8 row_mask:0xf bank_mask:0xf
v_and_b32_dpp v15, v10, v4 row_newbcast:8 row_mask:0xf bank_mask:0xf
v_and_b32_dpp v16, v10, v5 row_newbcast:8 row_mask:0xf bank_mask:0xf
v_and_b32_dpp v17, v10, v6 row_newbcast:8 row_mask:0xf bank_mask:0xf
s_nop 1
v_mfma_f32_16x16x32_bf16 v[18:21], a[24:27], v[14:17], v[18:21]
v_and_b32_dpp v14, v10, v3 row_newbcast:12 row_mask:0xf bank_mask:0xf
v_and_b32_dpp v15, v10, v4 row_newbcast:12 row_mask:0xf bank_mask:0xf
v_and_b32_dpp v16, v10, v5 row_newbcast:12 row_mask:0xf bank_mask:0xf
v_and_b32_dpp v17, v10, v6 row_newbcast:12 row_mask:0xf bank_mask:0xf
s_nop 1
v_mfma_f32_16x16x32_bf16 v[18:21], a[28:31], v[14:17], v[18:21]
ds_read_b128 a[0:3], v11 offset:16384
ds_read_b128 a[4:7], v11 offset:17408
ds_read_b128 a[8:11], v11 offset:18432
ds_read_b128 a[12:15], v11 offset:19456
ds_read_b128 a[16:19], v11 offset:20480
ds_read_b128 a[20:23], v11 offset:21504
ds_read_b128 a[24:27], v11 offset:22528
ds_read_b128 a[28:31], v11 offset:23552
s_waitcnt lgkmcnt(0)
s_add_u32 s29, s28, 229376
s_add_u32 m0, s45, 16384
s_nop 0
buffer_load_dwordx4 v0, s[16:19], s29 offen offset:0 nt lds
buffer_load_dwordx4 v0, s[16:19], s29 offen offset:1024 nt lds
buffer_load_dwordx4 v0, s[16:19], s29 offen offset:2048 nt lds
buffer_load_dwordx4 v0, s[16:19], s29 offen offset:3072 nt lds
s_add_u32 s29, s28, 229376
s_add_u32 m0, s45, 20480
s_nop 0
buffer_load_dwordx4 v1, s[20:23], s29 offen offset:0 nt lds
buffer_load_dwordx4 v1, s[20:23], s29 offen offset:1024 nt lds
buffer_load_dwordx4 v1, s[20:23], s29 offen offset:2048 nt lds
buffer_load_dwordx4 v1, s[20:23], s29 offen offset:3072 nt lds
v_mfma_f32_16x16x32_bf16 v[14:17], a[0:3], v[22:25], 0
v_mfma_f32_16x16x32_bf16 v[14:17], a[4:7], v[26:29], v[14:17]
v_mfma_f32_16x16x32_bf16 v[14:17], a[8:11], v[30:33], v[14:17]
v_mfma_f32_16x16x32_bf16 v[14:17], a[12:15], v[34:37], v[14:17]
s_nop 7
v_mul_f32_dpp v14, v14, v2 quad_perm:[0,0,0,0] row_mask:0xf bank_mask:0xf
v_mul_f32_dpp v15, v15, v2 quad_perm:[1,1,1,1] row_mask:0xf bank_mask:0xf
v_mul_f32_dpp v16, v16, v2 quad_perm:[2,2,2,2] row_mask:0xf bank_mask:0xf
v_mul_f32_dpp v17, v17, v2 quad_perm:[3,3,3,3] row_mask:0xf bank_mask:0xf
v_add_f32 v14, v14, v15
v_add_f32 v16, v16, v17
v_add_f32 v14, v14, v8
v_add_f32 v7, v14, v16
v_cmp_lt_f32 vcc, 0, v7
s_cbranch_vccnz Lnc_92_%=
s_cmp_lg_u32 s81, 0
s_cbranch_scc1 Lnc_92_%=
Lns_91_%=:
v_exp_f32 v7, v7
s_nop 0
v_add_f32 v9, v9, v7
v_cvt_pk_bf16_f32 v10, v7, v7
s_nop 1
v_and_b32_dpp v14, v10, v3 row_newbcast:0 row_mask:0xf bank_mask:0xf
v_and_b32_dpp v15, v10, v4 row_newbcast:0 row_mask:0xf bank_mask:0xf
v_and_b32_dpp v16, v10, v5 row_newbcast:0 row_mask:0xf bank_mask:0xf
v_and_b32_dpp v17, v10, v6 row_newbcast:0 row_mask:0xf bank_mask:0xf
s_nop 1
v_mfma_f32_16x16x32_bf16 v[18:21], a[16:19], v[14:17], v[18:21]
v_and_b32_dpp v14, v10, v3 row_newbcast:4 row_mask:0xf bank_mask:0xf
v_and_b32_dpp v15, v10, v4 row_newbcast:4 row_mask:0xf bank_mask:0xf
v_and_b32_dpp v16, v10, v5 row_newbcast:4 row_mask:0xf bank_mask:0xf
v_and_b32_dpp v17, v10, v6 row_newbcast:4 row_mask:0xf bank_mask:0xf
s_nop 1
v_mfma_f32_16x16x32_bf16 v[18:21], a[20:23], v[14:17], v[18:21]
v_and_b32_dpp v14, v10, v3 row_newbcast:8 row_mask:0xf bank_mask:0xf
v_and_b32_dpp v15, v10, v4 row_newbcast:8 row_mask:0xf bank_mask:0xf
v_and_b32_dpp v16, v10, v5 row_newbcast:8 row_mask:0xf bank_mask:0xf
v_and_b32_dpp v17, v10, v6 row_newbcast:8 row_mask:0xf bank_mask:0xf
s_nop 1
v_mfma_f32_16x16x32_bf16 v[18:21], a[24:27], v[14:17], v[18:21]
v_and_b32_dpp v14, v10, v3 row_newbcast:12 row_mask:0xf bank_mask:0xf
v_and_b32_dpp v15, v10, v4 row_newbcast:12 row_mask:0xf bank_mask:0xf
v_and_b32_dpp v16, v10, v5 row_newbcast:12 row_mask:0xf bank_mask:0xf
v_and_b32_dpp v17, v10, v6 row_newbcast:12 row_mask:0xf bank_mask:0xf
s_nop 1
v_mfma_f32_16x16x32_bf16 v[18:21], a[28:31], v[14:17], v[18:21]
ds_read_b128 a[0:3], v11 offset:24576
ds_read_b128 a[4:7], v11 offset:25600
ds_read_b128 a[8:11], v11 offset:26624
ds_read_b128 a[12:15], v11 offset:27648
ds_read_b128 a[16:19], v11 offset:28672
ds_read_b128 a[20:23], v11 offset:29696
ds_read_b128 a[24:27], v11 offset:30720
ds_read_b128 a[28:31], v11 offset:31744
s_waitcnt lgkmcnt(0)
s_waitcnt vmcnt(28)
s_add_u32 s29, s28, 344064
s_add_u32 m0, s45, 24576
s_nop 0
buffer_load_dwordx4 v0, s[16:19], s29 offen offset:0 nt lds
buffer_load_dwordx4 v0, s[16:19], s29 offen offset:1024 nt lds
buffer_load_dwordx4 v0, s[16:19], s29 offen offset:2048 nt lds
buffer_load_dwordx4 v0, s[16:19], s29 offen offset:3072 nt lds
s_waitcnt vmcnt(28)
s_add_u32 s29, s28, 344064
s_add_u32 m0, s45, 28672
s_nop 0
buffer_load_dwordx4 v1, s[20:23], s29 offen offset:0 nt lds
buffer_load_dwordx4 v1, s[20:23], s29 offen offset:1024 nt lds
buffer_load_dwordx4 v1, s[20:23], s29 offen offset:2048 nt lds
buffer_load_dwordx4 v1, s[20:23], s29 offen offset:3072 nt lds
v_mfma_f32_16x16x32_bf16 v[14:17], a[0:3], v[22:25], 0
v_mfma_f32_16x16x32_bf16 v[14:17], a[4:7], v[26:29], v[14:17]
v_mfma_f32_16x16x32_bf16 v[14:17], a[8:11], v[30:33], v[14:17]
v_mfma_f32_16x16x32_bf16 v[14:17], a[12:15], v[34:37], v[14:17]
s_nop 7
v_mul_f32_dpp v14, v14, v2 quad_perm:[0,0,0,0] row_mask:0xf bank_mask:0xf
v_mul_f32_dpp v15, v15, v2 quad_perm:[1,1,1,1] row_mask:0xf bank_mask:0xf
v_mul_f32_dpp v16, v16, v2 quad_perm:[2,2,2,2] row_mask:0xf bank_mask:0xf
v_mul_f32_dpp v17, v17, v2 quad_perm:[3,3,3,3] row_mask:0xf bank_mask:0xf
v_add_f32 v14, v14, v15
v_add_f32 v16, v16, v17
v_add_f32 v14, v14, v8
v_add_f32 v7, v14, v16
v_cmp_lt_f32 vcc, 0, v7
s_cbranch_vccnz Lnc_94_%=
s_cmp_lg_u32 s81, 0
s_cbranch_scc1 Lnc_94_%=
Lns_93_%=:
v_exp_f32 v7, v7
s_nop 0
v_add_f32 v9, v9, v7
v_cvt_pk_bf16_f32 v10, v7, v7
s_nop 1
v_and_b32_dpp v14, v10, v3 row_newbcast:0 row_mask:0xf bank_mask:0xf
v_and_b32_dpp v15, v10, v4 row_newbcast:0 row_mask:0xf bank_mask:0xf
v_and_b32_dpp v16, v10, v5 row_newbcast:0 row_mask:0xf bank_mask:0xf
v_and_b32_dpp v17, v10, v6 row_newbcast:0 row_mask:0xf bank_mask:0xf
s_nop 1
v_mfma_f32_16x16x32_bf16 v[18:21], a[16:19], v[14:17], v[18:21]
v_and_b32_dpp v14, v10, v3 row_newbcast:4 row_mask:0xf bank_mask:0xf
v_and_b32_dpp v15, v10, v4 row_newbcast:4 row_mask:0xf bank_mask:0xf
v_and_b32_dpp v16, v10, v5 row_newbcast:4 row_mask:0xf bank_mask:0xf
v_and_b32_dpp v17, v10, v6 row_newbcast:4 row_mask:0xf bank_mask:0xf
s_nop 1
v_mfma_f32_16x16x32_bf16 v[18:21], a[20:23], v[14:17], v[18:21]
v_and_b32_dpp v14, v10, v3 row_newbcast:8 row_mask:0xf bank_mask:0xf
v_and_b32_dpp v15, v10, v4 row_newbcast:8 row_mask:0xf bank_mask:0xf
v_and_b32_dpp v16, v10, v5 row_newbcast:8 row_mask:0xf bank_mask:0xf
v_and_b32_dpp v17, v10, v6 row_newbcast:8 row_mask:0xf bank_mask:0xf
s_nop 1
v_mfma_f32_16x16x32_bf16 v[18:21], a[24:27], v[14:17], v[18:21]
v_and_b32_dpp v14, v10, v3 row_newbcast:12 row_mask:0xf bank_mask:0xf
v_and_b32_dpp v15, v10, v4 row_newbcast:12 row_mask:0xf bank_mask:0xf
v_and_b32_dpp v16, v10, v5 row_newbcast:12 row_mask:0xf bank_mask:0xf
v_and_b32_dpp v17, v10, v6 row_newbcast:12 row_mask:0xf bank_mask:0xf
s_nop 1
v_mfma_f32_16x16x32_bf16 v[18:21], a[28:31], v[14:17], v[18:21]
ds_read_b128 a[0:3], v11 offset:32768
ds_read_b128 a[4:7], v11 offset:33792
ds_read_b128 a[8:11], v11 offset:34816
ds_read_b128 a[12:15], v11 offset:35840
ds_read_b128 a[16:19], v11 offset:36864
ds_read_b128 a[20:23], v11 offset:37888
ds_read_b128 a[24:27], v11 offset:38912
ds_read_b128 a[28:31], v11 offset:39936
s_waitcnt lgkmcnt(0)
s_waitcnt vmcnt(28)
s_add_u32 s29, s28, 458752
s_add_u32 m0, s45, 32768
s_nop 0
buffer_load_dwordx4 v0, s[16:19], s29 offen offset:0 nt lds
buffer_load_dwordx4 v0, s[16:19], s29 offen offset:1024 nt lds
buffer_load_dwordx4 v0, s[16:19], s29 offen offset:2048 nt lds
buffer_load_dwordx4 v0, s[16:19], s29 offen offset:3072 nt lds
s_waitcnt vmcnt(28)
s_add_u32 s29, s28, 458752
s_add_u32 m0, s45, 36864
s_nop 0
buffer_load_dwordx4 v1, s[20:23], s29 offen offset:0 nt lds
buffer_load_dwordx4 v1, s[20:23], s29 offen offset:1024 nt lds
buffer_load_dwordx4 v1, s[20:23], s29 offen offset:2048 nt lds
buffer_load_dwordx4 v1, s[20:23], s29 offen offset:3072 nt lds
v_mfma_f32_16x16x32_bf16 v[14:17], a[0:3], v[22:25], 0
v_mfma_f32_16x16x32_bf16 v[14:17], a[4:7], v[26:29], v[14:17]
v_mfma_f32_16x16x32_bf16 v[14:17], a[8:11], v[30:33], v[14:17]
v_mfma_f32_16x16x32_bf16 v[14:17], a[12:15], v[34:37], v[14:17]
s_nop 7
v_mul_f32_dpp v14, v14, v2 quad_perm:[0,0,0,0] row_mask:0xf bank_mask:0xf
v_mul_f32_dpp v15, v15, v2 quad_perm:[1,1,1,1] row_mask:0xf bank_mask:0xf
v_mul_f32_dpp v16, v16, v2 quad_perm:[2,2,2,2] row_mask:0xf bank_mask:0xf
v_mul_f32_dpp v17, v17, v2 quad_perm:[3,3,3,3] row_mask:0xf bank_mask:0xf
v_add_f32 v14, v14, v15
v_add_f32 v16, v16, v17
v_add_f32 v14, v14, v8
v_add_f32 v7, v14, v16
v_cmp_lt_f32 vcc, 0, v7
s_cbranch_vccnz Lnc_96_%=
s_cmp_lg_u32 s81, 0
s_cbranch_scc1 Lnc_96_%=
Lns_95_%=:
v_exp_f32 v7, v7
s_nop 0
v_add_f32 v9, v9, v7
v_cvt_pk_bf16_f32 v10, v7, v7
s_nop 1
v_and_b32_dpp v14, v10, v3 row_newbcast:0 row_mask:0xf bank_mask:0xf
v_and_b32_dpp v15, v10, v4 row_newbcast:0 row_mask:0xf bank_mask:0xf
v_and_b32_dpp v16, v10, v5 row_newbcast:0 row_mask:0xf bank_mask:0xf
v_and_b32_dpp v17, v10, v6 row_newbcast:0 row_mask:0xf bank_mask:0xf
s_nop 1
v_mfma_f32_16x16x32_bf16 v[18:21], a[16:19], v[14:17], v[18:21]
v_and_b32_dpp v14, v10, v3 row_newbcast:4 row_mask:0xf bank_mask:0xf
v_and_b32_dpp v15, v10, v4 row_newbcast:4 row_mask:0xf bank_mask:0xf
v_and_b32_dpp v16, v10, v5 row_newbcast:4 row_mask:0xf bank_mask:0xf
v_and_b32_dpp v17, v10, v6 row_newbcast:4 row_mask:0xf bank_mask:0xf
s_nop 1
v_mfma_f32_16x16x32_bf16 v[18:21], a[20:23], v[14:17], v[18:21]
v_and_b32_dpp v14, v10, v3 row_newbcast:8 row_mask:0xf bank_mask:0xf
v_and_b32_dpp v15, v10, v4 row_newbcast:8 row_mask:0xf bank_mask:0xf
v_and_b32_dpp v16, v10, v5 row_newbcast:8 row_mask:0xf bank_mask:0xf
v_and_b32_dpp v17, v10, v6 row_newbcast:8 row_mask:0xf bank_mask:0xf
s_nop 1
v_mfma_f32_16x16x32_bf16 v[18:21], a[24:27], v[14:17], v[18:21]
v_and_b32_dpp v14, v10, v3 row_newbcast:12 row_mask:0xf bank_mask:0xf
v_and_b32_dpp v15, v10, v4 row_newbcast:12 row_mask:0xf bank_mask:0xf
v_and_b32_dpp v16, v10, v5 row_newbcast:12 row_mask:0xf bank_mask:0xf
v_and_b32_dpp v17, v10, v6 row_newbcast:12 row_mask:0xf bank_mask:0xf
s_nop 1
v_mfma_f32_16x16x32_bf16 v[18:21], a[28:31], v[14:17], v[18:21]
s_waitcnt vmcnt(28)
s_add_u32 s29, s28, 573440
buffer_load_dwordx4 a[0:3], v0, s[16:19], s29 offen offset:0 nt
buffer_load_dwordx4 a[4:7], v0, s[16:19], s29 offen offset:1024 nt
buffer_load_dwordx4 a[8:11], v0, s[16:19], s29 offen offset:2048 nt
buffer_load_dwordx4 a[12:15], v0, s[16:19], s29 offen offset:3072 nt
s_waitcnt vmcnt(28)
s_add_u32 s29, s28, 573440
buffer_load_dwordx4 a[16:19], v1, s[20:23], s29 offen offset:0 nt
buffer_load_dwordx4 a[20:23], v1, s[20:23], s29 offen offset:1024 nt
buffer_load_dwordx4 a[24:27], v1, s[20:23], s29 offen offset:2048 nt
buffer_load_dwordx4 a[28:31], v1, s[20:23], s29 offen offset:3072 nt
s_nop 7
v_swap_b32 v40, v18
v_swap_b32 v41, v19
v_swap_b32 v42, v20
v_swap_b32 v43, v21
v_swap_b32 v44, v9
v_swap_b32 v45, v8
s_mov_b32 s50, s81
s_mov_b32 s81, s92
s_mov_b32 s92, s50
v_or3_b32 v14, v22, v26, v30
v_or_b32 v14, v14, v34
v_or3_b32 v15, v23, v27, v31
v_or_b32 v15, v15, v35
v_or3_b32 v16, v24, v28, v32
v_or_b32 v16, v16, v36
v_or3_b32 v17, v25, v29, v33
v_or_b32 v17, v17, v37
v_cndmask_b32_e64 v22, 0, v38, s[36:37]
v_cndmask_b32_e64 v23, 0, v39, s[36:37]
v_cndmask_b32_e64 v24, 0, v46, s[36:37]
v_cndmask_b32_e64 v25, 0, v47, s[36:37]
v_cndmask_b32_e64 v26, 0, v38, s[38:39]
v_cndmask_b32_e64 v27, 0, v39, s[38:39]
v_cndmask_b32_e64 v28, 0, v46, s[38:39]
v_cndmask_b32_e64 v29, 0, v47, s[38:39]
v_cndmask_b32_e64 v30, 0, v38, s[40:41]
v_cndmask_b32_e64 v31, 0, v39, s[40:41]
v_cndmask_b32_e64 v32, 0, v46, s[40:41]
v_cndmask_b32_e64 v33, 0, v47, s[40:41]
v_cndmask_b32_e64 v34, 0, v38, s[42:43]
v_cndmask_b32_e64 v35, 0, v39, s[42:43]
v_cndmask_b32_e64 v36, 0, v46, s[42:43]
v_cndmask_b32_e64 v37, 0, v47, s[42:43]
s_nop 1
v_mov_b32 v38, v14
v_mov_b32 v39, v15
v_mov_b32 v46, v16
v_mov_b32 v47, v17
s_cmp_eq_u32 s83, 2
s_cbranch_scc1 Lpq_97_%=
s_swappc_b64 s[86:87], s[88:89]
Lpq_97_%=:
ds_read_b128 v[48:51], v11 offset:0
ds_read_b128 v[52:55], v11 offset:1024
ds_read_b128 v[56:59], v11 offset:2048
ds_read_b128 v[60:63], v11 offset:3072
ds_read_b128 v[64:67], v11 offset:4096
ds_read_b128 v[68:71], v11 offset:5120
ds_read_b128 v[72:75], v11 offset:6144
ds_read_b128 v[76:79], v11 offset:7168
s_waitcnt lgkmcnt(0)
v_mfma_f32_16x16x32_bf16 v[14:17], v[48:51], v[22:25], 0
v_mfma_f32_16x16x32_bf16 v[14:17], v[52:55], v[26:29], v[14:17]
v_mfma_f32_16x16x32_bf16 v[14:17], v[56:59], v[30:33], v[14:17]
v_mfma_f32_16x16x32_bf16 v[14:17], v[60:63], v[34:37], v[14:17]
s_nop 7
v_mul_f32_dpp v14, v14, v2 quad_perm:[0,0,0,0] row_mask:0xf bank_mask:0xf
v_mul_f32_dpp v15, v15, v2 quad_perm:[1,1,1,1] row_mask:0xf bank_mask:0xf
v_mul_f32_dpp v16, v16, v2 quad_perm:[2,2,2,2] row_mask:0xf bank_mask:0xf
v_mul_f32_dpp v17, v17, v2 quad_perm:[3,3,3,3] row_mask:0xf bank_mask:0xf
v_add_f32 v14, v14, v15
v_add_f32 v16, v16, v17
v_add_f32 v14, v14, v8
v_add_f32 v7, v14, v16
v_cmp_lt_f32 vcc, 0, v7
s_cbranch_vccnz Lnc_99_%=
s_cmp_lg_u32 s81, 0
s_cbranch_scc1 Lnc_99_%=
Lns_98_%=:
v_exp_f32 v7, v7
s_nop 0
v_add_f32 v9, v9, v7
v_cvt_pk_bf16_f32 v10, v7, v7
s_nop 1
v_and_b32_dpp v14, v10, v3 row_newbcast:0 row_mask:0xf bank_mask:0xf
v_and_b32_dpp v15, v10, v4 row_newbcast:0 row_mask:0xf bank_mask:0xf
v_and_b32_dpp v16, v10, v5 row_newbcast:0 row_mask:0xf bank_mask:0xf
v_and_b32_dpp v17, v10, v6 row_newbcast:0 row_mask:0xf bank_mask:0xf
s_nop 1
v_mfma_f32_16x16x32_bf16 v[18:21], v[64:67], v[14:17], v[18:21]
v_and_b32_dpp v14, v10, v3 row_newbcast:4 row_mask:0xf bank_mask:0xf
v_and_b32_dpp v15, v10, v4 row_newbcast:4 row_mask:0xf bank_mask:0xf
v_and_b32_dpp v16, v10, v5 row_newbcast:4 row_mask:0xf bank_mask:0xf
v_and_b32_dpp v17, v10, v6 row_newbcast:4 row_mask:0xf bank_mask:0xf
s_nop 1
v_mfma_f32_16x16x32_bf16 v[18:21], v[68:71], v[14:17], v[18:21]
v_and_b32_dpp v14, v10, v3 row_newbcast:8 row_mask:0xf bank_mask:0xf
v_and_b32_dpp v15, v10, v4 row_newbcast:8 row_mask:0xf bank_mask:0xf
v_and_b32_dpp v16, v10, v5 row_newbcast:8 row_mask:0xf bank_mask:0xf
v_and_b32_dpp v17, v10, v6 row_newbcast:8 row_mask:0xf bank_mask:0xf
s_nop 1
v_mfma_f32_16x16x32_bf16 v[18:21], v[72:75], v[14:17], v[18:21]
v_and_b32_dpp v14, v10, v3 row_newbcast:12 row_mask:0xf bank_mask:0xf
v_and_b32_dpp v15, v10, v4 row_newbcast:12 row_mask:0xf bank_mask:0xf
v_and_b32_dpp v16, v10, v5 row_newbcast:12 row_mask:0xf bank_mask:0xf
v_and_b32_dpp v17, v10, v6 row_newbcast:12 row_mask:0xf bank_mask:0xf
s_nop 1
v_mfma_f32_16x16x32_bf16 v[18:21], v[76:79], v[14:17], v[18:21]
s_nop 7
v_swap_b32 v40, v18
v_swap_b32 v41, v19
v_swap_b32 v42, v20
v_swap_b32 v43, v21
v_swap_b32 v44, v9
v_swap_b32 v45, v8
s_mov_b32 s50, s81
s_mov_b32 s81, s92
s_mov_b32 s92, s50
v_or3_b32 v14, v22, v26, v30
v_or_b32 v14, v14, v34
v_or3_b32 v15, v23, v27, v31
v_or_b32 v15, v15, v35
v_or3_b32 v16, v24, v28, v32
v_or_b32 v16, v16, v36
v_or3_b32 v17, v25, v29, v33
v_or_b32 v17, v17, v37
v_cndmask_b32_e64 v22, 0, v38, s[36:37]
v_cndmask_b32_e64 v23, 0, v39, s[36:37]
v_cndmask_b32_e64 v24, 0, v46, s[36:37]
v_cndmask_b32_e64 v25, 0, v47, s[36:37]
v_cndmask_b32_e64 v26, 0, v38, s[38:39]
v_cndmask_b32_e64 v27, 0, v39, s[38:39]
v_cndmask_b32_e64 v28, 0, v46, s[38:39]
v_cndmask_b32_e64 v29, 0, v47, s[38:39]
v_cndmask_b32_e64 v30, 0, v38, s[40:41]
v_cndmask_b32_e64 v31, 0, v39, s[40:41]
v_cndmask_b32_e64 v32, 0, v46, s[40:41]
v_cndmask_b32_e64 v33, 0, v47, s[40:41]
v_cndmask_b32_e64 v34, 0, v38, s[42:43]
v_cndmask_b32_e64 v35, 0, v39, s[42:43]
v_cndmask_b32_e64 v36, 0, v46, s[42:43]
v_cndmask_b32_e64 v37, 0, v47, s[42:43]
s_nop 1
v_mov_b32 v38, v14
v_mov_b32 v39, v15
v_mov_b32 v46, v16
v_mov_b32 v47, v17
v_mfma_f32_16x16x32_bf16 v[14:17], a[32:35], v[22:25], 0
v_mfma_f32_16x16x32_bf16 v[14:17], a[36:39], v[26:29], v[14:17]
v_mfma_f32_16x16x32_bf16 v[14:17], a[40:43], v[30:33], v[14:17]
v_mfma_f32_16x16x32_bf16 v[14:17], a[44:47], v[34:37], v[14:17]
s_waitcnt vmcnt(28)
s_add_u32 s29, s28, 688128
buffer_load_dwordx4 a[32:35], v0, s[16:19], s29 offen offset:0 nt
buffer_load_dwordx4 a[36:39], v0, s[16:19], s29 offen offset:1024 nt
buffer_load_dwordx4 a[40:43], v0, s[16:19], s29 offen offset:2048 nt
buffer_load_dwordx4 a[44:47], v0, s[16:19], s29 offen offset:3072 nt
s_nop 1
v_mul_f32_dpp v14, v14, v2 quad_perm:[0,0,0,0] row_mask:0xf bank_mask:0xf
v_mul_f32_dpp v15, v15, v2 quad_perm:[1,1,1,1] row_mask:0xf bank_mask:0xf
v_mul_f32_dpp v16, v16, v2 quad_perm:[2,2,2,2] row_mask:0xf bank_mask:0xf
v_mul_f32_dpp v17, v17, v2 quad_perm:[3,3,3,3] row_mask:0xf bank_mask:0xf
v_add_f32 v14, v14, v15
v_add_f32 v16, v16, v17
v_add_f32 v14, v14, v8
v_add_f32 v7, v14, v16
v_cmp_lt_f32 vcc, 0, v7
s_cbranch_vccnz Lnc_101_%=
s_cmp_lg_u32 s81, 0
s_cbranch_scc1 Lnc_101_%=
Lns_100_%=:
v_exp_f32 v7, v7
s_nop 0
v_add_f32 v9, v9, v7
v_cvt_pk_bf16_f32 v10, v7, v7
s_nop 1
v_and_b32_dpp v14, v10, v3 row_newbcast:0 row_mask:0xf bank_mask:0xf
v_and_b32_dpp v15, v10, v4 row_newbcast:0 row_mask:0xf bank_mask:0xf
v_and_b32_dpp v16, v10, v5 row_newbcast:0 row_mask:0xf bank_mask:0xf
v_and_b32_dpp v17, v10, v6 row_newbcast:0 row_mask:0xf bank_mask:0xf
s_nop 1
v_mfma_f32_16x16x32_bf16 v[18:21], a[48:51], v[14:17], v[18:21]
v_and_b32_dpp v14, v10, v3 row_newbcast:4 row_mask:0xf bank_mask:0xf
v_and_b32_dpp v15, v10, v4 row_newbcast:4 row_mask:0xf bank_mask:0xf
v_and_b32_dpp v16, v10, v5 row_newbcast:4 row_mask:0xf bank_mask:0xf
v_and_b32_dpp v17, v10, v6 row_newbcast:4 row_mask:0xf bank_mask:0xf
s_nop 1
v_mfma_f32_16x16x32_bf16 v[18:21], a[52:55], v[14:17], v[18:21]
v_and_b32_dpp v14, v10, v3 row_newbcast:8 row_mask:0xf bank_mask:0xf
v_and_b32_dpp v15, v10, v4 row_newbcast:8 row_mask:0xf bank_mask:0xf
v_and_b32_dpp v16, v10, v5 row_newbcast:8 row_mask:0xf bank_mask:0xf
v_and_b32_dpp v17, v10, v6 row_newbcast:8 row_mask:0xf bank_mask:0xf
s_nop 1
v_mfma_f32_16x16x32_bf16 v[18:21], a[56:59], v[14:17], v[18:21]
v_and_b32_dpp v14, v10, v3 row_newbcast:12 row_mask:0xf bank_mask:0xf
v_and_b32_dpp v15, v10, v4 row_newbcast:12 row_mask:0xf bank_mask:0xf
v_and_b32_dpp v16, v10, v5 row_newbcast:12 row_mask:0xf bank_mask:0xf
v_and_b32_dpp v17, v10, v6 row_newbcast:12 row_mask:0xf bank_mask:0xf
s_nop 1
v_mfma_f32_16x16x32_bf16 v[18:21], a[60:63], v[14:17], v[18:21]
s_waitcnt vmcnt(28)
s_add_u32 s29, s28, 688128
buffer_load_dwordx4 a[48:51], v1, s[20:23], s29 offen offset:0 nt
buffer_load_dwordx4 a[52:55], v1, s[20:23], s29 offen offset:1024 nt
buffer_load_dwordx4 a[56:59], v1, s[20:23], s29 offen offset:2048 nt
buffer_load_dwordx4 a[60:63], v1, s[20:23], s29 offen offset:3072 nt
v_mfma_f32_16x16x32_bf16 v[14:17], a[64:67], v[22:25], 0
v_mfma_f32_16x16x32_bf16 v[14:17], a[68:71], v[26:29], v[14:17]
v_mfma_f32_16x16x32_bf16 v[14:17], a[72:75], v[30:33], v[14:17]
v_mfma_f32_16x16x32_bf16 v[14:17], a[76:79], v[34:37], v[14:17]
s_waitcnt vmcnt(28)
s_add_u32 s29, s28, 802816
buffer_load_dwordx4 a[64:67], v0, s[16:19], s29 offen offset:0 nt
buffer_load_dwordx4 a[68:71], v0, s[16:19], s29 offen offset:1024 nt
buffer_load_dwordx4 a[72:75], v0, s[16:19], s29 offen offset:2048 nt
buffer_load_dwordx4 a[76:79], v0, s[16:19], s29 offen offset:3072 nt
s_nop 1
v_mul_f32_dpp v14, v14, v2 quad_perm:[0,0,0,0] row_mask:0xf bank_mask:0xf
v_mul_f32_dpp v15, v15, v2 quad_perm:[1,1,1,1] row_mask:0xf bank_mask:0xf
v_mul_f32_dpp v16, v16, v2 quad_perm:[2,2,2,2] row_mask:0xf bank_mask:0xf
v_mul_f32_dpp v17, v17, v2 quad_perm:[3,3,3,3] row_mask:0xf bank_mask:0xf
v_add_f32 v14, v14, v15
v_add_f32 v16, v16, v17
v_add_f32 v14, v14, v8
v_add_f32 v7, v14, v16
v_cmp_lt_f32 vcc, 0, v7
s_cbranch_vccnz Lnc_103_%=
s_cmp_lg_u32 s81, 0
s_cbranch_scc1 Lnc_103_%=
Lns_102_%=:
v_exp_f32 v7, v7
s_nop 0
v_add_f32 v9, v9, v7
v_cvt_pk_bf16_f32 v10, v7, v7
s_nop 1
v_and_b32_dpp v14, v10, v3 row_newbcast:0 row_mask:0xf bank_mask:0xf
v_and_b32_dpp v15, v10, v4 row_newbcast:0 row_mask:0xf bank_mask:0xf
v_and_b32_dpp v16, v10, v5 row_newbcast:0 row_mask:0xf bank_mask:0xf
v_and_b32_dpp v17, v10, v6 row_newbcast:0 row_mask:0xf bank_mask:0xf
s_nop 1
v_mfma_f32_16x16x32_bf16 v[18:21], a[80:83], v[14:17], v[18:21]
v_and_b32_dpp v14, v10, v3 row_newbcast:4 row_mask:0xf bank_mask:0xf
v_and_b32_dpp v15, v10, v4 row_newbcast:4 row_mask:0xf bank_mask:0xf
v_and_b32_dpp v16, v10, v5 row_newbcast:4 row_mask:0xf bank_mask:0xf
v_and_b32_dpp v17, v10, v6 row_newbcast:4 row_mask:0xf bank_mask:0xf
s_nop 1
v_mfma_f32_16x16x32_bf16 v[18:21], a[84:87], v[14:17], v[18:21]
v_and_b32_dpp v14, v10, v3 row_newbcast:8 row_mask:0xf bank_mask:0xf
v_and_b32_dpp v15, v10, v4 row_newbcast:8 row_mask:0xf bank_mask:0xf
v_and_b32_dpp v16, v10, v5 row_newbcast:8 row_mask:0xf bank_mask:0xf
v_and_b32_dpp v17, v10, v6 row_newbcast:8 row_mask:0xf bank_mask:0xf
s_nop 1
v_mfma_f32_16x16x32_bf16 v[18:21], a[88:91], v[14:17], v[18:21]
v_and_b32_dpp v14, v10, v3 row_newbcast:12 row_mask:0xf bank_mask:0xf
v_and_b32_dpp v15, v10, v4 row_newbcast:12 row_mask:0xf bank_mask:0xf
v_and_b32_dpp v16, v10, v5 row_newbcast:12 row_mask:0xf bank_mask:0xf
v_and_b32_dpp v17, v10, v6 row_newbcast:12 row_mask:0xf bank_mask:0xf
s_nop 1
v_mfma_f32_16x16x32_bf16 v[18:21], a[92:95], v[14:17], v[18:21]
s_waitcnt vmcnt(28)
s_add_u32 s29, s28, 802816
buffer_load_dwordx4 a[80:83], v1, s[20:23], s29 offen offset:0 nt
buffer_load_dwordx4 a[84:87], v1, s[20:23], s29 offen offset:1024 nt
buffer_load_dwordx4 a[88:91], v1, s[20:23], s29 offen offset:2048 nt
buffer_load_dwordx4 a[92:95], v1, s[20:23], s29 offen offset:3072 nt
s_nop 7
v_swap_b32 v40, v18
v_swap_b32 v41, v19
v_swap_b32 v42, v20
v_swap_b32 v43, v21
v_swap_b32 v44, v9
v_swap_b32 v45, v8
s_mov_b32 s50, s81
s_mov_b32 s81, s92
s_mov_b32 s92, s50
v_or3_b32 v14, v22, v26, v30
v_or_b32 v14, v14, v34
v_or3_b32 v15, v23, v27, v31
v_or_b32 v15, v15, v35
v_or3_b32 v16, v24, v28, v32
v_or_b32 v16, v16, v36
v_or3_b32 v17, v25, v29, v33
v_or_b32 v17, v17, v37
v_cndmask_b32_e64 v22, 0, v38, s[36:37]
v_cndmask_b32_e64 v23, 0, v39, s[36:37]
v_cndmask_b32_e64 v24, 0, v46, s[36:37]
v_cndmask_b32_e64 v25, 0, v47, s[36:37]
v_cndmask_b32_e64 v26, 0, v38, s[38:39]
v_cndmask_b32_e64 v27, 0, v39, s[38:39]
v_cndmask_b32_e64 v28, 0, v46, s[38:39]
v_cndmask_b32_e64 v29, 0, v47, s[38:39]
v_cndmask_b32_e64 v30, 0, v38, s[40:41]
v_cndmask_b32_e64 v31, 0, v39, s[40:41]
v_cndmask_b32_e64 v32, 0, v46, s[40:41]
v_cndmask_b32_e64 v33, 0, v47, s[40:41]
v_cndmask_b32_e64 v34, 0, v38, s[42:43]
v_cndmask_b32_e64 v35, 0, v39, s[42:43]
v_cndmask_b32_e64 v36, 0, v46, s[42:43]
v_cndmask_b32_e64 v37, 0, v47, s[42:43]
s_nop 1
v_mov_b32 v38, v14
v_mov_b32 v39, v15
v_mov_b32 v46, v16
v_mov_b32 v47, v17
ds_read_b128 v[48:51], v11 offset:8192
ds_read_b128 v[52:55], v11 offset:9216
ds_read_b128 v[56:59], v11 offset:10240
ds_read_b128 v[60:63], v11 offset:11264
ds_read_b128 v[64:67], v11 offset:12288
ds_read_b128 v[68:71], v11 offset:13312
ds_read_b128 v[72:75], v11 offset:14336
ds_read_b128 v[76:79], v11 offset:15360
s_waitcnt lgkmcnt(0)
v_mfma_f32_16x16x32_bf16 v[14:17], v[48:51], v[22:25], 0
v_mfma_f32_16x16x32_bf16 v[14:17], v[52:55], v[26:29], v[14:17]
v_mfma_f32_16x16x32_bf16 v[14:17], v[56:59], v[30:33], v[14:17]
v_mfma_f32_16x16x32_bf16 v[14:17], v[60:63], v[34:37], v[14:17]
s_nop 7
v_mul_f32_dpp v14, v14, v2 quad_perm:[0,0,0,0] row_mask:0xf bank_mask:0xf
v_mul_f32_dpp v15, v15, v2 quad_perm:[1,1,1,1] row_mask:0xf bank_mask:0xf
v_mul_f32_dpp v16, v16, v2 quad_perm:[2,2,2,2] row_mask:0xf bank_mask:0xf
v_mul_f32_dpp v17, v17, v2 quad_perm:[3,3,3,3] row_mask:0xf bank_mask:0xf
v_add_f32 v14, v14, v15
v_add_f32 v16, v16, v17
v_add_f32 v14, v14, v8
v_add_f32 v7, v14, v16
v_cmp_lt_f32 vcc, 0, v7
s_cbranch_vccnz Lnc_105_%=
s_cmp_lg_u32 s81, 0
s_cbranch_scc1 Lnc_105_%=
Lns_104_%=:
v_exp_f32 v7, v7
s_nop 0
v_add_f32 v9, v9, v7
v_cvt_pk_bf16_f32 v10, v7, v7
s_nop 1
v_and_b32_dpp v14, v10, v3 row_newbcast:0 row_mask:0xf bank_mask:0xf
v_and_b32_dpp v15, v10, v4 row_newbcast:0 row_mask:0xf bank_mask:0xf
v_and_b32_dpp v16, v10, v5 row_newbcast:0 row_mask:0xf bank_mask:0xf
v_and_b32_dpp v17, v10, v6 row_newbcast:0 row_mask:0xf bank_mask:0xf
s_nop 1
v_mfma_f32_16x16x32_bf16 v[18:21], v[64:67], v[14:17], v[18:21]
v_and_b32_dpp v14, v10, v3 row_newbcast:4 row_mask:0xf bank_mask:0xf
v_and_b32_dpp v15, v10, v4 row_newbcast:4 row_mask:0xf bank_mask:0xf
v_and_b32_dpp v16, v10, v5 row_newbcast:4 row_mask:0xf bank_mask:0xf
v_and_b32_dpp v17, v10, v6 row_newbcast:4 row_mask:0xf bank_mask:0xf
s_nop 1
v_mfma_f32_16x16x32_bf16 v[18:21], v[68:71], v[14:17], v[18:21]
v_and_b32_dpp v14, v10, v3 row_newbcast:8 row_mask:0xf bank_mask:0xf
v_and_b32_dpp v15, v10, v4 row_newbcast:8 row_mask:0xf bank_mask:0xf
v_and_b32_dpp v16, v10, v5 row_newbcast:8 row_mask:0xf bank_mask:0xf
v_and_b32_dpp v17, v10, v6 row_newbcast:8 row_mask:0xf bank_mask:0xf
s_nop 1
v_mfma_f32_16x16x32_bf16 v[18:21], v[72:75], v[14:17], v[18:21]
v_and_b32_dpp v14, v10, v3 row_newbcast:12 row_mask:0xf bank_mask:0xf
v_and_b32_dpp v15, v10, v4 row_newbcast:12 row_mask:0xf bank_mask:0xf
v_and_b32_dpp v16, v10, v5 row_newbcast:12 row_mask:0xf bank_mask:0xf
v_and_b32_dpp v17, v10, v6 row_newbcast:12 row_mask:0xf bank_mask:0xf
s_nop 1
v_mfma_f32_16x16x32_bf16 v[18:21], v[76:79], v[14:17], v[18:21]
ds_read_b128 v[48:51], v11 offset:16384
ds_read_b128 v[52:55], v11 offset:17408
ds_read_b128 v[56:59], v11 offset:18432
ds_read_b128 v[60:63], v11 offset:19456
ds_read_b128 v[64:67], v11 offset:20480
ds_read_b128 v[68:71], v11 offset:21504
ds_read_b128 v[72:75], v11 offset:22528
ds_read_b128 v[76:79], v11 offset:23552
s_waitcnt lgkmcnt(0)
v_mfma_f32_16x16x32_bf16 v[14:17], v[48:51], v[22:25], 0
v_mfma_f32_16x16x32_bf16 v[14:17], v[52:55], v[26:29], v[14:17]
v_mfma_f32_16x16x32_bf16 v[14:17], v[56:59], v[30:33], v[14:17]
v_mfma_f32_16x16x32_bf16 v[14:17], v[60:63], v[34:37], v[14:17]
s_nop 7
v_mul_f32_dpp v14, v14, v2 quad_perm:[0,0,0,0] row_mask:0xf bank_mask:0xf
v_mul_f32_dpp v15, v15, v2 quad_perm:[1,1,1,1] row_mask:0xf bank_mask:0xf
v_mul_f32_dpp v16, v16, v2 quad_perm:[2,2,2,2] row_mask:0xf bank_mask:0xf
v_mul_f32_dpp v17, v17, v2 quad_perm:[3,3,3,3] row_mask:0xf bank_mask:0xf
v_add_f32 v14, v14, v15
v_add_f32 v16, v16, v17
v_add_f32 v14, v14, v8
v_add_f32 v7, v14, v16
v_cmp_lt_f32 vcc, 0, v7
s_cbranch_vccnz Lnc_107_%=
s_cmp_lg_u32 s81, 0
s_cbranch_scc1 Lnc_107_%=
Lns_106_%=:
v_exp_f32 v7, v7
s_nop 0
v_add_f32 v9, v9, v7
v_cvt_pk_bf16_f32 v10, v7, v7
s_nop 1
v_and_b32_dpp v14, v10, v3 row_newbcast:0 row_mask:0xf bank_mask:0xf
v_and_b32_dpp v15, v10, v4 row_newbcast:0 row_mask:0xf bank_mask:0xf
v_and_b32_dpp v16, v10, v5 row_newbcast:0 row_mask:0xf bank_mask:0xf
v_and_b32_dpp v17, v10, v6 row_newbcast:0 row_mask:0xf bank_mask:0xf
s_nop 1
v_mfma_f32_16x16x32_bf16 v[18:21], v[64:67], v[14:17], v[18:21]
v_and_b32_dpp v14, v10, v3 row_newbcast:4 row_mask:0xf bank_mask:0xf
v_and_b32_dpp v15, v10, v4 row_newbcast:4 row_mask:0xf bank_mask:0xf
v_and_b32_dpp v16, v10, v5 row_newbcast:4 row_mask:0xf bank_mask:0xf
v_and_b32_dpp v17, v10, v6 row_newbcast:4 row_mask:0xf bank_mask:0xf
s_nop 1
v_mfma_f32_16x16x32_bf16 v[18:21], v[68:71], v[14:17], v[18:21]
v_and_b32_dpp v14, v10, v3 row_newbcast:8 row_mask:0xf bank_mask:0xf
v_and_b32_dpp v15, v10, v4 row_newbcast:8 row_mask:0xf bank_mask:0xf
v_and_b32_dpp v16, v10, v5 row_newbcast:8 row_mask:0xf bank_mask:0xf
v_and_b32_dpp v17, v10, v6 row_newbcast:8 row_mask:0xf bank_mask:0xf
s_nop 1
v_mfma_f32_16x16x32_bf16 v[18:21], v[72:75], v[14:17], v[18:21]
v_and_b32_dpp v14, v10, v3 row_newbcast:12 row_mask:0xf bank_mask:0xf
v_and_b32_dpp v15, v10, v4 row_newbcast:12 row_mask:0xf bank_mask:0xf
v_and_b32_dpp v16, v10, v5 row_newbcast:12 row_mask:0xf bank_mask:0xf
v_and_b32_dpp v17, v10, v6 row_newbcast:12 row_mask:0xf bank_mask:0xf
s_nop 1
v_mfma_f32_16x16x32_bf16 v[18:21], v[76:79], v[14:17], v[18:21]
s_nop 7
v_swap_b32 v40, v18
v_swap_b32 v41, v19
v_swap_b32 v42, v20
v_swap_b32 v43, v21
v_swap_b32 v44, v9
v_swap_b32 v45, v8
s_mov_b32 s50, s81
s_mov_b32 s81, s92
s_mov_b32 s92, s50
v_or3_b32 v14, v22, v26, v30
v_or_b32 v14, v14, v34
v_or3_b32 v15, v23, v27, v31
v_or_b32 v15, v15, v35
v_or3_b32 v16, v24, v28, v32
v_or_b32 v16, v16, v36
v_or3_b32 v17, v25, v29, v33
v_or_b32 v17, v17, v37
v_cndmask_b32_e64 v22, 0, v38, s[36:37]
v_cndmask_b32_e64 v23, 0, v39, s[36:37]
v_cndmask_b32_e64 v24, 0, v46, s[36:37]
v_cndmask_b32_e64 v25, 0, v47, s[36:37]
v_cndmask_b32_e64 v26, 0, v38, s[38:39]
v_cndmask_b32_e64 v27, 0, v39, s[38:39]
v_cndmask_b32_e64 v28, 0, v46, s[38:39]
v_cndmask_b32_e64 v29, 0, v47, s[38:39]
v_cndmask_b32_e64 v30, 0, v38, s[40:41]
v_cndmask_b32_e64 v31, 0, v39, s[40:41]
v_cndmask_b32_e64 v32, 0, v46, s[40:41]
v_cndmask_b32_e64 v33, 0, v47, s[40:41]
v_cndmask_b32_e64 v34, 0, v38, s[42:43]
v_cndmask_b32_e64 v35, 0, v39, s[42:43]
v_cndmask_b32_e64 v36, 0, v46, s[42:43]
v_cndmask_b32_e64 v37, 0, v47, s[42:43]
s_nop 1
v_mov_b32 v38, v14
v_mov_b32 v39, v15
v_mov_b32 v46, v16
v_mov_b32 v47, v17
v_mfma_f32_16x16x32_bf16 v[14:17], a[96:99], v[22:25], 0
v_mfma_f32_16x16x32_bf16 v[14:17], a[100:103], v[26:29], v[14:17]
v_mfma_f32_16x16x32_bf16 v[14:17], a[104:107], v[30:33], v[14:17]
v_mfma_f32_16x16x32_bf16 v[14:17], a[108:111], v[34:37], v[14:17]
s_waitcnt vmcnt(28)
s_add_u32 s29, s28, 917504
buffer_load_dwordx4 a[96:99], v0, s[16:19], s29 offen offset:0 nt
buffer_load_dwordx4 a[100:103], v0, s[16:19], s29 offen offset:1024 nt
buffer_load_dwordx4 a[104:107], v0, s[16:19], s29 offen offset:2048 nt
buffer_load_dwordx4 a[108:111], v0, s[16:19], s29 offen offset:3072 nt
s_nop 1
v_mul_f32_dpp v14, v14, v2 quad_perm:[0,0,0,0] row_mask:0xf bank_mask:0xf
v_mul_f32_dpp v15, v15, v2 quad_perm:[1,1,1,1] row_mask:0xf bank_mask:0xf
v_mul_f32_dpp v16, v16, v2 quad_perm:[2,2,2,2] row_mask:0xf bank_mask:0xf
v_mul_f32_dpp v17, v17, v2 quad_perm:[3,3,3,3] row_mask:0xf bank_mask:0xf
v_add_f32 v14, v14, v15
v_add_f32 v16, v16, v17
v_add_f32 v14, v14, v8
v_add_f32 v7, v14, v16
v_cmp_lt_f32 vcc, 0, v7
s_cbranch_vccnz Lnc_109_%=
s_cmp_lg_u32 s81, 0
s_cbranch_scc1 Lnc_109_%=
Lns_108_%=:
v_exp_f32 v7, v7
s_nop 0
v_add_f32 v9, v9, v7
v_cvt_pk_bf16_f32 v10, v7, v7
s_nop 1
v_and_b32_dpp v14, v10, v3 row_newbcast:0 row_mask:0xf bank_mask:0xf
v_and_b32_dpp v15, v10, v4 row_newbcast:0 row_mask:0xf bank_mask:0xf
v_and_b32_dpp v16, v10, v5 row_newbcast:0 row_mask:0xf bank_mask:0xf
v_and_b32_dpp v17, v10, v6 row_newbcast:0 row_mask:0xf bank_mask:0xf
s_nop 1
v_mfma_f32_16x16x32_bf16 v[18:21], a[112:115], v[14:17], v[18:21]
v_and_b32_dpp v14, v10, v3 row_newbcast:4 row_mask:0xf bank_mask:0xf
v_and_b32_dpp v15, v10, v4 row_newbcast:4 row_mask:0xf bank_mask:0xf
v_and_b32_dpp v16, v10, v5 row_newbcast:4 row_mask:0xf bank_mask:0xf
v_and_b32_dpp v17, v10, v6 row_newbcast:4 row_mask:0xf bank_mask:0xf
s_nop 1
v_mfma_f32_16x16x32_bf16 v[18:21], a[116:119], v[14:17], v[18:21]
v_and_b32_dpp v14, v10, v3 row_newbcast:8 row_mask:0xf bank_mask:0xf
v_and_b32_dpp v15, v10, v4 row_newbcast:8 row_mask:0xf bank_mask:0xf
v_and_b32_dpp v16, v10, v5 row_newbcast:8 row_mask:0xf bank_mask:0xf
v_and_b32_dpp v17, v10, v6 row_newbcast:8 row_mask:0xf bank_mask:0xf
s_nop 1
v_mfma_f32_16x16x32_bf16 v[18:21], a[120:123], v[14:17], v[18:21]
v_and_b32_dpp v14, v10, v3 row_newbcast:12 row_mask:0xf bank_mask:0xf
v_and_b32_dpp v15, v10, v4 row_newbcast:12 row_mask:0xf bank_mask:0xf
v_and_b32_dpp v16, v10, v5 row_newbcast:12 row_mask:0xf bank_mask:0xf
v_and_b32_dpp v17, v10, v6 row_newbcast:12 row_mask:0xf bank_mask:0xf
s_nop 1
v_mfma_f32_16x16x32_bf16 v[18:21], a[124:127], v[14:17], v[18:21]
s_waitcnt vmcnt(28)
s_add_u32 s29, s28, 917504
buffer_load_dwordx4 a[112:115], v1, s[20:23], s29 offen offset:0 nt
buffer_load_dwordx4 a[116:119], v1, s[20:23], s29 offen offset:1024 nt
buffer_load_dwordx4 a[120:123], v1, s[20:23], s29 offen offset:2048 nt
buffer_load_dwordx4 a[124:127], v1, s[20:23], s29 offen offset:3072 nt
v_mfma_f32_16x16x32_bf16 v[14:17], a[128:131], v[22:25], 0
v_mfma_f32_16x16x32_bf16 v[14:17], a[132:135], v[26:29], v[14:17]
v_mfma_f32_16x16x32_bf16 v[14:17], a[136:139], v[30:33], v[14:17]
v_mfma_f32_16x16x32_bf16 v[14:17], a[140:143], v[34:37], v[14:17]
s_waitcnt vmcnt(28)
s_add_u32 s29, s28, 1032192
buffer_load_dwordx4 a[128:131], v0, s[16:19], s29 offen offset:0 nt
buffer_load_dwordx4 a[132:135], v0, s[16:19], s29 offen offset:1024 nt
buffer_load_dwordx4 a[136:139], v0, s[16:19], s29 offen offset:2048 nt
buffer_load_dwordx4 a[140:143], v0, s[16:19], s29 offen offset:3072 nt
s_nop 1
v_mul_f32_dpp v14, v14, v2 quad_perm:[0,0,0,0] row_mask:0xf bank_mask:0xf
v_mul_f32_dpp v15, v15, v2 quad_perm:[1,1,1,1] row_mask:0xf bank_mask:0xf
v_mul_f32_dpp v16, v16, v2 quad_perm:[2,2,2,2] row_mask:0xf bank_mask:0xf
v_mul_f32_dpp v17, v17, v2 quad_perm:[3,3,3,3] row_mask:0xf bank_mask:0xf
v_add_f32 v14, v14, v15
v_add_f32 v16, v16, v17
v_add_f32 v14, v14, v8
v_add_f32 v7, v14, v16
v_cmp_lt_f32 vcc, 0, v7
s_cbranch_vccnz Lnc_111_%=
s_cmp_lg_u32 s81, 0
s_cbranch_scc1 Lnc_111_%=
Lns_110_%=:
v_exp_f32 v7, v7
s_nop 0
v_add_f32 v9, v9, v7
v_cvt_pk_bf16_f32 v10, v7, v7
s_nop 1
v_and_b32_dpp v14, v10, v3 row_newbcast:0 row_mask:0xf bank_mask:0xf
v_and_b32_dpp v15, v10, v4 row_newbcast:0 row_mask:0xf bank_mask:0xf
v_and_b32_dpp v16, v10, v5 row_newbcast:0 row_mask:0xf bank_mask:0xf
v_and_b32_dpp v17, v10, v6 row_newbcast:0 row_mask:0xf bank_mask:0xf
s_nop 1
v_mfma_f32_16x16x32_bf16 v[18:21], a[144:147], v[14:17], v[18:21]
v_and_b32_dpp v14, v10, v3 row_newbcast:4 row_mask:0xf bank_mask:0xf
v_and_b32_dpp v15, v10, v4 row_newbcast:4 row_mask:0xf bank_mask:0xf
v_and_b32_dpp v16, v10, v5 row_newbcast:4 row_mask:0xf bank_mask:0xf
v_and_b32_dpp v17, v10, v6 row_newbcast:4 row_mask:0xf bank_mask:0xf
s_nop 1
v_mfma_f32_16x16x32_bf16 v[18:21], a[148:151], v[14:17], v[18:21]
v_and_b32_dpp v14, v10, v3 row_newbcast:8 row_mask:0xf bank_mask:0xf
v_and_b32_dpp v15, v10, v4 row_newbcast:8 row_mask:0xf bank_mask:0xf
v_and_b32_dpp v16, v10, v5 row_newbcast:8 row_mask:0xf bank_mask:0xf
v_and_b32_dpp v17, v10, v6 row_newbcast:8 row_mask:0xf bank_mask:0xf
s_nop 1
v_mfma_f32_16x16x32_bf16 v[18:21], a[152:155], v[14:17], v[18:21]
v_and_b32_dpp v14, v10, v3 row_newbcast:12 row_mask:0xf bank_mask:0xf
v_and_b32_dpp v15, v10, v4 row_newbcast:12 row_mask:0xf bank_mask:0xf
v_and_b32_dpp v16, v10, v5 row_newbcast:12 row_mask:0xf bank_mask:0xf
v_and_b32_dpp v17, v10, v6 row_newbcast:12 row_mask:0xf bank_mask:0xf
s_nop 1
v_mfma_f32_16x16x32_bf16 v[18:21], a[156:159], v[14:17], v[18:21]
s_waitcnt vmcnt(28)
s_add_u32 s29, s28, 1032192
buffer_load_dwordx4 a[144:147], v1, s[20:23], s29 offen offset:0 nt
buffer_load_dwordx4 a[148:151], v1, s[20:23], s29 offen offset:1024 nt
buffer_load_dwordx4 a[152:155], v1, s[20:23], s29 offen offset:2048 nt
buffer_load_dwordx4 a[156:159], v1, s[20:23], s29 offen offset:3072 nt
s_nop 7
v_swap_b32 v40, v18
v_swap_b32 v41, v19
v_swap_b32 v42, v20
v_swap_b32 v43, v21
v_swap_b32 v44, v9
v_swap_b32 v45, v8
s_mov_b32 s50, s81
s_mov_b32 s81, s92
s_mov_b32 s92, s50
v_or3_b32 v14, v22, v26, v30
v_or_b32 v14, v14, v34
v_or3_b32 v15, v23, v27, v31
v_or_b32 v15, v15, v35
v_or3_b32 v16, v24, v28, v32
v_or_b32 v16, v16, v36
v_or3_b32 v17, v25, v29, v33
v_or_b32 v17, v17, v37
v_cndmask_b32_e64 v22, 0, v38, s[36:37]
v_cndmask_b32_e64 v23, 0, v39, s[36:37]
v_cndmask_b32_e64 v24, 0, v46, s[36:37]
v_cndmask_b32_e64 v25, 0, v47, s[36:37]
v_cndmask_b32_e64 v26, 0, v38, s[38:39]
v_cndmask_b32_e64 v27, 0, v39, s[38:39]
v_cndmask_b32_e64 v28, 0, v46, s[38:39]
v_cndmask_b32_e64 v29, 0, v47, s[38:39]
v_cndmask_b32_e64 v30, 0, v38, s[40:41]
v_cndmask_b32_e64 v31, 0, v39, s[40:41]
v_cndmask_b32_e64 v32, 0, v46, s[40:41]
v_cndmask_b32_e64 v33, 0, v47, s[40:41]
v_cndmask_b32_e64 v34, 0, v38, s[42:43]
v_cndmask_b32_e64 v35, 0, v39, s[42:43]
v_cndmask_b32_e64 v36, 0, v46, s[42:43]
v_cndmask_b32_e64 v37, 0, v47, s[42:43]
s_nop 1
v_mov_b32 v38, v14
v_mov_b32 v39, v15
v_mov_b32 v46, v16
v_mov_b32 v47, v17
ds_read_b128 v[48:51], v11 offset:24576
ds_read_b128 v[52:55], v11 offset:25600
ds_read_b128 v[56:59], v11 offset:26624
ds_read_b128 v[60:63], v11 offset:27648
ds_read_b128 v[64:67], v11 offset:28672
ds_read_b128 v[68:71], v11 offset:29696
ds_read_b128 v[72:75], v11 offset:30720
ds_read_b128 v[76:79], v11 offset:31744
s_waitcnt lgkmcnt(0)
v_mfma_f32_16x16x32_bf16 v[14:17], v[48:51], v[22:25], 0
v_mfma_f32_16x16x32_bf16 v[14:17], v[52:55], v[26:29], v[14:17]
v_mfma_f32_16x16x32_bf16 v[14:17], v[56:59], v[30:33], v[14:17]
v_mfma_f32_16x16x32_bf16 v[14:17], v[60:63], v[34:37], v[14:17]
s_nop 7
v_mul_f32_dpp v14, v14, v2 quad_perm:[0,0,0,0] row_mask:0xf bank_mask:0xf
v_mul_f32_dpp v15, v15, v2 quad_perm:[1,1,1,1] row_mask:0xf bank_mask:0xf
v_mul_f32_dpp v16, v16, v2 quad_perm:[2,2,2,2] row_mask:0xf bank_mask:0xf
v_mul_f32_dpp v17, v17, v2 quad_perm:[3,3,3,3] row_mask:0xf bank_mask:0xf
v_add_f32 v14, v14, v15
v_add_f32 v16, v16, v17
v_add_f32 v14, v14, v8
v_add_f32 v7, v14, v16
v_cmp_lt_f32 vcc, 0, v7
s_cbranch_vccnz Lnc_113_%=
s_cmp_lg_u32 s81, 0
s_cbranch_scc1 Lnc_113_%=
Lns_112_%=:
v_exp_f32 v7, v7
s_nop 0
v_add_f32 v9, v9, v7
v_cvt_pk_bf16_f32 v10, v7, v7
s_nop 1
v_and_b32_dpp v14, v10, v3 row_newbcast:0 row_mask:0xf bank_mask:0xf
v_and_b32_dpp v15, v10, v4 row_newbcast:0 row_mask:0xf bank_mask:0xf
v_and_b32_dpp v16, v10, v5 row_newbcast:0 row_mask:0xf bank_mask:0xf
v_and_b32_dpp v17, v10, v6 row_newbcast:0 row_mask:0xf bank_mask:0xf
s_nop 1
v_mfma_f32_16x16x32_bf16 v[18:21], v[64:67], v[14:17], v[18:21]
v_and_b32_dpp v14, v10, v3 row_newbcast:4 row_mask:0xf bank_mask:0xf
v_and_b32_dpp v15, v10, v4 row_newbcast:4 row_mask:0xf bank_mask:0xf
v_and_b32_dpp v16, v10, v5 row_newbcast:4 row_mask:0xf bank_mask:0xf
v_and_b32_dpp v17, v10, v6 row_newbcast:4 row_mask:0xf bank_mask:0xf
s_nop 1
v_mfma_f32_16x16x32_bf16 v[18:21], v[68:71], v[14:17], v[18:21]
v_and_b32_dpp v14, v10, v3 row_newbcast:8 row_mask:0xf bank_mask:0xf
v_and_b32_dpp v15, v10, v4 row_newbcast:8 row_mask:0xf bank_mask:0xf
v_and_b32_dpp v16, v10, v5 row_newbcast:8 row_mask:0xf bank_mask:0xf
v_and_b32_dpp v17, v10, v6 row_newbcast:8 row_mask:0xf bank_mask:0xf
s_nop 1
v_mfma_f32_16x16x32_bf16 v[18:21], v[72:75], v[14:17], v[18:21]
v_and_b32_dpp v14, v10, v3 row_newbcast:12 row_mask:0xf bank_mask:0xf
v_and_b32_dpp v15, v10, v4 row_newbcast:12 row_mask:0xf bank_mask:0xf
v_and_b32_dpp v16, v10, v5 row_newbcast:12 row_mask:0xf bank_mask:0xf
v_and_b32_dpp v17, v10, v6 row_newbcast:12 row_mask:0xf bank_mask:0xf
s_nop 1
v_mfma_f32_16x16x32_bf16 v[18:21], v[76:79], v[14:17], v[18:21]
ds_read_b128 v[48:51], v11 offset:32768
ds_read_b128 v[52:55], v11 offset:33792
ds_read_b128 v[56:59], v11 offset:34816
ds_read_b128 v[60:63], v11 offset:35840
ds_read_b128 v[64:67], v11 offset:36864
ds_read_b128 v[68:71], v11 offset:37888
ds_read_b128 v[72:75], v11 offset:38912
ds_read_b128 v[76:79], v11 offset:39936
s_waitcnt lgkmcnt(0)
v_mfma_f32_16x16x32_bf16 v[14:17], v[48:51], v[22:25], 0
v_mfma_f32_16x16x32_bf16 v[14:17], v[52:55], v[26:29], v[14:17]
v_mfma_f32_16x16x32_bf16 v[14:17], v[56:59], v[30:33], v[14:17]
v_mfma_f32_16x16x32_bf16 v[14:17], v[60:63], v[34:37], v[14:17]
s_nop 7
v_mul_f32_dpp v14, v14, v2 quad_perm:[0,0,0,0] row_mask:0xf bank_mask:0xf
v_mul_f32_dpp v15, v15, v2 quad_perm:[1,1,1,1] row_mask:0xf bank_mask:0xf
v_mul_f32_dpp v16, v16, v2 quad_perm:[2,2,2,2] row_mask:0xf bank_mask:0xf
v_mul_f32_dpp v17, v17, v2 quad_perm:[3,3,3,3] row_mask:0xf bank_mask:0xf
v_add_f32 v14, v14, v15
v_add_f32 v16, v16, v17
v_add_f32 v14, v14, v8
v_add_f32 v7, v14, v16
v_cmp_lt_f32 vcc, 0, v7
s_cbranch_vccnz Lnc_115_%=
s_cmp_lg_u32 s81, 0
s_cbranch_scc1 Lnc_115_%=
Lns_114_%=:
v_exp_f32 v7, v7
s_nop 0
v_add_f32 v9, v9, v7
v_cvt_pk_bf16_f32 v10, v7, v7
s_nop 1
v_and_b32_dpp v14, v10, v3 row_newbcast:0 row_mask:0xf bank_mask:0xf
v_and_b32_dpp v15, v10, v4 row_newbcast:0 row_mask:0xf bank_mask:0xf
v_and_b32_dpp v16, v10, v5 row_newbcast:0 row_mask:0xf bank_mask:0xf
v_and_b32_dpp v17, v10, v6 row_newbcast:0 row_mask:0xf bank_mask:0xf
s_nop 1
v_mfma_f32_16x16x32_bf16 v[18:21], v[64:67], v[14:17], v[18:21]
v_and_b32_dpp v14, v10, v3 row_newbcast:4 row_mask:0xf bank_mask:0xf
v_and_b32_dpp v15, v10, v4 row_newbcast:4 row_mask:0xf bank_mask:0xf
v_and_b32_dpp v16, v10, v5 row_newbcast:4 row_mask:0xf bank_mask:0xf
v_and_b32_dpp v17, v10, v6 row_newbcast:4 row_mask:0xf bank_mask:0xf
s_nop 1
v_mfma_f32_16x16x32_bf16 v[18:21], v[68:71], v[14:17], v[18:21]
v_and_b32_dpp v14, v10, v3 row_newbcast:8 row_mask:0xf bank_mask:0xf
v_and_b32_dpp v15, v10, v4 row_newbcast:8 row_mask:0xf bank_mask:0xf
v_and_b32_dpp v16, v10, v5 row_newbcast:8 row_mask:0xf bank_mask:0xf
v_and_b32_dpp v17, v10, v6 row_newbcast:8 row_mask:0xf bank_mask:0xf
s_nop 1
v_mfma_f32_16x16x32_bf16 v[18:21], v[72:75], v[14:17], v[18:21]
v_and_b32_dpp v14, v10, v3 row_newbcast:12 row_mask:0xf bank_mask:0xf
v_and_b32_dpp v15, v10, v4 row_newbcast:12 row_mask:0xf bank_mask:0xf
v_and_b32_dpp v16, v10, v5 row_newbcast:12 row_mask:0xf bank_mask:0xf
v_and_b32_dpp v17, v10, v6 row_newbcast:12 row_mask:0xf bank_mask:0xf
s_nop 1
v_mfma_f32_16x16x32_bf16 v[18:21], v[76:79], v[14:17], v[18:21]
s_mov_b32 s76, s44
s_add_u32 s70, s28, 2064384
s_mov_b32 s77, 0
s_add_u32 s71, s28, 2179072
s_mov_b32 s78, 0
s_mov_b32 s80, 0
s_mov_b32 s82, 0
s_waitcnt vmcnt(28)
s_cmp_eq_u32 s77, 1
s_cselect_b32 s56, s16, s8
s_cselect_b32 s57, s17, s9
s_mov_b32 s58, 0x400000
buffer_load_dwordx4 v[48:51], v0, s[56:59], s70 offen offset:0 nt
buffer_load_dwordx4 v[52:55], v0, s[56:59], s70 offen offset:1024 nt
buffer_load_dwordx4 v[56:59], v0, s[56:59], s70 offen offset:2048 nt
buffer_load_dwordx4 v[60:63], v0, s[56:59], s70 offen offset:3072 nt
s_waitcnt vmcnt(28)
s_cmp_eq_u32 s77, 1
s_cselect_b32 s60, s20, s12
s_cselect_b32 s61, s21, s13
s_mov_b32 s62, 0x400000
buffer_load_dwordx4 v[64:67], v1, s[60:63], s70 offen offset:0 nt
buffer_load_dwordx4 v[68:71], v1, s[60:63], s70 offen offset:1024 nt
buffer_load_dwordx4 v[72:75], v1, s[60:63], s70 offen offset:2048 nt
buffer_load_dwordx4 v[76:79], v1, s[60:63], s70 offen offset:3072 nt
s_waitcnt vmcnt(28)
s_cmp_eq_u32 s78, 1
s_cselect_b32 s56, s16, s8
s_cselect_b32 s57, s17, s9
s_mov_b32 s58, 0x400000
buffer_load_dwordx4 v[80:83], v0, s[56:59], s71 offen offset:0 nt
buffer_load_dwordx4 v[84:87], v0, s[56:59], s71 offen offset:1024 nt
buffer_load_dwordx4 v[88:91], v0, s[56:59], s71 offen offset:2048 nt
buffer_load_dwordx4 v[92:95], v0, s[56:59], s71 offen offset:3072 nt
v_mfma_f32_16x16x32_bf16 v[14:17], a[0:3], v[22:25], 0
v_mfma_f32_16x16x32_bf16 v[14:17], a[4:7], v[26:29], v[14:17]
v_mfma_f32_16x16x32_bf16 v[14:17], a[8:11], v[30:33], v[14:17]
v_mfma_f32_16x16x32_bf16 v[14:17], a[12:15], v[34:37], v[14:17]
s_nop 7
v_mul_f32_dpp v14, v14, v2 quad_perm:[0,0,0,0] row_mask:0xf bank_mask:0xf
v_mul_f32_dpp v15, v15, v2 quad_perm:[1,1,1,1] row_mask:0xf bank_mask:0xf
v_mul_f32_dpp v16, v16, v2 quad_perm:[2,2,2,2] row_mask:0xf bank_mask:0xf
v_mul_f32_dpp v17, v17, v2 quad_perm:[3,3,3,3] row_mask:0xf bank_mask:0xf
v_add_f32 v14, v14, v15
v_add_f32 v16, v16, v17
v_add_f32 v14, v14, v8
v_add_f32 v7, v14, v16
v_cmp_lt_f32 vcc, 0, v7
s_cbranch_vccnz Lnc_117_%=
s_cmp_lg_u32 s81, 0
s_cbranch_scc1 Lnc_117_%=
Lns_116_%=:
v_exp_f32 v7, v7
s_nop 0
v_add_f32 v9, v9, v7
v_cvt_pk_bf16_f32 v10, v7, v7
s_nop 1
v_and_b32_dpp v14, v10, v3 row_newbcast:0 row_mask:0xf bank_mask:0xf
v_and_b32_dpp v15, v10, v4 row_newbcast:0 row_mask:0xf bank_mask:0xf
v_and_b32_dpp v16, v10, v5 row_newbcast:0 row_mask:0xf bank_mask:0xf
v_and_b32_dpp v17, v10, v6 row_newbcast:0 row_mask:0xf bank_mask:0xf
s_nop 1
v_mfma_f32_16x16x32_bf16 v[18:21], a[16:19], v[14:17], v[18:21]
v_and_b32_dpp v14, v10, v3 row_newbcast:4 row_mask:0xf bank_mask:0xf
v_and_b32_dpp v15, v10, v4 row_newbcast:4 row_mask:0xf bank_mask:0xf
v_and_b32_dpp v16, v10, v5 row_newbcast:4 row_mask:0xf bank_mask:0xf
v_and_b32_dpp v17, v10, v6 row_newbcast:4 row_mask:0xf bank_mask:0xf
s_nop 1
v_mfma_f32_16x16x32_bf16 v[18:21], a[20:23], v[14:17], v[18:21]
v_and_b32_dpp v14, v10, v3 row_newbcast:8 row_mask:0xf bank_mask:0xf
v_and_b32_dpp v15, v10, v4 row_newbcast:8 row_mask:0xf bank_mask:0xf
v_and_b32_dpp v16, v10, v5 row_newbcast:8 row_mask:0xf bank_mask:0xf
v_and_b32_dpp v17, v10, v6 row_newbcast:8 row_mask:0xf bank_mask:0xf
s_nop 1
v_mfma_f32_16x16x32_bf16 v[18:21], a[24:27], v[14:17], v[18:21]
v_and_b32_dpp v14, v10, v3 row_newbcast:12 row_mask:0xf bank_mask:0xf
v_and_b32_dpp v15, v10, v4 row_newbcast:12 row_mask:0xf bank_mask:0xf
v_and_b32_dpp v16, v10, v5 row_newbcast:12 row_mask:0xf bank_mask:0xf
v_and_b32_dpp v17, v10, v6 row_newbcast:12 row_mask:0xf bank_mask:0xf
s_nop 1
v_mfma_f32_16x16x32_bf16 v[18:21], a[28:31], v[14:17], v[18:21]
s_nop 7
v_swap_b32 v40, v18
v_swap_b32 v41, v19
v_swap_b32 v42, v20
v_swap_b32 v43, v21
v_swap_b32 v44, v9
v_swap_b32 v45, v8
s_mov_b32 s50, s81
s_mov_b32 s81, s92
s_mov_b32 s92, s50
v_or3_b32 v14, v22, v26, v30
v_or_b32 v14, v14, v34
v_or3_b32 v15, v23, v27, v31
v_or_b32 v15, v15, v35
v_or3_b32 v16, v24, v28, v32
v_or_b32 v16, v16, v36
v_or3_b32 v17, v25, v29, v33
v_or_b32 v17, v17, v37
v_cndmask_b32_e64 v22, 0, v38, s[36:37]
v_cndmask_b32_e64 v23, 0, v39, s[36:37]
v_cndmask_b32_e64 v24, 0, v46, s[36:37]
v_cndmask_b32_e64 v25, 0, v47, s[36:37]
v_cndmask_b32_e64 v26, 0, v38, s[38:39]
v_cndmask_b32_e64 v27, 0, v39, s[38:39]
v_cndmask_b32_e64 v28, 0, v46, s[38:39]
v_cndmask_b32_e64 v29, 0, v47, s[38:39]
v_cndmask_b32_e64 v30, 0, v38, s[40:41]
v_cndmask_b32_e64 v31, 0, v39, s[40:41]
v_cndmask_b32_e64 v32, 0, v46, s[40:41]
v_cndmask_b32_e64 v33, 0, v47, s[40:41]
v_cndmask_b32_e64 v34, 0, v38, s[42:43]
v_cndmask_b32_e64 v35, 0, v39, s[42:43]
v_cndmask_b32_e64 v36, 0, v46, s[42:43]
v_cndmask_b32_e64 v37, 0, v47, s[42:43]
s_nop 1
v_mov_b32 v38, v14
v_mov_b32 v39, v15
v_mov_b32 v46, v16
v_mov_b32 v47, v17
v_mfma_f32_16x16x32_bf16 v[14:17], a[160:163], v[22:25], 0
v_mfma_f32_16x16x32_bf16 v[14:17], a[164:167], v[26:29], v[14:17]
v_mfma_f32_16x16x32_bf16 v[14:17], a[168:171], v[30:33], v[14:17]
v_mfma_f32_16x16x32_bf16 v[14:17], a[172:175], v[34:37], v[14:17]
s_waitcnt vmcnt(28)
s_add_u32 s29, s28, 1146880
buffer_load_dwordx4 a[160:163], v0, s[16:19], s29 offen offset:0 nt
buffer_load_dwordx4 a[164:167], v0, s[16:19], s29 offen offset:1024 nt
buffer_load_dwordx4 a[168:171], v0, s[16:19], s29 offen offset:2048 nt
buffer_load_dwordx4 a[172:175], v0, s[16:19], s29 offen offset:3072 nt
s_nop 1
v_mul_f32_dpp v14, v14, v2 quad_perm:[0,0,0,0] row_mask:0xf bank_mask:0xf
v_mul_f32_dpp v15, v15, v2 quad_perm:[1,1,1,1] row_mask:0xf bank_mask:0xf
v_mul_f32_dpp v16, v16, v2 quad_perm:[2,2,2,2] row_mask:0xf bank_mask:0xf
v_mul_f32_dpp v17, v17, v2 quad_perm:[3,3,3,3] row_mask:0xf bank_mask:0xf
v_add_f32 v14, v14, v15
v_add_f32 v16, v16, v17
v_add_f32 v14, v14, v8
v_add_f32 v7, v14, v16
v_cmp_lt_f32 vcc, 0, v7
s_cbranch_vccnz Lnc_119_%=
s_cmp_lg_u32 s81, 0
s_cbranch_scc1 Lnc_119_%=
Lns_118_%=:
v_exp_f32 v7, v7
s_nop 0
v_add_f32 v9, v9, v7
v_cvt_pk_bf16_f32 v10, v7, v7
s_nop 1
v_and_b32_dpp v14, v10, v3 row_newbcast:0 row_mask:0xf bank_mask:0xf
v_and_b32_dpp v15, v10, v4 row_newbcast:0 row_mask:0xf bank_mask:0xf
v_and_b32_dpp v16, v10, v5 row_newbcast:0 row_mask:0xf bank_mask:0xf
v_and_b32_dpp v17, v10, v6 row_newbcast:0 row_mask:0xf bank_mask:0xf
s_nop 1
v_mfma_f32_16x16x32_bf16 v[18:21], a[176:179], v[14:17], v[18:21]
v_and_b32_dpp v14, v10, v3 row_newbcast:4 row_mask:0xf bank_mask:0xf
v_and_b32_dpp v15, v10, v4 row_newbcast:4 row_mask:0xf bank_mask:0xf
v_and_b32_dpp v16, v10, v5 row_newbcast:4 row_mask:0xf bank_mask:0xf
v_and_b32_dpp v17, v10, v6 row_newbcast:4 row_mask:0xf bank_mask:0xf
s_nop 1
v_mfma_f32_16x16x32_bf16 v[18:21], a[180:183], v[14:17], v[18:21]
v_and_b32_dpp v14, v10, v3 row_newbcast:8 row_mask:0xf bank_mask:0xf
v_and_b32_dpp v15, v10, v4 row_newbcast:8 row_mask:0xf bank_mask:0xf
v_and_b32_dpp v16, v10, v5 row_newbcast:8 row_mask:0xf bank_mask:0xf
v_and_b32_dpp v17, v10, v6 row_newbcast:8 row_mask:0xf bank_mask:0xf
s_nop 1
v_mfma_f32_16x16x32_bf16 v[18:21], a[184:187], v[14:17], v[18:21]
v_and_b32_dpp v14, v10, v3 row_newbcast:12 row_mask:0xf bank_mask:0xf
v_and_b32_dpp v15, v10, v4 row_newbcast:12 row_mask:0xf bank_mask:0xf
v_and_b32_dpp v16, v10, v5 row_newbcast:12 row_mask:0xf bank_mask:0xf
v_and_b32_dpp v17, v10, v6 row_newbcast:12 row_mask:0xf bank_mask:0xf
s_nop 1
v_mfma_f32_16x16x32_bf16 v[18:21], a[188:191], v[14:17], v[18:21]
s_waitcnt vmcnt(28)
s_add_u32 s29, s28, 1146880
buffer_load_dwordx4 a[176:179], v1, s[20:23], s29 offen offset:0 nt
buffer_load_dwordx4 a[180:183], v1, s[20:23], s29 offen offset:1024 nt
buffer_load_dwordx4 a[184:187], v1, s[20:23], s29 offen offset:2048 nt
buffer_load_dwordx4 a[188:191], v1, s[20:23], s29 offen offset:3072 nt
v_mfma_f32_16x16x32_bf16 v[14:17], a[192:195], v[22:25], 0
v_mfma_f32_16x16x32_bf16 v[14:17], a[196:199], v[26:29], v[14:17]
v_mfma_f32_16x16x32_bf16 v[14:17], a[200:203], v[30:33], v[14:17]
v_mfma_f32_16x16x32_bf16 v[14:17], a[204:207], v[34:37], v[14:17]
s_waitcnt vmcnt(28)
s_add_u32 s29, s28, 1261568
buffer_load_dwordx4 a[192:195], v0, s[16:19], s29 offen offset:0 nt
buffer_load_dwordx4 a[196:199], v0, s[16:19], s29 offen offset:1024 nt
buffer_load_dwordx4 a[200:203], v0, s[16:19], s29 offen offset:2048 nt
buffer_load_dwordx4 a[204:207], v0, s[16:19], s29 offen offset:3072 nt
s_nop 1
v_mul_f32_dpp v14, v14, v2 quad_perm:[0,0,0,0] row_mask:0xf bank_mask:0xf
v_mul_f32_dpp v15, v15, v2 quad_perm:[1,1,1,1] row_mask:0xf bank_mask:0xf
v_mul_f32_dpp v16, v16, v2 quad_perm:[2,2,2,2] row_mask:0xf bank_mask:0xf
v_mul_f32_dpp v17, v17, v2 quad_perm:[3,3,3,3] row_mask:0xf bank_mask:0xf
v_add_f32 v14, v14, v15
v_add_f32 v16, v16, v17
v_add_f32 v14, v14, v8
v_add_f32 v7, v14, v16
v_cmp_lt_f32 vcc, 0, v7
s_cbranch_vccnz Lnc_121_%=
s_cmp_lg_u32 s81, 0
s_cbranch_scc1 Lnc_121_%=
Lns_120_%=:
v_exp_f32 v7, v7
s_nop 0
v_add_f32 v9, v9, v7
v_cvt_pk_bf16_f32 v10, v7, v7
s_nop 1
v_and_b32_dpp v14, v10, v3 row_newbcast:0 row_mask:0xf bank_mask:0xf
v_and_b32_dpp v15, v10, v4 row_newbcast:0 row_mask:0xf bank_mask:0xf
v_and_b32_dpp v16, v10, v5 row_newbcast:0 row_mask:0xf bank_mask:0xf
v_and_b32_dpp v17, v10, v6 row_newbcast:0 row_mask:0xf bank_mask:0xf
s_nop 1
v_mfma_f32_16x16x32_bf16 v[18:21], a[208:211], v[14:17], v[18:21]
v_and_b32_dpp v14, v10, v3 row_newbcast:4 row_mask:0xf bank_mask:0xf
v_and_b32_dpp v15, v10, v4 row_newbcast:4 row_mask:0xf bank_mask:0xf
v_and_b32_dpp v16, v10, v5 row_newbcast:4 row_mask:0xf bank_mask:0xf
v_and_b32_dpp v17, v10, v6 row_newbcast:4 row_mask:0xf bank_mask:0xf
s_nop 1
v_mfma_f32_16x16x32_bf16 v[18:21], a[212:215], v[14:17], v[18:21]
v_and_b32_dpp v14, v10, v3 row_newbcast:8 row_mask:0xf bank_mask:0xf
v_and_b32_dpp v15, v10, v4 row_newbcast:8 row_mask:0xf bank_mask:0xf
v_and_b32_dpp v16, v10, v5 row_newbcast:8 row_mask:0xf bank_mask:0xf
v_and_b32_dpp v17, v10, v6 row_newbcast:8 row_mask:0xf bank_mask:0xf
s_nop 1
v_mfma_f32_16x16x32_bf16 v[18:21], a[216:219], v[14:17], v[18:21]
v_and_b32_dpp v14, v10, v3 row_newbcast:12 row_mask:0xf bank_mask:0xf
v_and_b32_dpp v15, v10, v4 row_newbcast:12 row_mask:0xf bank_mask:0xf
v_and_b32_dpp v16, v10, v5 row_newbcast:12 row_mask:0xf bank_mask:0xf
v_and_b32_dpp v17, v10, v6 row_newbcast:12 row_mask:0xf bank_mask:0xf
s_nop 1
v_mfma_f32_16x16x32_bf16 v[18:21], a[220:223], v[14:17], v[18:21]
s_waitcnt vmcnt(28)
s_add_u32 s29, s28, 1261568
buffer_load_dwordx4 a[208:211], v1, s[20:23], s29 offen offset:0 nt
buffer_load_dwordx4 a[212:215], v1, s[20:23], s29 offen offset:1024 nt
buffer_load_dwordx4 a[216:219], v1, s[20:23], s29 offen offset:2048 nt
buffer_load_dwordx4 a[220:223], v1, s[20:23], s29 offen offset:3072 nt
s_nop 7
v_swap_b32 v40, v18
v_swap_b32 v41, v19
v_swap_b32 v42, v20
v_swap_b32 v43, v21
v_swap_b32 v44, v9
v_swap_b32 v45, v8
s_mov_b32 s50, s81
s_mov_b32 s81, s92
s_mov_b32 s92, s50
v_or3_b32 v14, v22, v26, v30
v_or_b32 v14, v14, v34
v_or3_b32 v15, v23, v27, v31
v_or_b32 v15, v15, v35
v_or3_b32 v16, v24, v28, v32
v_or_b32 v16, v16, v36
v_or3_b32 v17, v25, v29, v33
v_or_b32 v17, v17, v37
v_cndmask_b32_e64 v22, 0, v38, s[36:37]
v_cndmask_b32_e64 v23, 0, v39, s[36:37]
v_cndmask_b32_e64 v24, 0, v46, s[36:37]
v_cndmask_b32_e64 v25, 0, v47, s[36:37]
v_cndmask_b32_e64 v26, 0, v38, s[38:39]
v_cndmask_b32_e64 v27, 0, v39, s[38:39]
v_cndmask_b32_e64 v28, 0, v46, s[38:39]
v_cndmask_b32_e64 v29, 0, v47, s[38:39]
v_cndmask_b32_e64 v30, 0, v38, s[40:41]
v_cndmask_b32_e64 v31, 0, v39, s[40:41]
v_cndmask_b32_e64 v32, 0, v46, s[40:41]
v_cndmask_b32_e64 v33, 0, v47, s[40:41]
v_cndmask_b32_e64 v34, 0, v38, s[42:43]
v_cndmask_b32_e64 v35, 0, v39, s[42:43]
v_cndmask_b32_e64 v36, 0, v46, s[42:43]
v_cndmask_b32_e64 v37, 0, v47, s[42:43]
s_nop 1
v_mov_b32 v38, v14
v_mov_b32 v39, v15
v_mov_b32 v46, v16
v_mov_b32 v47, v17
v_mfma_f32_16x16x32_bf16 v[14:17], a[32:35], v[22:25], 0
v_mfma_f32_16x16x32_bf16 v[14:17], a[36:39], v[26:29], v[14:17]
v_mfma_f32_16x16x32_bf16 v[14:17], a[40:43], v[30:33], v[14:17]
v_mfma_f32_16x16x32_bf16 v[14:17], a[44:47], v[34:37], v[14:17]
s_nop 7
v_mul_f32_dpp v14, v14, v2 quad_perm:[0,0,0,0] row_mask:0xf bank_mask:0xf
v_mul_f32_dpp v15, v15, v2 quad_perm:[1,1,1,1] row_mask:0xf bank_mask:0xf
v_mul_f32_dpp v16, v16, v2 quad_perm:[2,2,2,2] row_mask:0xf bank_mask:0xf
v_mul_f32_dpp v17, v17, v2 quad_perm:[3,3,3,3] row_mask:0xf bank_mask:0xf
v_add_f32 v14, v14, v15
v_add_f32 v16, v16, v17
v_add_f32 v14, v14, v8
v_add_f32 v7, v14, v16
v_cmp_lt_f32 vcc, 0, v7
s_cbranch_vccnz Lnc_123_%=
s_cmp_lg_u32 s81, 0
s_cbranch_scc1 Lnc_123_%=
Lns_122_%=:
v_exp_f32 v7, v7
s_nop 0
v_add_f32 v9, v9, v7
v_cvt_pk_bf16_f32 v10, v7, v7
s_nop 1
v_and_b32_dpp v14, v10, v3 row_newbcast:0 row_mask:0xf bank_mask:0xf
v_and_b32_dpp v15, v10, v4 row_newbcast:0 row_mask:0xf bank_mask:0xf
v_and_b32_dpp v16, v10, v5 row_newbcast:0 row_mask:0xf bank_mask:0xf
v_and_b32_dpp v17, v10, v6 row_newbcast:0 row_mask:0xf bank_mask:0xf
s_nop 1
v_mfma_f32_16x16x32_bf16 v[18:21], a[48:51], v[14:17], v[18:21]
v_and_b32_dpp v14, v10, v3 row_newbcast:4 row_mask:0xf bank_mask:0xf
v_and_b32_dpp v15, v10, v4 row_newbcast:4 row_mask:0xf bank_mask:0xf
v_and_b32_dpp v16, v10, v5 row_newbcast:4 row_mask:0xf bank_mask:0xf
v_and_b32_dpp v17, v10, v6 row_newbcast:4 row_mask:0xf bank_mask:0xf
s_nop 1
v_mfma_f32_16x16x32_bf16 v[18:21], a[52:55], v[14:17], v[18:21]
v_and_b32_dpp v14, v10, v3 row_newbcast:8 row_mask:0xf bank_mask:0xf
v_and_b32_dpp v15, v10, v4 row_newbcast:8 row_mask:0xf bank_mask:0xf
v_and_b32_dpp v16, v10, v5 row_newbcast:8 row_mask:0xf bank_mask:0xf
v_and_b32_dpp v17, v10, v6 row_newbcast:8 row_mask:0xf bank_mask:0xf
s_nop 1
v_mfma_f32_16x16x32_bf16 v[18:21], a[56:59], v[14:17], v[18:21]
v_and_b32_dpp v14, v10, v3 row_newbcast:12 row_mask:0xf bank_mask:0xf
v_and_b32_dpp v15, v10, v4 row_newbcast:12 row_mask:0xf bank_mask:0xf
v_and_b32_dpp v16, v10, v5 row_newbcast:12 row_mask:0xf bank_mask:0xf
v_and_b32_dpp v17, v10, v6 row_newbcast:12 row_mask:0xf bank_mask:0xf
s_nop 1
v_mfma_f32_16x16x32_bf16 v[18:21], a[60:63], v[14:17], v[18:21]
v_mfma_f32_16x16x32_bf16 v[14:17], a[64:67], v[22:25], 0
v_mfma_f32_16x16x32_bf16 v[14:17], a[68:71], v[26:29], v[14:17]
v_mfma_f32_16x16x32_bf16 v[14:17], a[72:75], v[30:33], v[14:17]
v_mfma_f32_16x16x32_bf16 v[14:17], a[76:79], v[34:37], v[14:17]
s_nop 7
v_mul_f32_dpp v14, v14, v2 quad_perm:[0,0,0,0] row_mask:0xf bank_mask:0xf
v_mul_f32_dpp v15, v15, v2 quad_perm:[1,1,1,1] row_mask:0xf bank_mask:0xf
v_mul_f32_dpp v16, v16, v2 quad_perm:[2,2,2,2] row_mask:0xf bank_mask:0xf
v_mul_f32_dpp v17, v17, v2 quad_perm:[3,3,3,3] row_mask:0xf bank_mask:0xf
v_add_f32 v14, v14, v15
v_add_f32 v16, v16, v17
v_add_f32 v14, v14, v8
v_add_f32 v7, v14, v16
v_cmp_lt_f32 vcc, 0, v7
s_cbranch_vccnz Lnc_125_%=
s_cmp_lg_u32 s81, 0
s_cbranch_scc1 Lnc_125_%=
Lns_124_%=:
v_exp_f32 v7, v7
s_nop 0
v_add_f32 v9, v9, v7
v_cvt_pk_bf16_f32 v10, v7, v7
s_nop 1
v_and_b32_dpp v14, v10, v3 row_newbcast:0 row_mask:0xf bank_mask:0xf
v_and_b32_dpp v15, v10, v4 row_newbcast:0 row_mask:0xf bank_mask:0xf
v_and_b32_dpp v16, v10, v5 row_newbcast:0 row_mask:0xf bank_mask:0xf
v_and_b32_dpp v17, v10, v6 row_newbcast:0 row_mask:0xf bank_mask:0xf
s_nop 1
v_mfma_f32_16x16x32_bf16 v[18:21], a[80:83], v[14:17], v[18:21]
v_and_b32_dpp v14, v10, v3 row_newbcast:4 row_mask:0xf bank_mask:0xf
v_and_b32_dpp v15, v10, v4 row_newbcast:4 row_mask:0xf bank_mask:0xf
v_and_b32_dpp v16, v10, v5 row_newbcast:4 row_mask:0xf bank_mask:0xf
v_and_b32_dpp v17, v10, v6 row_newbcast:4 row_mask:0xf bank_mask:0xf
s_nop 1
v_mfma_f32_16x16x32_bf16 v[18:21], a[84:87], v[14:17], v[18:21]
v_and_b32_dpp v14, v10, v3 row_newbcast:8 row_mask:0xf bank_mask:0xf
v_and_b32_dpp v15, v10, v4 row_newbcast:8 row_mask:0xf bank_mask:0xf
v_and_b32_dpp v16, v10, v5 row_newbcast:8 row_mask:0xf bank_mask:0xf
v_and_b32_dpp v17, v10, v6 row_newbcast:8 row_mask:0xf bank_mask:0xf
s_nop 1
v_mfma_f32_16x16x32_bf16 v[18:21], a[88:91], v[14:17], v[18:21]
v_and_b32_dpp v14, v10, v3 row_newbcast:12 row_mask:0xf bank_mask:0xf
v_and_b32_dpp v15, v10, v4 row_newbcast:12 row_mask:0xf bank_mask:0xf
v_and_b32_dpp v16, v10, v5 row_newbcast:12 row_mask:0xf bank_mask:0xf
v_and_b32_dpp v17, v10, v6 row_newbcast:12 row_mask:0xf bank_mask:0xf
s_nop 1
v_mfma_f32_16x16x32_bf16 v[18:21], a[92:95], v[14:17], v[18:21]
s_nop 7
v_swap_b32 v40, v18
v_swap_b32 v41, v19
v_swap_b32 v42, v20
v_swap_b32 v43, v21
v_swap_b32 v44, v9
v_swap_b32 v45, v8
s_mov_b32 s50, s81
s_mov_b32 s81, s92
s_mov_b32 s92, s50
v_or3_b32 v14, v22, v26, v30
v_or_b32 v14, v14, v34
v_or3_b32 v15, v23, v27, v31
v_or_b32 v15, v15, v35
v_or3_b32 v16, v24, v28, v32
v_or_b32 v16, v16, v36
v_or3_b32 v17, v25, v29, v33
v_or_b32 v17, v17, v37
v_cndmask_b32_e64 v22, 0, v38, s[36:37]
v_cndmask_b32_e64 v23, 0, v39, s[36:37]
v_cndmask_b32_e64 v24, 0, v46, s[36:37]
v_cndmask_b32_e64 v25, 0, v47, s[36:37]
v_cndmask_b32_e64 v26, 0, v38, s[38:39]
v_cndmask_b32_e64 v27, 0, v39, s[38:39]
v_cndmask_b32_e64 v28, 0, v46, s[38:39]
v_cndmask_b32_e64 v29, 0, v47, s[38:39]
v_cndmask_b32_e64 v30, 0, v38, s[40:41]
v_cndmask_b32_e64 v31, 0, v39, s[40:41]
v_cndmask_b32_e64 v32, 0, v46, s[40:41]
v_cndmask_b32_e64 v33, 0, v47, s[40:41]
v_cndmask_b32_e64 v34, 0, v38, s[42:43]
v_cndmask_b32_e64 v35, 0, v39, s[42:43]
v_cndmask_b32_e64 v36, 0, v46, s[42:43]
v_cndmask_b32_e64 v37, 0, v47, s[42:43]
s_nop 1
v_mov_b32 v38, v14
v_mov_b32 v39, v15
v_mov_b32 v46, v16
v_mov_b32 v47, v17
v_mfma_f32_16x16x32_bf16 v[14:17], a[224:227], v[22:25], 0
v_mfma_f32_16x16x32_bf16 v[14:17], a[228:231], v[26:29], v[14:17]
v_mfma_f32_16x16x32_bf16 v[14:17], a[232:235], v[30:33], v[14:17]
v_mfma_f32_16x16x32_bf16 v[14:17], a[236:239], v[34:37], v[14:17]
s_waitcnt vmcnt(28)
s_add_u32 s29, s28, 1376256
buffer_load_dwordx4 a[224:227], v0, s[16:19], s29 offen offset:0 nt
buffer_load_dwordx4 a[228:231], v0, s[16:19], s29 offen offset:1024 nt
buffer_load_dwordx4 a[232:235], v0, s[16:19], s29 offen offset:2048 nt
buffer_load_dwordx4 a[236:239], v0, s[16:19], s29 offen offset:3072 nt
s_nop 1
v_mul_f32_dpp v14, v14, v2 quad_perm:[0,0,0,0] row_mask:0xf bank_mask:0xf
v_mul_f32_dpp v15, v15, v2 quad_perm:[1,1,1,1] row_mask:0xf bank_mask:0xf
v_mul_f32_dpp v16, v16, v2 quad_perm:[2,2,2,2] row_mask:0xf bank_mask:0xf
v_mul_f32_dpp v17, v17, v2 quad_perm:[3,3,3,3] row_mask:0xf bank_mask:0xf
v_add_f32 v14, v14, v15
v_add_f32 v16, v16, v17
v_add_f32 v14, v14, v8
v_add_f32 v7, v14, v16
v_cmp_lt_f32 vcc, 0, v7
s_cbranch_vccnz Lnc_127_%=
s_cmp_lg_u32 s81, 0
s_cbranch_scc1 Lnc_127_%=
Lns_126_%=:
v_exp_f32 v7, v7
s_nop 0
v_add_f32 v9, v9, v7
v_cvt_pk_bf16_f32 v10, v7, v7
s_nop 1
v_and_b32_dpp v14, v10, v3 row_newbcast:0 row_mask:0xf bank_mask:0xf
v_and_b32_dpp v15, v10, v4 row_newbcast:0 row_mask:0xf bank_mask:0xf
v_and_b32_dpp v16, v10, v5 row_newbcast:0 row_mask:0xf bank_mask:0xf
v_and_b32_dpp v17, v10, v6 row_newbcast:0 row_mask:0xf bank_mask:0xf
s_nop 1
v_mfma_f32_16x16x32_bf16 v[18:21], a[240:243], v[14:17], v[18:21]
v_and_b32_dpp v14, v10, v3 row_newbcast:4 row_mask:0xf bank_mask:0xf
v_and_b32_dpp v15, v10, v4 row_newbcast:4 row_mask:0xf bank_mask:0xf
v_and_b32_dpp v16, v10, v5 row_newbcast:4 row_mask:0xf bank_mask:0xf
v_and_b32_dpp v17, v10, v6 row_newbcast:4 row_mask:0xf bank_mask:0xf
s_nop 1
v_mfma_f32_16x16x32_bf16 v[18:21], a[244:247], v[14:17], v[18:21]
v_and_b32_dpp v14, v10, v3 row_newbcast:8 row_mask:0xf bank_mask:0xf
v_and_b32_dpp v15, v10, v4 row_newbcast:8 row_mask:0xf bank_mask:0xf
v_and_b32_dpp v16, v10, v5 row_newbcast:8 row_mask:0xf bank_mask:0xf
v_and_b32_dpp v17, v10, v6 row_newbcast:8 row_mask:0xf bank_mask:0xf
s_nop 1
v_mfma_f32_16x16x32_bf16 v[18:21], a[248:251], v[14:17], v[18:21]
v_and_b32_dpp v14, v10, v3 row_newbcast:12 row_mask:0xf bank_mask:0xf
v_and_b32_dpp v15, v10, v4 row_newbcast:12 row_mask:0xf bank_mask:0xf
v_and_b32_dpp v16, v10, v5 row_newbcast:12 row_mask:0xf bank_mask:0xf
v_and_b32_dpp v17, v10, v6 row_newbcast:12 row_mask:0xf bank_mask:0xf
s_nop 1
v_mfma_f32_16x16x32_bf16 v[18:21], a[252:255], v[14:17], v[18:21]
s_waitcnt vmcnt(28)
s_add_u32 s29, s28, 1376256
buffer_load_dwordx4 a[240:243], v1, s[20:23], s29 offen offset:0 nt
buffer_load_dwordx4 a[244:247], v1, s[20:23], s29 offen offset:1024 nt
buffer_load_dwordx4 a[248:251], v1, s[20:23], s29 offen offset:2048 nt
buffer_load_dwordx4 a[252:255], v1, s[20:23], s29 offen offset:3072 nt
v_mfma_f32_16x16x32_bf16 v[14:17], v[96:99], v[22:25], 0
v_mfma_f32_16x16x32_bf16 v[14:17], v[100:103], v[26:29], v[14:17]
v_mfma_f32_16x16x32_bf16 v[14:17], v[104:107], v[30:33], v[14:17]
v_mfma_f32_16x16x32_bf16 v[14:17], v[108:111], v[34:37], v[14:17]
s_waitcnt vmcnt(28)
s_add_u32 s29, s28, 1490944
buffer_load_dwordx4 v[96:99], v0, s[16:19], s29 offen offset:0 nt
buffer_load_dwordx4 v[100:103], v0, s[16:19], s29 offen offset:1024 nt
buffer_load_dwordx4 v[104:107], v0, s[16:19], s29 offen offset:2048 nt
buffer_load_dwordx4 v[108:111], v0, s[16:19], s29 offen offset:3072 nt
s_nop 1
v_mul_f32_dpp v14, v14, v2 quad_perm:[0,0,0,0] row_mask:0xf bank_mask:0xf
v_mul_f32_dpp v15, v15, v2 quad_perm:[1,1,1,1] row_mask:0xf bank_mask:0xf
v_mul_f32_dpp v16, v16, v2 quad_perm:[2,2,2,2] row_mask:0xf bank_mask:0xf
v_mul_f32_dpp v17, v17, v2 quad_perm:[3,3,3,3] row_mask:0xf bank_mask:0xf
v_add_f32 v14, v14, v15
v_add_f32 v16, v16, v17
v_add_f32 v14, v14, v8
v_add_f32 v7, v14, v16
v_cmp_lt_f32 vcc, 0, v7
s_cbranch_vccnz Lnc_129_%=
s_cmp_lg_u32 s81, 0
s_cbranch_scc1 Lnc_129_%=
Lns_128_%=:
v_exp_f32 v7, v7
s_nop 0
v_add_f32 v9, v9, v7
v_cvt_pk_bf16_f32 v10, v7, v7
s_nop 1
v_and_b32_dpp v14, v10, v3 row_newbcast:0 row_mask:0xf bank_mask:0xf
v_and_b32_dpp v15, v10, v4 row_newbcast:0 row_mask:0xf bank_mask:0xf
v_and_b32_dpp v16, v10, v5 row_newbcast:0 row_mask:0xf bank_mask:0xf
v_and_b32_dpp v17, v10, v6 row_newbcast:0 row_mask:0xf bank_mask:0xf
s_nop 1
v_mfma_f32_16x16x32_bf16 v[18:21], v[112:115], v[14:17], v[18:21]
v_and_b32_dpp v14, v10, v3 row_newbcast:4 row_mask:0xf bank_mask:0xf
v_and_b32_dpp v15, v10, v4 row_newbcast:4 row_mask:0xf bank_mask:0xf
v_and_b32_dpp v16, v10, v5 row_newbcast:4 row_mask:0xf bank_mask:0xf
v_and_b32_dpp v17, v10, v6 row_newbcast:4 row_mask:0xf bank_mask:0xf
s_nop 1
v_mfma_f32_16x16x32_bf16 v[18:21], v[116:119], v[14:17], v[18:21]
v_and_b32_dpp v14, v10, v3 row_newbcast:8 row_mask:0xf bank_mask:0xf
v_and_b32_dpp v15, v10, v4 row_newbcast:8 row_mask:0xf bank_mask:0xf
v_and_b32_dpp v16, v10, v5 row_newbcast:8 row_mask:0xf bank_mask:0xf
v_and_b32_dpp v17, v10, v6 row_newbcast:8 row_mask:0xf bank_mask:0xf
s_nop 1
v_mfma_f32_16x16x32_bf16 v[18:21], v[120:123], v[14:17], v[18:21]
v_and_b32_dpp v14, v10, v3 row_newbcast:12 row_mask:0xf bank_mask:0xf
v_and_b32_dpp v15, v10, v4 row_newbcast:12 row_mask:0xf bank_mask:0xf
v_and_b32_dpp v16, v10, v5 row_newbcast:12 row_mask:0xf bank_mask:0xf
v_and_b32_dpp v17, v10, v6 row_newbcast:12 row_mask:0xf bank_mask:0xf
s_nop 1
v_mfma_f32_16x16x32_bf16 v[18:21], v[124:127], v[14:17], v[18:21]
s_waitcnt vmcnt(28)
s_add_u32 s29, s28, 1490944
buffer_load_dwordx4 v[112:115], v1, s[20:23], s29 offen offset:0 nt
buffer_load_dwordx4 v[116:119], v1, s[20:23], s29 offen offset:1024 nt
buffer_load_dwordx4 v[120:123], v1, s[20:23], s29 offen offset:2048 nt
buffer_load_dwordx4 v[124:127], v1, s[20:23], s29 offen offset:3072 nt
s_nop 7
v_swap_b32 v40, v18
v_swap_b32 v41, v19
v_swap_b32 v42, v20
v_swap_b32 v43, v21
v_swap_b32 v44, v9
v_swap_b32 v45, v8
s_mov_b32 s50, s81
s_mov_b32 s81, s92
s_mov_b32 s92, s50
v_or3_b32 v14, v22, v26, v30
v_or_b32 v14, v14, v34
v_or3_b32 v15, v23, v27, v31
v_or_b32 v15, v15, v35
v_or3_b32 v16, v24, v28, v32
v_or_b32 v16, v16, v36
v_or3_b32 v17, v25, v29, v33
v_or_b32 v17, v17, v37
v_cndmask_b32_e64 v22, 0, v38, s[36:37]
v_cndmask_b32_e64 v23, 0, v39, s[36:37]
v_cndmask_b32_e64 v24, 0, v46, s[36:37]
v_cndmask_b32_e64 v25, 0, v47, s[36:37]
v_cndmask_b32_e64 v26, 0, v38, s[38:39]
v_cndmask_b32_e64 v27, 0, v39, s[38:39]
v_cndmask_b32_e64 v28, 0, v46, s[38:39]
v_cndmask_b32_e64 v29, 0, v47, s[38:39]
v_cndmask_b32_e64 v30, 0, v38, s[40:41]
v_cndmask_b32_e64 v31, 0, v39, s[40:41]
v_cndmask_b32_e64 v32, 0, v46, s[40:41]
v_cndmask_b32_e64 v33, 0, v47, s[40:41]
v_cndmask_b32_e64 v34, 0, v38, s[42:43]
v_cndmask_b32_e64 v35, 0, v39, s[42:43]
v_cndmask_b32_e64 v36, 0, v46, s[42:43]
v_cndmask_b32_e64 v37, 0, v47, s[42:43]
s_nop 1
v_mov_b32 v38, v14
v_mov_b32 v39, v15
v_mov_b32 v46, v16
v_mov_b32 v47, v17
v_mfma_f32_16x16x32_bf16 v[14:17], a[96:99], v[22:25], 0
v_mfma_f32_16x16x32_bf16 v[14:17], a[100:103], v[26:29], v[14:17]
v_mfma_f32_16x16x32_bf16 v[14:17], a[104:107], v[30:33], v[14:17]
v_mfma_f32_16x16x32_bf16 v[14:17], a[108:111], v[34:37], v[14:17]
s_nop 7
v_mul_f32_dpp v14, v14, v2 quad_perm:[0,0,0,0] row_mask:0xf bank_mask:0xf
v_mul_f32_dpp v15, v15, v2 quad_perm:[1,1,1,1] row_mask:0xf bank_mask:0xf
v_mul_f32_dpp v16, v16, v2 quad_perm:[2,2,2,2] row_mask:0xf bank_mask:0xf
v_mul_f32_dpp v17, v17, v2 quad_perm:[3,3,3,3] row_mask:0xf bank_mask:0xf
v_add_f32 v14, v14, v15
v_add_f32 v16, v16, v17
v_add_f32 v14, v14, v8
v_add_f32 v7, v14, v16
v_cmp_lt_f32 vcc, 0, v7
s_cbranch_vccnz Lnc_131_%=
s_cmp_lg_u32 s81, 0
s_cbranch_scc1 Lnc_131_%=
Lns_130_%=:
v_exp_f32 v7, v7
s_nop 0
v_add_f32 v9, v9, v7
v_cvt_pk_bf16_f32 v10, v7, v7
s_nop 1
v_and_b32_dpp v14, v10, v3 row_newbcast:0 row_mask:0xf bank_mask:0xf
v_and_b32_dpp v15, v10, v4 row_newbcast:0 row_mask:0xf bank_mask:0xf
v_and_b32_dpp v16, v10, v5 row_newbcast:0 row_mask:0xf bank_mask:0xf
v_and_b32_dpp v17, v10, v6 row_newbcast:0 row_mask:0xf bank_mask:0xf
s_nop 1
v_mfma_f32_16x16x32_bf16 v[18:21], a[112:115], v[14:17], v[18:21]
v_and_b32_dpp v14, v10, v3 row_newbcast:4 row_mask:0xf bank_mask:0xf
v_and_b32_dpp v15, v10, v4 row_newbcast:4 row_mask:0xf bank_mask:0xf
v_and_b32_dpp v16, v10, v5 row_newbcast:4 row_mask:0xf bank_mask:0xf
v_and_b32_dpp v17, v10, v6 row_newbcast:4 row_mask:0xf bank_mask:0xf
s_nop 1
v_mfma_f32_16x16x32_bf16 v[18:21], a[116:119], v[14:17], v[18:21]
v_and_b32_dpp v14, v10, v3 row_newbcast:8 row_mask:0xf bank_mask:0xf
v_and_b32_dpp v15, v10, v4 row_newbcast:8 row_mask:0xf bank_mask:0xf
v_and_b32_dpp v16, v10, v5 row_newbcast:8 row_mask:0xf bank_mask:0xf
v_and_b32_dpp v17, v10, v6 row_newbcast:8 row_mask:0xf bank_mask:0xf
s_nop 1
v_mfma_f32_16x16x32_bf16 v[18:21], a[120:123], v[14:17], v[18:21]
v_and_b32_dpp v14, v10, v3 row_newbcast:12 row_mask:0xf bank_mask:0xf
v_and_b32_dpp v15, v10, v4 row_newbcast:12 row_mask:0xf bank_mask:0xf
v_and_b32_dpp v16, v10, v5 row_newbcast:12 row_mask:0xf bank_mask:0xf
v_and_b32_dpp v17, v10, v6 row_newbcast:12 row_mask:0xf bank_mask:0xf
s_nop 1
v_mfma_f32_16x16x32_bf16 v[18:21], a[124:127], v[14:17], v[18:21]
v_mfma_f32_16x16x32_bf16 v[14:17], a[128:131], v[22:25], 0
v_mfma_f32_16x16x32_bf16 v[14:17], a[132:135], v[26:29], v[14:17]
v_mfma_f32_16x16x32_bf16 v[14:17], a[136:139], v[30:33], v[14:17]
v_mfma_f32_16x16x32_bf16 v[14:17], a[140:143], v[34:37], v[14:17]
s_nop 7
v_mul_f32_dpp v14, v14, v2 quad_perm:[0,0,0,0] row_mask:0xf bank_mask:0xf
v_mul_f32_dpp v15, v15, v2 quad_perm:[1,1,1,1] row_mask:0xf bank_mask:0xf
v_mul_f32_dpp v16, v16, v2 quad_perm:[2,2,2,2] row_mask:0xf bank_mask:0xf
v_mul_f32_dpp v17, v17, v2 quad_perm:[3,3,3,3] row_mask:0xf bank_mask:0xf
v_add_f32 v14, v14, v15
v_add_f32 v16, v16, v17
v_add_f32 v14, v14, v8
v_add_f32 v7, v14, v16
v_cmp_lt_f32 vcc, 0, v7
s_cbranch_vccnz Lnc_133_%=
s_cmp_lg_u32 s81, 0
s_cbranch_scc1 Lnc_133_%=
Lns_132_%=:
v_exp_f32 v7, v7
s_nop 0
v_add_f32 v9, v9, v7
v_cvt_pk_bf16_f32 v10, v7, v7
s_nop 1
v_and_b32_dpp v14, v10, v3 row_newbcast:0 row_mask:0xf bank_mask:0xf
v_and_b32_dpp v15, v10, v4 row_newbcast:0 row_mask:0xf bank_mask:0xf
v_and_b32_dpp v16, v10, v5 row_newbcast:0 row_mask:0xf bank_mask:0xf
v_and_b32_dpp v17, v10, v6 row_newbcast:0 row_mask:0xf bank_mask:0xf
s_nop 1
v_mfma_f32_16x16x32_bf16 v[18:21], a[144:147], v[14:17], v[18:21]
v_and_b32_dpp v14, v10, v3 row_newbcast:4 row_mask:0xf bank_mask:0xf
v_and_b32_dpp v15, v10, v4 row_newbcast:4 row_mask:0xf bank_mask:0xf
v_and_b32_dpp v16, v10, v5 row_newbcast:4 row_mask:0xf bank_mask:0xf
v_and_b32_dpp v17, v10, v6 row_newbcast:4 row_mask:0xf bank_mask:0xf
s_nop 1
v_mfma_f32_16x16x32_bf16 v[18:21], a[148:151], v[14:17], v[18:21]
v_and_b32_dpp v14, v10, v3 row_newbcast:8 row_mask:0xf bank_mask:0xf
v_and_b32_dpp v15, v10, v4 row_newbcast:8 row_mask:0xf bank_mask:0xf
v_and_b32_dpp v16, v10, v5 row_newbcast:8 row_mask:0xf bank_mask:0xf
v_and_b32_dpp v17, v10, v6 row_newbcast:8 row_mask:0xf bank_mask:0xf
s_nop 1
v_mfma_f32_16x16x32_bf16 v[18:21], a[152:155], v[14:17], v[18:21]
v_and_b32_dpp v14, v10, v3 row_newbcast:12 row_mask:0xf bank_mask:0xf
v_and_b32_dpp v15, v10, v4 row_newbcast:12 row_mask:0xf bank_mask:0xf
v_and_b32_dpp v16, v10, v5 row_newbcast:12 row_mask:0xf bank_mask:0xf
v_and_b32_dpp v17, v10, v6 row_newbcast:12 row_mask:0xf bank_mask:0xf
s_nop 1
v_mfma_f32_16x16x32_bf16 v[18:21], a[156:159], v[14:17], v[18:21]
s_nop 7
v_swap_b32 v40, v18
v_swap_b32 v41, v19
v_swap_b32 v42, v20
v_swap_b32 v43, v21
v_swap_b32 v44, v9
v_swap_b32 v45, v8
s_mov_b32 s50, s81
s_mov_b32 s81, s92
s_mov_b32 s92, s50
v_or3_b32 v14, v22, v26, v30
v_or_b32 v14, v14, v34
v_or3_b32 v15, v23, v27, v31
v_or_b32 v15, v15, v35
v_or3_b32 v16, v24, v28, v32
v_or_b32 v16, v16, v36
v_or3_b32 v17, v25, v29, v33
v_or_b32 v17, v17, v37
v_cndmask_b32_e64 v22, 0, v38, s[36:37]
v_cndmask_b32_e64 v23, 0, v39, s[36:37]
v_cndmask_b32_e64 v24, 0, v46, s[36:37]
v_cndmask_b32_e64 v25, 0, v47, s[36:37]
v_cndmask_b32_e64 v26, 0, v38, s[38:39]
v_cndmask_b32_e64 v27, 0, v39, s[38:39]
v_cndmask_b32_e64 v28, 0, v46, s[38:39]
v_cndmask_b32_e64 v29, 0, v47, s[38:39]
v_cndmask_b32_e64 v30, 0, v38, s[40:41]
v_cndmask_b32_e64 v31, 0, v39, s[40:41]
v_cndmask_b32_e64 v32, 0, v46, s[40:41]
v_cndmask_b32_e64 v33, 0, v47, s[40:41]
v_cndmask_b32_e64 v34, 0, v38, s[42:43]
v_cndmask_b32_e64 v35, 0, v39, s[42:43]
v_cndmask_b32_e64 v36, 0, v46, s[42:43]
v_cndmask_b32_e64 v37, 0, v47, s[42:43]
s_nop 1
v_mov_b32 v38, v14
v_mov_b32 v39, v15
v_mov_b32 v46, v16
v_mov_b32 v47, v17
v_mfma_f32_16x16x32_bf16 v[14:17], v[128:131], v[22:25], 0
v_mfma_f32_16x16x32_bf16 v[14:17], v[132:135], v[26:29], v[14:17]
v_mfma_f32_16x16x32_bf16 v[14:17], v[136:139], v[30:33], v[14:17]
v_mfma_f32_16x16x32_bf16 v[14:17], v[140:143], v[34:37], v[14:17]
s_waitcnt vmcnt(28)
s_add_u32 s29, s28, 1605632
buffer_load_dwordx4 v[128:131], v0, s[16:19], s29 offen offset:0 nt
buffer_load_dwordx4 v[132:135], v0, s[16:19], s29 offen offset:1024 nt
buffer_load_dwordx4 v[136:139], v0, s[16:19], s29 offen offset:2048 nt
buffer_load_dwordx4 v[140:143], v0, s[16:19], s29 offen offset:3072 nt
s_nop 1
v_mul_f32_dpp v14, v14, v2 quad_perm:[0,0,0,0] row_mask:0xf bank_mask:0xf
v_mul_f32_dpp v15, v15, v2 quad_perm:[1,1,1,1] row_mask:0xf bank_mask:0xf
v_mul_f32_dpp v16, v16, v2 quad_perm:[2,2,2,2] row_mask:0xf bank_mask:0xf
v_mul_f32_dpp v17, v17, v2 quad_perm:[3,3,3,3] row_mask:0xf bank_mask:0xf
v_add_f32 v14, v14, v15
v_add_f32 v16, v16, v17
v_add_f32 v14, v14, v8
v_add_f32 v7, v14, v16
v_cmp_lt_f32 vcc, 0, v7
s_cbranch_vccnz Lnc_135_%=
s_cmp_lg_u32 s81, 0
s_cbranch_scc1 Lnc_135_%=
Lns_134_%=:
v_exp_f32 v7, v7
s_nop 0
v_add_f32 v9, v9, v7
v_cvt_pk_bf16_f32 v10, v7, v7
s_nop 1
v_and_b32_dpp v14, v10, v3 row_newbcast:0 row_mask:0xf bank_mask:0xf
v_and_b32_dpp v15, v10, v4 row_newbcast:0 row_mask:0xf bank_mask:0xf
v_and_b32_dpp v16, v10, v5 row_newbcast:0 row_mask:0xf bank_mask:0xf
v_and_b32_dpp v17, v10, v6 row_newbcast:0 row_mask:0xf bank_mask:0xf
s_nop 1
v_mfma_f32_16x16x32_bf16 v[18:21], v[144:147], v[14:17], v[18:21]
v_and_b32_dpp v14, v10, v3 row_newbcast:4 row_mask:0xf bank_mask:0xf
v_and_b32_dpp v15, v10, v4 row_newbcast:4 row_mask:0xf bank_mask:0xf
v_and_b32_dpp v16, v10, v5 row_newbcast:4 row_mask:0xf bank_mask:0xf
v_and_b32_dpp v17, v10, v6 row_newbcast:4 row_mask:0xf bank_mask:0xf
s_nop 1
v_mfma_f32_16x16x32_bf16 v[18:21], v[148:151], v[14:17], v[18:21]
v_and_b32_dpp v14, v10, v3 row_newbcast:8 row_mask:0xf bank_mask:0xf
v_and_b32_dpp v15, v10, v4 row_newbcast:8 row_mask:0xf bank_mask:0xf
v_and_b32_dpp v16, v10, v5 row_newbcast:8 row_mask:0xf bank_mask:0xf
v_and_b32_dpp v17, v10, v6 row_newbcast:8 row_mask:0xf bank_mask:0xf
s_nop 1
v_mfma_f32_16x16x32_bf16 v[18:21], v[152:155], v[14:17], v[18:21]
v_and_b32_dpp v14, v10, v3 row_newbcast:12 row_mask:0xf bank_mask:0xf
v_and_b32_dpp v15, v10, v4 row_newbcast:12 row_mask:0xf bank_mask:0xf
v_and_b32_dpp v16, v10, v5 row_newbcast:12 row_mask:0xf bank_mask:0xf
v_and_b32_dpp v17, v10, v6 row_newbcast:12 row_mask:0xf bank_mask:0xf
s_nop 1
v_mfma_f32_16x16x32_bf16 v[18:21], v[156:159], v[14:17], v[18:21]
s_waitcnt vmcnt(28)
s_add_u32 s29, s28, 1605632
buffer_load_dwordx4 v[144:147], v1, s[20:23], s29 offen offset:0 nt
buffer_load_dwordx4 v[148:151], v1, s[20:23], s29 offen offset:1024 nt
buffer_load_dwordx4 v[152:155], v1, s[20:23], s29 offen offset:2048 nt
buffer_load_dwordx4 v[156:159], v1, s[20:23], s29 offen offset:3072 nt
v_mfma_f32_16x16x32_bf16 v[14:17], v[160:163], v[22:25], 0
v_mfma_f32_16x16x32_bf16 v[14:17], v[164:167], v[26:29], v[14:17]
v_mfma_f32_16x16x32_bf16 v[14:17], v[168:171], v[30:33], v[14:17]
v_mfma_f32_16x16x32_bf16 v[14:17], v[172:175], v[34:37], v[14:17]
s_waitcnt vmcnt(28)
s_add_u32 s29, s28, 1720320
buffer_load_dwordx4 v[160:163], v0, s[16:19], s29 offen offset:0 nt
buffer_load_dwordx4 v[164:167], v0, s[16:19], s29 offen offset:1024 nt
buffer_load_dwordx4 v[168:171], v0, s[16:19], s29 offen offset:2048 nt
buffer_load_dwordx4 v[172:175], v0, s[16:19], s29 offen offset:3072 nt
s_nop 1
v_mul_f32_dpp v14, v14, v2 quad_perm:[0,0,0,0] row_mask:0xf bank_mask:0xf
v_mul_f32_dpp v15, v15, v2 quad_perm:[1,1,1,1] row_mask:0xf bank_mask:0xf
v_mul_f32_dpp v16, v16, v2 quad_perm:[2,2,2,2] row_mask:0xf bank_mask:0xf
v_mul_f32_dpp v17, v17, v2 quad_perm:[3,3,3,3] row_mask:0xf bank_mask:0xf
v_add_f32 v14, v14, v15
v_add_f32 v16, v16, v17
v_add_f32 v14, v14, v8
v_add_f32 v7, v14, v16
v_cmp_lt_f32 vcc, 0, v7
s_cbranch_vccnz Lnc_137_%=
s_cmp_lg_u32 s81, 0
s_cbranch_scc1 Lnc_137_%=
Lns_136_%=:
v_exp_f32 v7, v7
s_nop 0
v_add_f32 v9, v9, v7
v_cvt_pk_bf16_f32 v10, v7, v7
s_nop 1
v_and_b32_dpp v14, v10, v3 row_newbcast:0 row_mask:0xf bank_mask:0xf
v_and_b32_dpp v15, v10, v4 row_newbcast:0 row_mask:0xf bank_mask:0xf
v_and_b32_dpp v16, v10, v5 row_newbcast:0 row_mask:0xf bank_mask:0xf
v_and_b32_dpp v17, v10, v6 row_newbcast:0 row_mask:0xf bank_mask:0xf
s_nop 1
v_mfma_f32_16x16x32_bf16 v[18:21], v[176:179], v[14:17], v[18:21]
v_and_b32_dpp v14, v10, v3 row_newbcast:4 row_mask:0xf bank_mask:0xf
v_and_b32_dpp v15, v10, v4 row_newbcast:4 row_mask:0xf bank_mask:0xf
v_and_b32_dpp v16, v10, v5 row_newbcast:4 row_mask:0xf bank_mask:0xf
v_and_b32_dpp v17, v10, v6 row_newbcast:4 row_mask:0xf bank_mask:0xf
s_nop 1
v_mfma_f32_16x16x32_bf16 v[18:21], v[180:183], v[14:17], v[18:21]
v_and_b32_dpp v14, v10, v3 row_newbcast:8 row_mask:0xf bank_mask:0xf
v_and_b32_dpp v15, v10, v4 row_newbcast:8 row_mask:0xf bank_mask:0xf
v_and_b32_dpp v16, v10, v5 row_newbcast:8 row_mask:0xf bank_mask:0xf
v_and_b32_dpp v17, v10, v6 row_newbcast:8 row_mask:0xf bank_mask:0xf
s_nop 1
v_mfma_f32_16x16x32_bf16 v[18:21], v[184:187], v[14:17], v[18:21]
v_and_b32_dpp v14, v10, v3 row_newbcast:12 row_mask:0xf bank_mask:0xf
v_and_b32_dpp v15, v10, v4 row_newbcast:12 row_mask:0xf bank_mask:0xf
v_and_b32_dpp v16, v10, v5 row_newbcast:12 row_mask:0xf bank_mask:0xf
v_and_b32_dpp v17, v10, v6 row_newbcast:12 row_mask:0xf bank_mask:0xf
s_nop 1
v_mfma_f32_16x16x32_bf16 v[18:21], v[188:191], v[14:17], v[18:21]
s_waitcnt vmcnt(28)
s_add_u32 s29, s28, 1720320
buffer_load_dwordx4 v[176:179], v1, s[20:23], s29 offen offset:0 nt
buffer_load_dwordx4 v[180:183], v1, s[20:23], s29 offen offset:1024 nt
buffer_load_dwordx4 v[184:187], v1, s[20:23], s29 offen offset:2048 nt
buffer_load_dwordx4 v[188:191], v1, s[20:23], s29 offen offset:3072 nt
s_nop 7
v_swap_b32 v40, v18
v_swap_b32 v41, v19
v_swap_b32 v42, v20
v_swap_b32 v43, v21
v_swap_b32 v44, v9
v_swap_b32 v45, v8
s_mov_b32 s50, s81
s_mov_b32 s81, s92
s_mov_b32 s92, s50
v_or3_b32 v14, v22, v26, v30
v_or_b32 v14, v14, v34
v_or3_b32 v15, v23, v27, v31
v_or_b32 v15, v15, v35
v_or3_b32 v16, v24, v28, v32
v_or_b32 v16, v16, v36
v_or3_b32 v17, v25, v29, v33
v_or_b32 v17, v17, v37
v_cndmask_b32_e64 v22, 0, v38, s[36:37]
v_cndmask_b32_e64 v23, 0, v39, s[36:37]
v_cndmask_b32_e64 v24, 0, v46, s[36:37]
v_cndmask_b32_e64 v25, 0, v47, s[36:37]
v_cndmask_b32_e64 v26, 0, v38, s[38:39]
v_cndmask_b32_e64 v27, 0, v39, s[38:39]
v_cndmask_b32_e64 v28, 0, v46, s[38:39]
v_cndmask_b32_e64 v29, 0, v47, s[38:39]
v_cndmask_b32_e64 v30, 0, v38, s[40:41]
v_cndmask_b32_e64 v31, 0, v39, s[40:41]
v_cndmask_b32_e64 v32, 0, v46, s[40:41]
v_cndmask_b32_e64 v33, 0, v47, s[40:41]
v_cndmask_b32_e64 v34, 0, v38, s[42:43]
v_cndmask_b32_e64 v35, 0, v39, s[42:43]
v_cndmask_b32_e64 v36, 0, v46, s[42:43]
v_cndmask_b32_e64 v37, 0, v47, s[42:43]
s_nop 1
v_mov_b32 v38, v14
v_mov_b32 v39, v15
v_mov_b32 v46, v16
v_mov_b32 v47, v17
v_mfma_f32_16x16x32_bf16 v[14:17], a[160:163], v[22:25], 0
v_mfma_f32_16x16x32_bf16 v[14:17], a[164:167], v[26:29], v[14:17]
v_mfma_f32_16x16x32_bf16 v[14:17], a[168:171], v[30:33], v[14:17]
v_mfma_f32_16x16x32_bf16 v[14:17], a[172:175], v[34:37], v[14:17]
s_nop 7
v_mul_f32_dpp v14, v14, v2 quad_perm:[0,0,0,0] row_mask:0xf bank_mask:0xf
v_mul_f32_dpp v15, v15, v2 quad_perm:[1,1,1,1] row_mask:0xf bank_mask:0xf
v_mul_f32_dpp v16, v16, v2 quad_perm:[2,2,2,2] row_mask:0xf bank_mask:0xf
v_mul_f32_dpp v17, v17, v2 quad_perm:[3,3,3,3] row_mask:0xf bank_mask:0xf
v_add_f32 v14, v14, v15
v_add_f32 v16, v16, v17
v_add_f32 v14, v14, v8
v_add_f32 v7, v14, v16
v_cmp_lt_f32 vcc, 0, v7
s_cbranch_vccnz Lnc_139_%=
s_cmp_lg_u32 s81, 0
s_cbranch_scc1 Lnc_139_%=
Lns_138_%=:
v_exp_f32 v7, v7
s_nop 0
v_add_f32 v9, v9, v7
v_cvt_pk_bf16_f32 v10, v7, v7
s_nop 1
v_and_b32_dpp v14, v10, v3 row_newbcast:0 row_mask:0xf bank_mask:0xf
v_and_b32_dpp v15, v10, v4 row_newbcast:0 row_mask:0xf bank_mask:0xf
v_and_b32_dpp v16, v10, v5 row_newbcast:0 row_mask:0xf bank_mask:0xf
v_and_b32_dpp v17, v10, v6 row_newbcast:0 row_mask:0xf bank_mask:0xf
s_nop 1
v_mfma_f32_16x16x32_bf16 v[18:21], a[176:179], v[14:17], v[18:21]
v_and_b32_dpp v14, v10, v3 row_newbcast:4 row_mask:0xf bank_mask:0xf
v_and_b32_dpp v15, v10, v4 row_newbcast:4 row_mask:0xf bank_mask:0xf
v_and_b32_dpp v16, v10, v5 row_newbcast:4 row_mask:0xf bank_mask:0xf
v_and_b32_dpp v17, v10, v6 row_newbcast:4 row_mask:0xf bank_mask:0xf
s_nop 1
v_mfma_f32_16x16x32_bf16 v[18:21], a[180:183], v[14:17], v[18:21]
v_and_b32_dpp v14, v10, v3 row_newbcast:8 row_mask:0xf bank_mask:0xf
v_and_b32_dpp v15, v10, v4 row_newbcast:8 row_mask:0xf bank_mask:0xf
v_and_b32_dpp v16, v10, v5 row_newbcast:8 row_mask:0xf bank_mask:0xf
v_and_b32_dpp v17, v10, v6 row_newbcast:8 row_mask:0xf bank_mask:0xf
s_nop 1
v_mfma_f32_16x16x32_bf16 v[18:21], a[184:187], v[14:17], v[18:21]
v_and_b32_dpp v14, v10, v3 row_newbcast:12 row_mask:0xf bank_mask:0xf
v_and_b32_dpp v15, v10, v4 row_newbcast:12 row_mask:0xf bank_mask:0xf
v_and_b32_dpp v16, v10, v5 row_newbcast:12 row_mask:0xf bank_mask:0xf
v_and_b32_dpp v17, v10, v6 row_newbcast:12 row_mask:0xf bank_mask:0xf
s_nop 1
v_mfma_f32_16x16x32_bf16 v[18:21], a[188:191], v[14:17], v[18:21]
v_mfma_f32_16x16x32_bf16 v[14:17], a[192:195], v[22:25], 0
v_mfma_f32_16x16x32_bf16 v[14:17], a[196:199], v[26:29], v[14:17]
v_mfma_f32_16x16x32_bf16 v[14:17], a[200:203], v[30:33], v[14:17]
v_mfma_f32_16x16x32_bf16 v[14:17], a[204:207], v[34:37], v[14:17]
s_nop 7
v_mul_f32_dpp v14, v14, v2 quad_perm:[0,0,0,0] row_mask:0xf bank_mask:0xf
v_mul_f32_dpp v15, v15, v2 quad_perm:[1,1,1,1] row_mask:0xf bank_mask:0xf
v_mul_f32_dpp v16, v16, v2 quad_perm:[2,2,2,2] row_mask:0xf bank_mask:0xf
v_mul_f32_dpp v17, v17, v2 quad_perm:[3,3,3,3] row_mask:0xf bank_mask:0xf
v_add_f32 v14, v14, v15
v_add_f32 v16, v16, v17
v_add_f32 v14, v14, v8
v_add_f32 v7, v14, v16
v_cmp_lt_f32 vcc, 0, v7
s_cbranch_vccnz Lnc_141_%=
s_cmp_lg_u32 s81, 0
s_cbranch_scc1 Lnc_141_%=
Lns_140_%=:
v_exp_f32 v7, v7
s_nop 0
v_add_f32 v9, v9, v7
v_cvt_pk_bf16_f32 v10, v7, v7
s_nop 1
v_and_b32_dpp v14, v10, v3 row_newbcast:0 row_mask:0xf bank_mask:0xf
v_and_b32_dpp v15, v10, v4 row_newbcast:0 row_mask:0xf bank_mask:0xf
v_and_b32_dpp v16, v10, v5 row_newbcast:0 row_mask:0xf bank_mask:0xf
v_and_b32_dpp v17, v10, v6 row_newbcast:0 row_mask:0xf bank_mask:0xf
s_nop 1
v_mfma_f32_16x16x32_bf16 v[18:21], a[208:211], v[14:17], v[18:21]
v_and_b32_dpp v14, v10, v3 row_newbcast:4 row_mask:0xf bank_mask:0xf
v_and_b32_dpp v15, v10, v4 row_newbcast:4 row_mask:0xf bank_mask:0xf
v_and_b32_dpp v16, v10, v5 row_newbcast:4 row_mask:0xf bank_mask:0xf
v_and_b32_dpp v17, v10, v6 row_newbcast:4 row_mask:0xf bank_mask:0xf
s_nop 1
v_mfma_f32_16x16x32_bf16 v[18:21], a[212:215], v[14:17], v[18:21]
v_and_b32_dpp v14, v10, v3 row_newbcast:8 row_mask:0xf bank_mask:0xf
v_and_b32_dpp v15, v10, v4 row_newbcast:8 row_mask:0xf bank_mask:0xf
v_and_b32_dpp v16, v10, v5 row_newbcast:8 row_mask:0xf bank_mask:0xf
v_and_b32_dpp v17, v10, v6 row_newbcast:8 row_mask:0xf bank_mask:0xf
s_nop 1
v_mfma_f32_16x16x32_bf16 v[18:21], a[216:219], v[14:17], v[18:21]
v_and_b32_dpp v14, v10, v3 row_newbcast:12 row_mask:0xf bank_mask:0xf
v_and_b32_dpp v15, v10, v4 row_newbcast:12 row_mask:0xf bank_mask:0xf
v_and_b32_dpp v16, v10, v5 row_newbcast:12 row_mask:0xf bank_mask:0xf
v_and_b32_dpp v17, v10, v6 row_newbcast:12 row_mask:0xf bank_mask:0xf
s_nop 1
v_mfma_f32_16x16x32_bf16 v[18:21], a[220:223], v[14:17], v[18:21]
s_nop 7
v_swap_b32 v40, v18
v_swap_b32 v41, v19
v_swap_b32 v42, v20
v_swap_b32 v43, v21
v_swap_b32 v44, v9
v_swap_b32 v45, v8
s_mov_b32 s50, s81
s_mov_b32 s81, s92
s_mov_b32 s92, s50
v_or3_b32 v14, v22, v26, v30
v_or_b32 v14, v14, v34
v_or3_b32 v15, v23, v27, v31
v_or_b32 v15, v15, v35
v_or3_b32 v16, v24, v28, v32
v_or_b32 v16, v16, v36
v_or3_b32 v17, v25, v29, v33
v_or_b32 v17, v17, v37
v_cndmask_b32_e64 v22, 0, v38, s[36:37]
v_cndmask_b32_e64 v23, 0, v39, s[36:37]
v_cndmask_b32_e64 v24, 0, v46, s[36:37]
v_cndmask_b32_e64 v25, 0, v47, s[36:37]
v_cndmask_b32_e64 v26, 0, v38, s[38:39]
v_cndmask_b32_e64 v27, 0, v39, s[38:39]
v_cndmask_b32_e64 v28, 0, v46, s[38:39]
v_cndmask_b32_e64 v29, 0, v47, s[38:39]
v_cndmask_b32_e64 v30, 0, v38, s[40:41]
v_cndmask_b32_e64 v31, 0, v39, s[40:41]
v_cndmask_b32_e64 v32, 0, v46, s[40:41]
v_cndmask_b32_e64 v33, 0, v47, s[40:41]
v_cndmask_b32_e64 v34, 0, v38, s[42:43]
v_cndmask_b32_e64 v35, 0, v39, s[42:43]
v_cndmask_b32_e64 v36, 0, v46, s[42:43]
v_cndmask_b32_e64 v37, 0, v47, s[42:43]
s_nop 1
v_mov_b32 v38, v14
v_mov_b32 v39, v15
v_mov_b32 v46, v16
v_mov_b32 v47, v17
v_mfma_f32_16x16x32_bf16 v[14:17], v[192:195], v[22:25], 0
v_mfma_f32_16x16x32_bf16 v[14:17], v[196:199], v[26:29], v[14:17]
v_mfma_f32_16x16x32_bf16 v[14:17], v[200:203], v[30:33], v[14:17]
v_mfma_f32_16x16x32_bf16 v[14:17], v[204:207], v[34:37], v[14:17]
s_waitcnt vmcnt(28)
s_add_u32 s29, s28, 1835008
buffer_load_dwordx4 v[192:195], v0, s[16:19], s29 offen offset:0 nt
buffer_load_dwordx4 v[196:199], v0, s[16:19], s29 offen offset:1024 nt
buffer_load_dwordx4 v[200:203], v0, s[16:19], s29 offen offset:2048 nt
buffer_load_dwordx4 v[204:207], v0, s[16:19], s29 offen offset:3072 nt
s_nop 1
v_mul_f32_dpp v14, v14, v2 quad_perm:[0,0,0,0] row_mask:0xf bank_mask:0xf
v_mul_f32_dpp v15, v15, v2 quad_perm:[1,1,1,1] row_mask:0xf bank_mask:0xf
v_mul_f32_dpp v16, v16, v2 quad_perm:[2,2,2,2] row_mask:0xf bank_mask:0xf
v_mul_f32_dpp v17, v17, v2 quad_perm:[3,3,3,3] row_mask:0xf bank_mask:0xf
v_add_f32 v14, v14, v15
v_add_f32 v16, v16, v17
v_add_f32 v14, v14, v8
v_add_f32 v7, v14, v16
v_cmp_lt_f32 vcc, 0, v7
s_cbranch_vccnz Lnc_143_%=
s_cmp_lg_u32 s81, 0
s_cbranch_scc1 Lnc_143_%=
Lns_142_%=:
v_exp_f32 v7, v7
s_nop 0
v_add_f32 v9, v9, v7
v_cvt_pk_bf16_f32 v10, v7, v7
s_nop 1
v_and_b32_dpp v14, v10, v3 row_newbcast:0 row_mask:0xf bank_mask:0xf
v_and_b32_dpp v15, v10, v4 row_newbcast:0 row_mask:0xf bank_mask:0xf
v_and_b32_dpp v16, v10, v5 row_newbcast:0 row_mask:0xf bank_mask:0xf
v_and_b32_dpp v17, v10, v6 row_newbcast:0 row_mask:0xf bank_mask:0xf
s_nop 1
v_mfma_f32_16x16x32_bf16 v[18:21], v[208:211], v[14:17], v[18:21]
v_and_b32_dpp v14, v10, v3 row_newbcast:4 row_mask:0xf bank_mask:0xf
v_and_b32_dpp v15, v10, v4 row_newbcast:4 row_mask:0xf bank_mask:0xf
v_and_b32_dpp v16, v10, v5 row_newbcast:4 row_mask:0xf bank_mask:0xf
v_and_b32_dpp v17, v10, v6 row_newbcast:4 row_mask:0xf bank_mask:0xf
s_nop 1
v_mfma_f32_16x16x32_bf16 v[18:21], v[212:215], v[14:17], v[18:21]
v_and_b32_dpp v14, v10, v3 row_newbcast:8 row_mask:0xf bank_mask:0xf
v_and_b32_dpp v15, v10, v4 row_newbcast:8 row_mask:0xf bank_mask:0xf
v_and_b32_dpp v16, v10, v5 row_newbcast:8 row_mask:0xf bank_mask:0xf
v_and_b32_dpp v17, v10, v6 row_newbcast:8 row_mask:0xf bank_mask:0xf
s_nop 1
v_mfma_f32_16x16x32_bf16 v[18:21], v[216:219], v[14:17], v[18:21]
v_and_b32_dpp v14, v10, v3 row_newbcast:12 row_mask:0xf bank_mask:0xf
v_and_b32_dpp v15, v10, v4 row_newbcast:12 row_mask:0xf bank_mask:0xf
v_and_b32_dpp v16, v10, v5 row_newbcast:12 row_mask:0xf bank_mask:0xf
v_and_b32_dpp v17, v10, v6 row_newbcast:12 row_mask:0xf bank_mask:0xf
s_nop 1
v_mfma_f32_16x16x32_bf16 v[18:21], v[220:223], v[14:17], v[18:21]
s_waitcnt vmcnt(28)
s_add_u32 s29, s28, 1835008
buffer_load_dwordx4 v[208:211], v1, s[20:23], s29 offen offset:0 nt
buffer_load_dwordx4 v[212:215], v1, s[20:23], s29 offen offset:1024 nt
buffer_load_dwordx4 v[216:219], v1, s[20:23], s29 offen offset:2048 nt
buffer_load_dwordx4 v[220:223], v1, s[20:23], s29 offen offset:3072 nt
v_mfma_f32_16x16x32_bf16 v[14:17], v[224:227], v[22:25], 0
v_mfma_f32_16x16x32_bf16 v[14:17], v[228:231], v[26:29], v[14:17]
v_mfma_f32_16x16x32_bf16 v[14:17], v[232:235], v[30:33], v[14:17]
v_mfma_f32_16x16x32_bf16 v[14:17], v[236:239], v[34:37], v[14:17]
s_waitcnt vmcnt(28)
s_add_u32 s29, s28, 1949696
buffer_load_dwordx4 v[224:227], v0, s[16:19], s29 offen offset:0 nt
buffer_load_dwordx4 v[228:231], v0, s[16:19], s29 offen offset:1024 nt
buffer_load_dwordx4 v[232:235], v0, s[16:19], s29 offen offset:2048 nt
buffer_load_dwordx4 v[236:239], v0, s[16:19], s29 offen offset:3072 nt
s_nop 1
v_mul_f32_dpp v14, v14, v2 quad_perm:[0,0,0,0] row_mask:0xf bank_mask:0xf
v_mul_f32_dpp v15, v15, v2 quad_perm:[1,1,1,1] row_mask:0xf bank_mask:0xf
v_mul_f32_dpp v16, v16, v2 quad_perm:[2,2,2,2] row_mask:0xf bank_mask:0xf
v_mul_f32_dpp v17, v17, v2 quad_perm:[3,3,3,3] row_mask:0xf bank_mask:0xf
v_add_f32 v14, v14, v15
v_add_f32 v16, v16, v17
v_add_f32 v14, v14, v8
v_add_f32 v7, v14, v16
v_cmp_lt_f32 vcc, 0, v7
s_cbranch_vccnz Lnc_145_%=
s_cmp_lg_u32 s81, 0
s_cbranch_scc1 Lnc_145_%=
Lns_144_%=:
v_exp_f32 v7, v7
s_nop 0
v_add_f32 v9, v9, v7
v_cvt_pk_bf16_f32 v10, v7, v7
s_nop 1
v_and_b32_dpp v14, v10, v3 row_newbcast:0 row_mask:0xf bank_mask:0xf
v_and_b32_dpp v15, v10, v4 row_newbcast:0 row_mask:0xf bank_mask:0xf
v_and_b32_dpp v16, v10, v5 row_newbcast:0 row_mask:0xf bank_mask:0xf
v_and_b32_dpp v17, v10, v6 row_newbcast:0 row_mask:0xf bank_mask:0xf
s_nop 1
v_mfma_f32_16x16x32_bf16 v[18:21], v[240:243], v[14:17], v[18:21]
v_and_b32_dpp v14, v10, v3 row_newbcast:4 row_mask:0xf bank_mask:0xf
v_and_b32_dpp v15, v10, v4 row_newbcast:4 row_mask:0xf bank_mask:0xf
v_and_b32_dpp v16, v10, v5 row_newbcast:4 row_mask:0xf bank_mask:0xf
v_and_b32_dpp v17, v10, v6 row_newbcast:4 row_mask:0xf bank_mask:0xf
s_nop 1
v_mfma_f32_16x16x32_bf16 v[18:21], v[244:247], v[14:17], v[18:21]
v_and_b32_dpp v14, v10, v3 row_newbcast:8 row_mask:0xf bank_mask:0xf
v_and_b32_dpp v15, v10, v4 row_newbcast:8 row_mask:0xf bank_mask:0xf
v_and_b32_dpp v16, v10, v5 row_newbcast:8 row_mask:0xf bank_mask:0xf
v_and_b32_dpp v17, v10, v6 row_newbcast:8 row_mask:0xf bank_mask:0xf
s_nop 1
v_mfma_f32_16x16x32_bf16 v[18:21], v[248:251], v[14:17], v[18:21]
v_and_b32_dpp v14, v10, v3 row_newbcast:12 row_mask:0xf bank_mask:0xf
v_and_b32_dpp v15, v10, v4 row_newbcast:12 row_mask:0xf bank_mask:0xf
v_and_b32_dpp v16, v10, v5 row_newbcast:12 row_mask:0xf bank_mask:0xf
v_and_b32_dpp v17, v10, v6 row_newbcast:12 row_mask:0xf bank_mask:0xf
s_nop 1
v_mfma_f32_16x16x32_bf16 v[18:21], v[252:255], v[14:17], v[18:21]
s_waitcnt vmcnt(28)
s_add_u32 s29, s28, 1949696
buffer_load_dwordx4 v[240:243], v1, s[20:23], s29 offen offset:0 nt
buffer_load_dwordx4 v[244:247], v1, s[20:23], s29 offen offset:1024 nt
buffer_load_dwordx4 v[248:251], v1, s[20:23], s29 offen offset:2048 nt
buffer_load_dwordx4 v[252:255], v1, s[20:23], s29 offen offset:3072 nt
s_nop 7
v_swap_b32 v40, v18
v_swap_b32 v41, v19
v_swap_b32 v42, v20
v_swap_b32 v43, v21
v_swap_b32 v44, v9
v_swap_b32 v45, v8
s_mov_b32 s50, s81
s_mov_b32 s81, s92
s_mov_b32 s92, s50
v_or3_b32 v14, v22, v26, v30
v_or_b32 v14, v14, v34
v_or3_b32 v15, v23, v27, v31
v_or_b32 v15, v15, v35
v_or3_b32 v16, v24, v28, v32
v_or_b32 v16, v16, v36
v_or3_b32 v17, v25, v29, v33
v_or_b32 v17, v17, v37
v_cndmask_b32_e64 v22, 0, v38, s[36:37]
v_cndmask_b32_e64 v23, 0, v39, s[36:37]
v_cndmask_b32_e64 v24, 0, v46, s[36:37]
v_cndmask_b32_e64 v25, 0, v47, s[36:37]
v_cndmask_b32_e64 v26, 0, v38, s[38:39]
v_cndmask_b32_e64 v27, 0, v39, s[38:39]
v_cndmask_b32_e64 v28, 0, v46, s[38:39]
v_cndmask_b32_e64 v29, 0, v47, s[38:39]
v_cndmask_b32_e64 v30, 0, v38, s[40:41]
v_cndmask_b32_e64 v31, 0, v39, s[40:41]
v_cndmask_b32_e64 v32, 0, v46, s[40:41]
v_cndmask_b32_e64 v33, 0, v47, s[40:41]
v_cndmask_b32_e64 v34, 0, v38, s[42:43]
v_cndmask_b32_e64 v35, 0, v39, s[42:43]
v_cndmask_b32_e64 v36, 0, v46, s[42:43]
v_cndmask_b32_e64 v37, 0, v47, s[42:43]
s_nop 1
v_mov_b32 v38, v14
v_mov_b32 v39, v15
v_mov_b32 v46, v16
v_mov_b32 v47, v17
v_mfma_f32_16x16x32_bf16 v[14:17], a[224:227], v[22:25], 0
v_mfma_f32_16x16x32_bf16 v[14:17], a[228:231], v[26:29], v[14:17]
v_mfma_f32_16x16x32_bf16 v[14:17], a[232:235], v[30:33], v[14:17]
v_mfma_f32_16x16x32_bf16 v[14:17], a[236:239], v[34:37], v[14:17]
s_nop 7
v_mul_f32_dpp v14, v14, v2 quad_perm:[0,0,0,0] row_mask:0xf bank_mask:0xf
v_mul_f32_dpp v15, v15, v2 quad_perm:[1,1,1,1] row_mask:0xf bank_mask:0xf
v_mul_f32_dpp v16, v16, v2 quad_perm:[2,2,2,2] row_mask:0xf bank_mask:0xf
v_mul_f32_dpp v17, v17, v2 quad_perm:[3,3,3,3] row_mask:0xf bank_mask:0xf
v_add_f32 v14, v14, v15
v_add_f32 v16, v16, v17
v_add_f32 v14, v14, v8
v_add_f32 v7, v14, v16
v_cmp_lt_f32 vcc, 0, v7
s_cbranch_vccnz Lnc_147_%=
s_cmp_lg_u32 s81, 0
s_cbranch_scc1 Lnc_147_%=
Lns_146_%=:
v_exp_f32 v7, v7
s_nop 0
v_add_f32 v9, v9, v7
v_cvt_pk_bf16_f32 v10, v7, v7
s_nop 1
v_and_b32_dpp v14, v10, v3 row_newbcast:0 row_mask:0xf bank_mask:0xf
v_and_b32_dpp v15, v10, v4 row_newbcast:0 row_mask:0xf bank_mask:0xf
v_and_b32_dpp v16, v10, v5 row_newbcast:0 row_mask:0xf bank_mask:0xf
v_and_b32_dpp v17, v10, v6 row_newbcast:0 row_mask:0xf bank_mask:0xf
s_nop 1
v_mfma_f32_16x16x32_bf16 v[18:21], a[240:243], v[14:17], v[18:21]
v_and_b32_dpp v14, v10, v3 row_newbcast:4 row_mask:0xf bank_mask:0xf
v_and_b32_dpp v15, v10, v4 row_newbcast:4 row_mask:0xf bank_mask:0xf
v_and_b32_dpp v16, v10, v5 row_newbcast:4 row_mask:0xf bank_mask:0xf
v_and_b32_dpp v17, v10, v6 row_newbcast:4 row_mask:0xf bank_mask:0xf
s_nop 1
v_mfma_f32_16x16x32_bf16 v[18:21], a[244:247], v[14:17], v[18:21]
v_and_b32_dpp v14, v10, v3 row_newbcast:8 row_mask:0xf bank_mask:0xf
v_and_b32_dpp v15, v10, v4 row_newbcast:8 row_mask:0xf bank_mask:0xf
v_and_b32_dpp v16, v10, v5 row_newbcast:8 row_mask:0xf bank_mask:0xf
v_and_b32_dpp v17, v10, v6 row_newbcast:8 row_mask:0xf bank_mask:0xf
s_nop 1
v_mfma_f32_16x16x32_bf16 v[18:21], a[248:251], v[14:17], v[18:21]
v_and_b32_dpp v14, v10, v3 row_newbcast:12 row_mask:0xf bank_mask:0xf
v_and_b32_dpp v15, v10, v4 row_newbcast:12 row_mask:0xf bank_mask:0xf
v_and_b32_dpp v16, v10, v5 row_newbcast:12 row_mask:0xf bank_mask:0xf
v_and_b32_dpp v17, v10, v6 row_newbcast:12 row_mask:0xf bank_mask:0xf
s_nop 1
v_mfma_f32_16x16x32_bf16 v[18:21], a[252:255], v[14:17], v[18:21]
v_mfma_f32_16x16x32_bf16 v[14:17], v[96:99], v[22:25], 0
v_mfma_f32_16x16x32_bf16 v[14:17], v[100:103], v[26:29], v[14:17]
v_mfma_f32_16x16x32_bf16 v[14:17], v[104:107], v[30:33], v[14:17]
v_mfma_f32_16x16x32_bf16 v[14:17], v[108:111], v[34:37], v[14:17]
s_nop 7
v_mul_f32_dpp v14, v14, v2 quad_perm:[0,0,0,0] row_mask:0xf bank_mask:0xf
v_mul_f32_dpp v15, v15, v2 quad_perm:[1,1,1,1] row_mask:0xf bank_mask:0xf
v_mul_f32_dpp v16, v16, v2 quad_perm:[2,2,2,2] row_mask:0xf bank_mask:0xf
v_mul_f32_dpp v17, v17, v2 quad_perm:[3,3,3,3] row_mask:0xf bank_mask:0xf
v_add_f32 v14, v14, v15
v_add_f32 v16, v16, v17
v_add_f32 v14, v14, v8
v_add_f32 v7, v14, v16
v_cmp_lt_f32 vcc, 0, v7
s_cbranch_vccnz Lnc_149_%=
s_cmp_lg_u32 s81, 0
s_cbranch_scc1 Lnc_149_%=
Lns_148_%=:
v_exp_f32 v7, v7
s_nop 0
v_add_f32 v9, v9, v7
v_cvt_pk_bf16_f32 v10, v7, v7
s_nop 1
v_and_b32_dpp v14, v10, v3 row_newbcast:0 row_mask:0xf bank_mask:0xf
v_and_b32_dpp v15, v10, v4 row_newbcast:0 row_mask:0xf bank_mask:0xf
v_and_b32_dpp v16, v10, v5 row_newbcast:0 row_mask:0xf bank_mask:0xf
v_and_b32_dpp v17, v10, v6 row_newbcast:0 row_mask:0xf bank_mask:0xf
s_nop 1
v_mfma_f32_16x16x32_bf16 v[18:21], v[112:115], v[14:17], v[18:21]
v_and_b32_dpp v14, v10, v3 row_newbcast:4 row_mask:0xf bank_mask:0xf
v_and_b32_dpp v15, v10, v4 row_newbcast:4 row_mask:0xf bank_mask:0xf
v_and_b32_dpp v16, v10, v5 row_newbcast:4 row_mask:0xf bank_mask:0xf
v_and_b32_dpp v17, v10, v6 row_newbcast:4 row_mask:0xf bank_mask:0xf
s_nop 1
v_mfma_f32_16x16x32_bf16 v[18:21], v[116:119], v[14:17], v[18:21]
v_and_b32_dpp v14, v10, v3 row_newbcast:8 row_mask:0xf bank_mask:0xf
v_and_b32_dpp v15, v10, v4 row_newbcast:8 row_mask:0xf bank_mask:0xf
v_and_b32_dpp v16, v10, v5 row_newbcast:8 row_mask:0xf bank_mask:0xf
v_and_b32_dpp v17, v10, v6 row_newbcast:8 row_mask:0xf bank_mask:0xf
s_nop 1
v_mfma_f32_16x16x32_bf16 v[18:21], v[120:123], v[14:17], v[18:21]
v_and_b32_dpp v14, v10, v3 row_newbcast:12 row_mask:0xf bank_mask:0xf
v_and_b32_dpp v15, v10, v4 row_newbcast:12 row_mask:0xf bank_mask:0xf
v_and_b32_dpp v16, v10, v5 row_newbcast:12 row_mask:0xf bank_mask:0xf
v_and_b32_dpp v17, v10, v6 row_newbcast:12 row_mask:0xf bank_mask:0xf
s_nop 1
v_mfma_f32_16x16x32_bf16 v[18:21], v[124:127], v[14:17], v[18:21]
s_waitcnt vmcnt(28)
v_mfma_f32_16x16x32_bf16 v[14:17], v[128:131], v[22:25], 0
v_mfma_f32_16x16x32_bf16 v[14:17], v[132:135], v[26:29], v[14:17]
v_mfma_f32_16x16x32_bf16 v[14:17], v[136:139], v[30:33], v[14:17]
v_mfma_f32_16x16x32_bf16 v[14:17], v[140:143], v[34:37], v[14:17]
s_nop 7
v_mul_f32_dpp v14, v14, v2 quad_perm:[0,0,0,0] row_mask:0xf bank_mask:0xf
v_mul_f32_dpp v15, v15, v2 quad_perm:[1,1,1,1] row_mask:0xf bank_mask:0xf
v_mul_f32_dpp v16, v16, v2 quad_perm:[2,2,2,2] row_mask:0xf bank_mask:0xf
v_mul_f32_dpp v17, v17, v2 quad_perm:[3,3,3,3] row_mask:0xf bank_mask:0xf
v_add_f32 v14, v14, v15
v_add_f32 v16, v16, v17
v_add_f32 v14, v14, v8
v_add_f32 v7, v14, v16
v_cmp_lt_f32 vcc, 0, v7
s_cbranch_vccnz Lnc_151_%=
s_cmp_lg_u32 s81, 0
s_cbranch_scc1 Lnc_151_%=
Lns_150_%=:
v_exp_f32 v7, v7
s_nop 0
v_add_f32 v9, v9, v7
v_cvt_pk_bf16_f32 v10, v7, v7
s_waitcnt vmcnt(24)
s_nop 1
v_and_b32_dpp v14, v10, v3 row_newbcast:0 row_mask:0xf bank_mask:0xf
v_and_b32_dpp v15, v10, v4 row_newbcast:0 row_mask:0xf bank_mask:0xf
v_and_b32_dpp v16, v10, v5 row_newbcast:0 row_mask:0xf bank_mask:0xf
v_and_b32_dpp v17, v10, v6 row_newbcast:0 row_mask:0xf bank_mask:0xf
s_nop 1
v_mfma_f32_16x16x32_bf16 v[18:21], v[144:147], v[14:17], v[18:21]
v_and_b32_dpp v14, v10, v3 row_newbcast:4 row_mask:0xf bank_mask:0xf
v_and_b32_dpp v15, v10, v4 row_newbcast:4 row_mask:0xf bank_mask:0xf
v_and_b32_dpp v16, v10, v5 row_newbcast:4 row_mask:0xf bank_mask:0xf
v_and_b32_dpp v17, v10, v6 row_newbcast:4 row_mask:0xf bank_mask:0xf
s_nop 1
v_mfma_f32_16x16x32_bf16 v[18:21], v[148:151], v[14:17], v[18:21]
v_and_b32_dpp v14, v10, v3 row_newbcast:8 row_mask:0xf bank_mask:0xf
v_and_b32_dpp v15, v10, v4 row_newbcast:8 row_mask:0xf bank_mask:0xf
v_and_b32_dpp v16, v10, v5 row_newbcast:8 row_mask:0xf bank_mask:0xf
v_and_b32_dpp v17, v10, v6 row_newbcast:8 row_mask:0xf bank_mask:0xf
s_nop 1
v_mfma_f32_16x16x32_bf16 v[18:21], v[152:155], v[14:17], v[18:21]
v_and_b32_dpp v14, v10, v3 row_newbcast:12 row_mask:0xf bank_mask:0xf
v_and_b32_dpp v15, v10, v4 row_newbcast:12 row_mask:0xf bank_mask:0xf
v_and_b32_dpp v16, v10, v5 row_newbcast:12 row_mask:0xf bank_mask:0xf
v_and_b32_dpp v17, v10, v6 row_newbcast:12 row_mask:0xf bank_mask:0xf
s_nop 1
v_mfma_f32_16x16x32_bf16 v[18:21], v[156:159], v[14:17], v[18:21]
s_waitcnt vmcnt(20)
v_mfma_f32_16x16x32_bf16 v[14:17], v[160:163], v[22:25], 0
v_mfma_f32_16x16x32_bf16 v[14:17], v[164:167], v[26:29], v[14:17]
v_mfma_f32_16x16x32_bf16 v[14:17], v[168:171], v[30:33], v[14:17]
v_mfma_f32_16x16x32_bf16 v[14:17], v[172:175], v[34:37], v[14:17]
s_nop 7
v_mul_f32_dpp v14, v14, v2 quad_perm:[0,0,0,0] row_mask:0xf bank_mask:0xf
v_mul_f32_dpp v15, v15, v2 quad_perm:[1,1,1,1] row_mask:0xf bank_mask:0xf
v_mul_f32_dpp v16, v16, v2 quad_perm:[2,2,2,2] row_mask:0xf bank_mask:0xf
v_mul_f32_dpp v17, v17, v2 quad_perm:[3,3,3,3] row_mask:0xf bank_mask:0xf
v_add_f32 v14, v14, v15
v_add_f32 v16, v16, v17
v_add_f32 v14, v14, v8
v_add_f32 v7, v14, v16
v_cmp_lt_f32 vcc, 0, v7
s_cbranch_vccnz Lnc_153_%=
s_cmp_lg_u32 s81, 0
s_cbranch_scc1 Lnc_153_%=
Lns_152_%=:
v_exp_f32 v7, v7
s_nop 0
v_add_f32 v9, v9, v7
v_cvt_pk_bf16_f32 v10, v7, v7
s_waitcnt vmcnt(16)
s_nop 1
v_and_b32_dpp v14, v10, v3 row_newbcast:0 row_mask:0xf bank_mask:0xf
v_and_b32_dpp v15, v10, v4 row_newbcast:0 row_mask:0xf bank_mask:0xf
v_and_b32_dpp v16, v10, v5 row_newbcast:0 row_mask:0xf bank_mask:0xf
v_and_b32_dpp v17, v10, v6 row_newbcast:0 row_mask:0xf bank_mask:0xf
s_nop 1
v_mfma_f32_16x16x32_bf16 v[18:21], v[176:179], v[14:17], v[18:21]
v_and_b32_dpp v14, v10, v3 row_newbcast:4 row_mask:0xf bank_mask:0xf
v_and_b32_dpp v15, v10, v4 row_newbcast:4 row_mask:0xf bank_mask:0xf
v_and_b32_dpp v16, v10, v5 row_newbcast:4 row_mask:0xf bank_mask:0xf
v_and_b32_dpp v17, v10, v6 row_newbcast:4 row_mask:0xf bank_mask:0xf
s_nop 1
v_mfma_f32_16x16x32_bf16 v[18:21], v[180:183], v[14:17], v[18:21]
v_and_b32_dpp v14, v10, v3 row_newbcast:8 row_mask:0xf bank_mask:0xf
v_and_b32_dpp v15, v10, v4 row_newbcast:8 row_mask:0xf bank_mask:0xf
v_and_b32_dpp v16, v10, v5 row_newbcast:8 row_mask:0xf bank_mask:0xf
v_and_b32_dpp v17, v10, v6 row_newbcast:8 row_mask:0xf bank_mask:0xf
s_nop 1
v_mfma_f32_16x16x32_bf16 v[18:21], v[184:187], v[14:17], v[18:21]
v_and_b32_dpp v14, v10, v3 row_newbcast:12 row_mask:0xf bank_mask:0xf
v_and_b32_dpp v15, v10, v4 row_newbcast:12 row_mask:0xf bank_mask:0xf
v_and_b32_dpp v16, v10, v5 row_newbcast:12 row_mask:0xf bank_mask:0xf
v_and_b32_dpp v17, v10, v6 row_newbcast:12 row_mask:0xf bank_mask:0xf
s_nop 1
v_mfma_f32_16x16x32_bf16 v[18:21], v[188:191], v[14:17], v[18:21]
s_waitcnt vmcnt(12)
v_mfma_f32_16x16x32_bf16 v[14:17], v[192:195], v[22:25], 0
v_mfma_f32_16x16x32_bf16 v[14:17], v[196:199], v[26:29], v[14:17]
v_mfma_f32_16x16x32_bf16 v[14:17], v[200:203], v[30:33], v[14:17]
v_mfma_f32_16x16x32_bf16 v[14:17], v[204:207], v[34:37], v[14:17]
s_nop 7
v_mul_f32_dpp v14, v14, v2 quad_perm:[0,0,0,0] row_mask:0xf bank_mask:0xf
v_mul_f32_dpp v15, v15, v2 quad_perm:[1,1,1,1] row_mask:0xf bank_mask:0xf
v_mul_f32_dpp v16, v16, v2 quad_perm:[2,2,2,2] row_mask:0xf bank_mask:0xf
v_mul_f32_dpp v17, v17, v2 quad_perm:[3,3,3,3] row_mask:0xf bank_mask:0xf
v_add_f32 v14, v14, v15
v_add_f32 v16, v16, v17
v_add_f32 v14, v14, v8
v_add_f32 v7, v14, v16
v_cmp_lt_f32 vcc, 0, v7
s_cbranch_vccnz Lnc_155_%=
s_cmp_lg_u32 s81, 0
s_cbranch_scc1 Lnc_155_%=
Lns_154_%=:
v_exp_f32 v7, v7
s_nop 0
v_add_f32 v9, v9, v7
v_cvt_pk_bf16_f32 v10, v7, v7
s_waitcnt vmcnt(8)
s_nop 1
v_and_b32_dpp v14, v10, v3 row_newbcast:0 row_mask:0xf bank_mask:0xf
v_and_b32_dpp v15, v10, v4 row_newbcast:0 row_mask:0xf bank_mask:0xf
v_and_b32_dpp v16, v10, v5 row_newbcast:0 row_mask:0xf bank_mask:0xf
v_and_b32_dpp v17, v10, v6 row_newbcast:0 row_mask:0xf bank_mask:0xf
s_nop 1
v_mfma_f32_16x16x32_bf16 v[18:21], v[208:211], v[14:17], v[18:21]
v_and_b32_dpp v14, v10, v3 row_newbcast:4 row_mask:0xf bank_mask:0xf
v_and_b32_dpp v15, v10, v4 row_newbcast:4 row_mask:0xf bank_mask:0xf
v_and_b32_dpp v16, v10, v5 row_newbcast:4 row_mask:0xf bank_mask:0xf
v_and_b32_dpp v17, v10, v6 row_newbcast:4 row_mask:0xf bank_mask:0xf
s_nop 1
v_mfma_f32_16x16x32_bf16 v[18:21], v[212:215], v[14:17], v[18:21]
v_and_b32_dpp v14, v10, v3 row_newbcast:8 row_mask:0xf bank_mask:0xf
v_and_b32_dpp v15, v10, v4 row_newbcast:8 row_mask:0xf bank_mask:0xf
v_and_b32_dpp v16, v10, v5 row_newbcast:8 row_mask:0xf bank_mask:0xf
v_and_b32_dpp v17, v10, v6 row_newbcast:8 row_mask:0xf bank_mask:0xf
s_nop 1
v_mfma_f32_16x16x32_bf16 v[18:21], v[216:219], v[14:17], v[18:21]
v_and_b32_dpp v14, v10, v3 row_newbcast:12 row_mask:0xf bank_mask:0xf
v_and_b32_dpp v15, v10, v4 row_newbcast:12 row_mask:0xf bank_mask:0xf
v_and_b32_dpp v16, v10, v5 row_newbcast:12 row_mask:0xf bank_mask:0xf
v_and_b32_dpp v17, v10, v6 row_newbcast:12 row_mask:0xf bank_mask:0xf
s_nop 1
v_mfma_f32_16x16x32_bf16 v[18:21], v[220:223], v[14:17], v[18:21]
s_waitcnt vmcnt(4)
v_mfma_f32_16x16x32_bf16 v[14:17], v[224:227], v[22:25], 0
v_mfma_f32_16x16x32_bf16 v[14:17], v[228:231], v[26:29], v[14:17]
v_mfma_f32_16x16x32_bf16 v[14:17], v[232:235], v[30:33], v[14:17]
v_mfma_f32_16x16x32_bf16 v[14:17], v[236:239], v[34:37], v[14:17]
s_nop 7
v_mul_f32_dpp v14, v14, v2 quad_perm:[0,0,0,0] row_mask:0xf bank_mask:0xf
v_mul_f32_dpp v15, v15, v2 quad_perm:[1,1,1,1] row_mask:0xf bank_mask:0xf
v_mul_f32_dpp v16, v16, v2 quad_perm:[2,2,2,2] row_mask:0xf bank_mask:0xf
v_mul_f32_dpp v17, v17, v2 quad_perm:[3,3,3,3] row_mask:0xf bank_mask:0xf
v_add_f32 v14, v14, v15
v_add_f32 v16, v16, v17
v_add_f32 v14, v14, v8
v_add_f32 v7, v14, v16
v_cmp_lt_f32 vcc, 0, v7
s_cbranch_vccnz Lnc_157_%=
s_cmp_lg_u32 s81, 0
s_cbranch_scc1 Lnc_157_%=
Lns_156_%=:
v_exp_f32 v7, v7
s_nop 0
v_add_f32 v9, v9, v7
v_cvt_pk_bf16_f32 v10, v7, v7
s_waitcnt vmcnt(0)
s_nop 1
v_and_b32_dpp v14, v10, v3 row_newbcast:0 row_mask:0xf bank_mask:0xf
v_and_b32_dpp v15, v10, v4 row_newbcast:0 row_mask:0xf bank_mask:0xf
v_and_b32_dpp v16, v10, v5 row_newbcast:0 row_mask:0xf bank_mask:0xf
v_and_b32_dpp v17, v10, v6 row_newbcast:0 row_mask:0xf bank_mask:0xf
s_nop 1
v_mfma_f32_16x16x32_bf16 v[18:21], v[240:243], v[14:17], v[18:21]
v_and_b32_dpp v14, v10, v3 row_newbcast:4 row_mask:0xf bank_mask:0xf
v_and_b32_dpp v15, v10, v4 row_newbcast:4 row_mask:0xf bank_mask:0xf
v_and_b32_dpp v16, v10, v5 row_newbcast:4 row_mask:0xf bank_mask:0xf
v_and_b32_dpp v17, v10, v6 row_newbcast:4 row_mask:0xf bank_mask:0xf
s_nop 1
v_mfma_f32_16x16x32_bf16 v[18:21], v[244:247], v[14:17], v[18:21]
v_and_b32_dpp v14, v10, v3 row_newbcast:8 row_mask:0xf bank_mask:0xf
v_and_b32_dpp v15, v10, v4 row_newbcast:8 row_mask:0xf bank_mask:0xf
v_and_b32_dpp v16, v10, v5 row_newbcast:8 row_mask:0xf bank_mask:0xf
v_and_b32_dpp v17, v10, v6 row_newbcast:8 row_mask:0xf bank_mask:0xf
s_nop 1
v_mfma_f32_16x16x32_bf16 v[18:21], v[248:251], v[14:17], v[18:21]
v_and_b32_dpp v14, v10, v3 row_newbcast:12 row_mask:0xf bank_mask:0xf
v_and_b32_dpp v15, v10, v4 row_newbcast:12 row_mask:0xf bank_mask:0xf
v_and_b32_dpp v16, v10, v5 row_newbcast:12 row_mask:0xf bank_mask:0xf
v_and_b32_dpp v17, v10, v6 row_newbcast:12 row_mask:0xf bank_mask:0xf
s_nop 1
v_mfma_f32_16x16x32_bf16 v[18:21], v[252:255], v[14:17], v[18:21]
s_nop 7
v_add_f32_dpp v10, v9, v9 row_ror:4 row_mask:0xf bank_mask:0xf
ds_read_b128 v[14:17], v11
s_nop 1
v_add_f32_dpp v10, v10, v10 row_ror:8 row_mask:0xf bank_mask:0xf
s_nop 0
v_readlane_b32 s50, v10, 0
v_readlane_b32 s51, v10, 16
v_readlane_b32 s52, v10, 32
v_readlane_b32 s53, v10, 48
s_nop 1
v_mov_b32 v9, s50
v_add_f32 v9, s51, v9
v_add_f32 v9, s52, v9
v_add_f32 v9, s53, v9
v_lshrrev_b32 v12, 8, v1
v_lshlrev_b32 v12, 7, v12
v_lshrrev_b32 v13, 2, v1
v_and_b32 v13, 60, v13
v_add3_u32 v12, v12, v13, s45
s_waitcnt lgkmcnt(0)
s_mov_b32 exec_lo, 0x00ff00ff
s_mov_b32 exec_hi, 0x00ff00ff
ds_write_b32 v12, v18 offset:0
ds_write_b32 v12, v19 offset:32
ds_write_b32 v12, v20 offset:64
ds_write_b32 v12, v21 offset:96
s_mov_b64 exec, -1
v_mov_b32 v13, s45
ds_write_b64 v13, v[8:9] offset:512
s_waitcnt lgkmcnt(0)
s_barrier
v_mov_b32 v12, s55
s_add_u32 s50, s55, 81920
v_mov_b32 v13, s50
ds_read_b64 v[18:19], v12 offset:512
ds_read_b64 v[20:21], v12 offset:41472
ds_read_b64 v[30:31], v13 offset:512
ds_read_b64 v[32:33], v13 offset:41472
s_lshl_b32 s50, s46, 7
v_lshrrev_b32 v7, 1, v1
v_add3_u32 v12, v12, v7, s50
v_add3_u32 v13, v13, v7, s50
ds_read_b64 v[22:23], v12
ds_read_b64 v[24:25], v12 offset:40960
ds_read_b64 v[26:27], v13
ds_read_b64 v[28:29], v13 offset:40960
s_waitcnt lgkmcnt(0)
s_barrier
ds_write_b128 v11, v[14:17]
v_min3_f32 v7, v18, v20, v30
v_min_f32 v7, v7, v32
v_sub_f32 v18, v7, v18
v_sub_f32 v20, v7, v20
v_sub_f32 v30, v7, v30
v_sub_f32 v32, v7, v32
v_exp_f32 v18, v18
v_exp_f32 v20, v20
v_exp_f32 v30, v30
v_exp_f32 v32, v32
s_nop 0
v_mul_f32 v10, v18, v19
v_fmac_f32 v10, v20, v21
v_fmac_f32 v10, v30, v31
v_fmac_f32 v10, v32, v33
v_mul_f32 v34, v18, v22
v_mul_f32 v36, v18, v23
v_fmac_f32 v34, v20, v24
v_fmac_f32 v36, v20, v25
v_fmac_f32 v34, v30, v26
v_fmac_f32 v36, v30, v27
v_fmac_f32 v34, v32, v28
v_fmac_f32 v36, v32, v29
v_sub_f32 v7, 0, v7
v_cmp_eq_u32 vcc, 0x100, v1
s_nop 1
v_cndmask_b32 v34, v34, v7, vcc
v_cndmask_b32 v36, v36, v10, vcc
s_add_u32 s31, s30, 2
v_mov_b32 v35, s31
v_mov_b32 v37, s31
s_add_u32 s50, s47, 224
s_lshl_b32 s51, s50, 10
s_lshl_b32 s52, s46, 8
s_add_u32 s51, s51, s52
s_add_u32 s51, s51, 131072
s_lshl_b32 s52, s50, 6
s_lshl_b32 s53, s46, 4
s_add_u32 s52, s52, s53
s_add_u32 s52, s52, 1114112
v_add_u32 v12, s51, v1
v_mov_b32 v13, s52
v_cndmask_b32 v12, v12, v13, vcc
s_mov_b32 exec_lo, 0x1ffff
s_mov_b32 exec_hi, 0
buffer_store_dwordx4 v[34:37], v12, s[24:27], 0 offen sc1
s_mov_b64 exec, -1
s_waitcnt lgkmcnt(0)
s_nop 1
; ==== C: second reading of the streamed tiles (batch 0, then batch 1); the retained tiles of batch 1 are consumed in between
s_nop 7
v_mov_b32 v18, v40
v_mov_b32 v19, v41
v_mov_b32 v20, v42
v_mov_b32 v21, v43
v_mov_b32 v9, v44
v_mov_b32 v8, v45
s_nop 1
s_mov_b32 s81, 0
v_cndmask_b32_e64 v22, 0, v38, s[36:37]
v_cndmask_b32_e64 v23, 0, v39, s[36:37]
v_cndmask_b32_e64 v24, 0, v46, s[36:37]
v_cndmask_b32_e64 v25, 0, v47, s[36:37]
v_cndmask_b32_e64 v26, 0, v38, s[38:39]
v_cndmask_b32_e64 v27, 0, v39, s[38:39]
v_cndmask_b32_e64 v28, 0, v46, s[38:39]
v_cndmask_b32_e64 v29, 0, v47, s[38:39]
v_cndmask_b32_e64 v30, 0, v38, s[40:41]
v_cndmask_b32_e64 v31, 0, v39, s[40:41]
v_cndmask_b32_e64 v32, 0, v46, s[40:41]
v_cndmask_b32_e64 v33, 0, v47, s[40:41]
v_cndmask_b32_e64 v34, 0, v38, s[42:43]
v_cndmask_b32_e64 v35, 0, v39, s[42:43]
v_cndmask_b32_e64 v36, 0, v46, s[42:43]
v_cndmask_b32_e64 v37, 0, v47, s[42:43]
v_and_b32 v12, 0xff, v0
v_lshlrev_b32 v12, 1, v12
s_lshl_b32 s50, s49, 9
s_add_u32 s50, s50, 53248
v_add_u32 v12, s50, v12
v_mov_b32 v39, v12
Lc_loop_%=:
s_waitcnt vmcnt(8)
s_cmp_lg_u32 s77, s80
s_cbranch_scc1 Losw_159_%=
Lnsw_158_%=:
v_mfma_f32_16x16x32_bf16 v[14:17], v[48:51], v[22:25], 0
v_mfma_f32_16x16x32_bf16 v[14:17], v[52:55], v[26:29], v[14:17]
v_mfma_f32_16x16x32_bf16 v[14:17], v[56:59], v[30:33], v[14:17]
v_mfma_f32_16x16x32_bf16 v[14:17], v[60:63], v[34:37], v[14:17]
s_cmp_eq_u32 s78, 1
s_cselect_b32 s60, s20, s12
s_cselect_b32 s61, s21, s13
s_mov_b32 s62, 0x400000
buffer_load_dwordx4 v[48:51], v1, s[60:63], s71 offen offset:0 nt
buffer_load_dwordx4 v[52:55], v1, s[60:63], s71 offen offset:1024 nt
buffer_load_dwordx4 v[56:59], v1, s[60:63], s71 offen offset:2048 nt
buffer_load_dwordx4 v[60:63], v1, s[60:63], s71 offen offset:3072 nt
s_cmp_lt_u32 s76, 464
s_cselect_b32 s79, 0, 1
s_cselect_b32 s50, 560, 40
s_add_u32 s50, s50, s76
s_lshl_b32 s72, s50, 12
s_cmp_lt_u32 s76, 984
s_cselect_b32 s79, s79, 2
s_add_u32 s76, s76, 28
s_cmp_lg_u32 s79, s78
s_cbranch_scc1 Lso_162_%=
Lsb_161_%=:
v_mul_f32_dpp v14, v14, v2 quad_perm:[0,0,0,0] row_mask:0xf bank_mask:0xf
v_mul_f32_dpp v15, v15, v2 quad_perm:[1,1,1,1] row_mask:0xf bank_mask:0xf
v_mul_f32_dpp v16, v16, v2 quad_perm:[2,2,2,2] row_mask:0xf bank_mask:0xf
v_mul_f32_dpp v17, v17, v2 quad_perm:[3,3,3,3] row_mask:0xf bank_mask:0xf
v_add_f32 v14, v14, v15
v_add_f32 v16, v16, v17
v_add_f32 v14, v14, v8
v_add_f32 v7, v14, v16
v_cmp_lt_f32 vcc, 0, v7
s_cbranch_vccnz Lnc_165_%=
s_cmp_lg_u32 s81, 0
s_cbranch_scc1 Lnc_165_%=
Lns_164_%=:
v_exp_f32 v7, v7
s_nop 0
v_add_f32 v9, v9, v7
v_cvt_pk_bf16_f32 v10, v7, v7
s_waitcnt vmcnt(8)
s_nop 1
v_and_b32_dpp v14, v10, v3 row_newbcast:0 row_mask:0xf bank_mask:0xf
v_and_b32_dpp v15, v10, v4 row_newbcast:0 row_mask:0xf bank_mask:0xf
v_and_b32_dpp v16, v10, v5 row_newbcast:0 row_mask:0xf bank_mask:0xf
v_and_b32_dpp v17, v10, v6 row_newbcast:0 row_mask:0xf bank_mask:0xf
s_nop 1
v_mfma_f32_16x16x32_bf16 v[18:21], v[64:67], v[14:17], v[18:21]
v_and_b32_dpp v14, v10, v3 row_newbcast:4 row_mask:0xf bank_mask:0xf
v_and_b32_dpp v15, v10, v4 row_newbcast:4 row_mask:0xf bank_mask:0xf
v_and_b32_dpp v16, v10, v5 row_newbcast:4 row_mask:0xf bank_mask:0xf
v_and_b32_dpp v17, v10, v6 row_newbcast:4 row_mask:0xf bank_mask:0xf
s_nop 1
v_mfma_f32_16x16x32_bf16 v[18:21], v[68:71], v[14:17], v[18:21]
v_and_b32_dpp v14, v10, v3 row_newbcast:8 row_mask:0xf bank_mask:0xf
v_and_b32_dpp v15, v10, v4 row_newbcast:8 row_mask:0xf bank_mask:0xf
v_and_b32_dpp v16, v10, v5 row_newbcast:8 row_mask:0xf bank_mask:0xf
v_and_b32_dpp v17, v10, v6 row_newbcast:8 row_mask:0xf bank_mask:0xf
s_nop 1
v_mfma_f32_16x16x32_bf16 v[18:21], v[72:75], v[14:17], v[18:21]
v_and_b32_dpp v14, v10, v3 row_newbcast:12 row_mask:0xf bank_mask:0xf
v_and_b32_dpp v15, v10, v4 row_newbcast:12 row_mask:0xf bank_mask:0xf
v_and_b32_dpp v16, v10, v5 row_newbcast:12 row_mask:0xf bank_mask:0xf
v_and_b32_dpp v17, v10, v6 row_newbcast:12 row_mask:0xf bank_mask:0xf
s_nop 1
v_mfma_f32_16x16x32_bf16 v[18:21], v[76:79], v[14:17], v[18:21]
s_cmp_eq_u32 s79, 2
s_cbranch_scc1 Lc_x1_%=
s_cmp_eq_u32 s79, 1
s_cselect_b32 s56, s16, s8
s_cselect_b32 s57, s17, s9
s_mov_b32 s58, 0x400000
buffer_load_dwordx4 v[64:67], v0, s[56:59], s72 offen offset:0 nt
buffer_load_dwordx4 v[68:71], v0, s[56:59], s72 offen offset:1024 nt
buffer_load_dwordx4 v[72:75], v0, s[56:59], s72 offen offset:2048 nt
buffer_load_dwordx4 v[76:79], v0, s[56:59], s72 offen offset:3072 nt
s_cmp_lg_u32 s82, 0
s_cbranch_scc1 Loco_167_%=
Lnco_166_%=:
s_waitcnt vmcnt(8)
s_cmp_lg_u32 s78, s80
s_cbranch_scc1 Losw_169_%=
Lnsw_168_%=:
v_mfma_f32_16x16x32_bf16 v[14:17], v[80:83], v[22:25], 0
v_mfma_f32_16x16x32_bf16 v[14:17], v[84:87], v[26:29], v[14:17]
v_mfma_f32_16x16x32_bf16 v[14:17], v[88:91], v[30:33], v[14:17]
v_mfma_f32_16x16x32_bf16 v[14:17], v[92:95], v[34:37], v[14:17]
s_cmp_eq_u32 s79, 1
s_cselect_b32 s60, s20, s12
s_cselect_b32 s61, s21, s13
s_mov_b32 s62, 0x400000
buffer_load_dwordx4 v[80:83], v1, s[60:63], s72 offen offset:0 nt
buffer_load_dwordx4 v[84:87], v1, s[60:63], s72 offen offset:1024 nt
buffer_load_dwordx4 v[88:91], v1, s[60:63], s72 offen offset:2048 nt
buffer_load_dwordx4 v[92:95], v1, s[60:63], s72 offen offset:3072 nt
s_cmp_lt_u32 s76, 464
s_cselect_b32 s77, 0, 1
s_cselect_b32 s50, 560, 40
s_add_u32 s50, s50, s76
s_lshl_b32 s70, s50, 12
s_cmp_lt_u32 s76, 984
s_cselect_b32 s77, s77, 2
s_add_u32 s76, s76, 28
s_cmp_lg_u32 s77, s79
s_cbranch_scc1 Lso_172_%=
Lsb_171_%=:
v_mul_f32_dpp v14, v14, v2 quad_perm:[0,0,0,0] row_mask:0xf bank_mask:0xf
v_mul_f32_dpp v15, v15, v2 quad_perm:[1,1,1,1] row_mask:0xf bank_mask:0xf
v_mul_f32_dpp v16, v16, v2 quad_perm:[2,2,2,2] row_mask:0xf bank_mask:0xf
v_mul_f32_dpp v17, v17, v2 quad_perm:[3,3,3,3] row_mask:0xf bank_mask:0xf
v_add_f32 v14, v14, v15
v_add_f32 v16, v16, v17
v_add_f32 v14, v14, v8
v_add_f32 v7, v14, v16
v_cmp_lt_f32 vcc, 0, v7
s_cbranch_vccnz Lnc_175_%=
s_cmp_lg_u32 s81, 0
s_cbranch_scc1 Lnc_175_%=
Lns_174_%=:
v_exp_f32 v7, v7
s_nop 0
v_add_f32 v9, v9, v7
v_cvt_pk_bf16_f32 v10, v7, v7
s_waitcnt vmcnt(8)
s_nop 1
v_and_b32_dpp v14, v10, v3 row_newbcast:0 row_mask:0xf bank_mask:0xf
v_and_b32_dpp v15, v10, v4 row_newbcast:0 row_mask:0xf bank_mask:0xf
v_and_b32_dpp v16, v10, v5 row_newbcast:0 row_mask:0xf bank_mask:0xf
v_and_b32_dpp v17, v10, v6 row_newbcast:0 row_mask:0xf bank_mask:0xf
s_nop 1
v_mfma_f32_16x16x32_bf16 v[18:21], v[48:51], v[14:17], v[18:21]
v_and_b32_dpp v14, v10, v3 row_newbcast:4 row_mask:0xf bank_mask:0xf
v_and_b32_dpp v15, v10, v4 row_newbcast:4 row_mask:0xf bank_mask:0xf
v_and_b32_dpp v16, v10, v5 row_newbcast:4 row_mask:0xf bank_mask:0xf
v_and_b32_dpp v17, v10, v6 row_newbcast:4 row_mask:0xf bank_mask:0xf
s_nop 1
v_mfma_f32_16x16x32_bf16 v[18:21], v[52:55], v[14:17], v[18:21]
v_and_b32_dpp v14, v10, v3 row_newbcast:8 row_mask:0xf bank_mask:0xf
v_and_b32_dpp v15, v10, v4 row_newbcast:8 row_mask:0xf bank_mask:0xf
v_and_b32_dpp v16, v10, v5 row_newbcast:8 row_mask:0xf bank_mask:0xf
v_and_b32_dpp v17, v10, v6 row_newbcast:8 row_mask:0xf bank_mask:0xf
s_nop 1
v_mfma_f32_16x16x32_bf16 v[18:21], v[56:59], v[14:17], v[18:21]
v_and_b32_dpp v14, v10, v3 row_newbcast:12 row_mask:0xf bank_mask:0xf
v_and_b32_dpp v15, v10, v4 row_newbcast:12 row_mask:0xf bank_mask:0xf
v_and_b32_dpp v16, v10, v5 row_newbcast:12 row_mask:0xf bank_mask:0xf
v_and_b32_dpp v17, v10, v6 row_newbcast:12 row_mask:0xf bank_mask:0xf
s_nop 1
v_mfma_f32_16x16x32_bf16 v[18:21], v[60:63], v[14:17], v[18:21]
s_cmp_eq_u32 s77, 2
s_cbranch_scc1 Lc_x2_%=
s_cmp_eq_u32 s77, 1
s_cselect_b32 s56, s16, s8
s_cselect_b32 s57, s17, s9
s_mov_b32 s58, 0x400000
buffer_load_dwordx4 v[48:51], v0, s[56:59], s70 offen offset:0 nt
buffer_load_dwordx4 v[52:55], v0, s[56:59], s70 offen offset:1024 nt
buffer_load_dwordx4 v[56:59], v0, s[56:59], s70 offen offset:2048 nt
buffer_load_dwordx4 v[60:63], v0, s[56:59], s70 offen offset:3072 nt
s_cmp_lg_u32 s82, 0
s_cbranch_scc1 Loco_177_%=
Lnco_176_%=:
s_waitcnt vmcnt(8)
s_cmp_lg_u32 s79, s80
s_cbranch_scc1 Losw_179_%=
Lnsw_178_%=:
v_mfma_f32_16x16x32_bf16 v[14:17], v[64:67], v[22:25], 0
v_mfma_f32_16x16x32_bf16 v[14:17], v[68:71], v[26:29], v[14:17]
v_mfma_f32_16x16x32_bf16 v[14:17], v[72:75], v[30:33], v[14:17]
v_mfma_f32_16x16x32_bf16 v[14:17], v[76:79], v[34:37], v[14:17]
s_cmp_eq_u32 s77, 1
s_cselect_b32 s60, s20, s12
s_cselect_b32 s61, s21, s13
s_mov_b32 s62, 0x400000
buffer_load_dwordx4 v[64:67], v1, s[60:63], s70 offen offset:0 nt
buffer_load_dwordx4 v[68:71], v1, s[60:63], s70 offen offset:1024 nt
buffer_load_dwordx4 v[72:75], v1, s[60:63], s70 offen offset:2048 nt
buffer_load_dwordx4 v[76:79], v1, s[60:63], s70 offen offset:3072 nt
s_cmp_lt_u32 s76, 464
s_cselect_b32 s78, 0, 1
s_cselect_b32 s50, 560, 40
s_add_u32 s50, s50, s76
s_lshl_b32 s71, s50, 12
s_cmp_lt_u32 s76, 984
s_cselect_b32 s78, s78, 2
s_add_u32 s76, s76, 28
s_cmp_lg_u32 s78, s77
s_cbranch_scc1 Lso_182_%=
Lsb_181_%=:
v_mul_f32_dpp v14, v14, v2 quad_perm:[0,0,0,0] row_mask:0xf bank_mask:0xf
v_mul_f32_dpp v15, v15, v2 quad_perm:[1,1,1,1] row_mask:0xf bank_mask:0xf
v_mul_f32_dpp v16, v16, v2 quad_perm:[2,2,2,2] row_mask:0xf bank_mask:0xf
v_mul_f32_dpp v17, v17, v2 quad_perm:[3,3,3,3] row_mask:0xf bank_mask:0xf
v_add_f32 v14, v14, v15
v_add_f32 v16, v16, v17
v_add_f32 v14, v14, v8
v_add_f32 v7, v14, v16
v_cmp_lt_f32 vcc, 0, v7
s_cbranch_vccnz Lnc_185_%=
s_cmp_lg_u32 s81, 0
s_cbranch_scc1 Lnc_185_%=
Lns_184_%=:
v_exp_f32 v7, v7
s_nop 0
v_add_f32 v9, v9, v7
v_cvt_pk_bf16_f32 v10, v7, v7
s_waitcnt vmcnt(8)
s_nop 1
v_and_b32_dpp v14, v10, v3 row_newbcast:0 row_mask:0xf bank_mask:0xf
v_and_b32_dpp v15, v10, v4 row_newbcast:0 row_mask:0xf bank_mask:0xf
v_and_b32_dpp v16, v10, v5 row_newbcast:0 row_mask:0xf bank_mask:0xf
v_and_b32_dpp v17, v10, v6 row_newbcast:0 row_mask:0xf bank_mask:0xf
s_nop 1
v_mfma_f32_16x16x32_bf16 v[18:21], v[80:83], v[14:17], v[18:21]
v_and_b32_dpp v14, v10, v3 row_newbcast:4 row_mask:0xf bank_mask:0xf
v_and_b32_dpp v15, v10, v4 row_newbcast:4 row_mask:0xf bank_mask:0xf
v_and_b32_dpp v16, v10, v5 row_newbcast:4 row_mask:0xf bank_mask:0xf
v_and_b32_dpp v17, v10, v6 row_newbcast:4 row_mask:0xf bank_mask:0xf
s_nop 1
v_mfma_f32_16x16x32_bf16 v[18:21], v[84:87], v[14:17], v[18:21]
v_and_b32_dpp v14, v10, v3 row_newbcast:8 row_mask:0xf bank_mask:0xf
v_and_b32_dpp v15, v10, v4 row_newbcast:8 row_mask:0xf bank_mask:0xf
v_and_b32_dpp v16, v10, v5 row_newbcast:8 row_mask:0xf bank_mask:0xf
v_and_b32_dpp v17, v10, v6 row_newbcast:8 row_mask:0xf bank_mask:0xf
s_nop 1
v_mfma_f32_16x16x32_bf16 v[18:21], v[88:91], v[14:17], v[18:21]
v_and_b32_dpp v14, v10, v3 row_newbcast:12 row_mask:0xf bank_mask:0xf
v_and_b32_dpp v15, v10, v4 row_newbcast:12 row_mask:0xf bank_mask:0xf
v_and_b32_dpp v16, v10, v5 row_newbcast:12 row_mask:0xf bank_mask:0xf
v_and_b32_dpp v17, v10, v6 row_newbcast:12 row_mask:0xf bank_mask:0xf
s_nop 1
v_mfma_f32_16x16x32_bf16 v[18:21], v[92:95], v[14:17], v[18:21]
s_cmp_eq_u32 s78, 2
s_cbranch_scc1 Lc_x0_%=
s_cmp_eq_u32 s78, 1
s_cselect_b32 s56, s16, s8
s_cselect_b32 s57, s17, s9
s_mov_b32 s58, 0x400000
buffer_load_dwordx4 v[80:83], v0, s[56:59], s71 offen offset:0 nt
buffer_load_dwordx4 v[84:87], v0, s[56:59], s71 offen offset:1024 nt
buffer_load_dwordx4 v[88:91], v0, s[56:59], s71 offen offset:2048 nt
buffer_load_dwordx4 v[92:95], v0, s[56:59], s71 offen offset:3072 nt
s_cmp_lg_u32 s82, 0
s_cbranch_scc1 Loco_187_%=
Lnco_186_%=:
s_branch Lc_loop_%=
Lc_x0_%=:
s_mov_b32 s83, s77
s_getpc_b64 s[88:89]
Lpc_188_%=:
s_add_u32 s88, s88, LPEND0_%=-Lpc_188_%=
s_addc_u32 s89, s89, 0
s_branch Lc_exit_%=
Lc_x1_%=:
s_mov_b32 s83, s78
s_getpc_b64 s[88:89]
Lpc_189_%=:
s_add_u32 s88, s88, LPEND1_%=-Lpc_189_%=
s_addc_u32 s89, s89, 0
s_branch Lc_exit_%=
Lc_x2_%=:
s_mov_b32 s83, s79
s_getpc_b64 s[88:89]
Lpc_190_%=:
s_add_u32 s88, s88, LPEND2_%=-Lpc_190_%=
s_addc_u32 s89, s89, 0
s_branch Lc_exit_%=
Lc_exit_%=:
s_waitcnt vmcnt(0)
s_cmp_eq_u32 s83, s80
s_cbranch_scc1 Lcs_191_%=
s_getpc_b64 s[90:91]
Lpc_192_%=:
s_add_u32 s90, s90, LSW3_%=-Lpc_192_%=
s_addc_u32 s91, s91, 0
s_swappc_b64 s[86:87], s[90:91]
Lcs_191_%=:
s_swappc_b64 s[86:87], s[88:89]
s_cmp_eq_u32 s80, 0
s_cbranch_scc0 Lct_193_%=
s_getpc_b64 s[90:91]
Lpc_194_%=:
s_add_u32 s90, s90, LSW3_%=-Lpc_194_%=
s_addc_u32 s91, s91, 0
s_swappc_b64 s[86:87], s[90:91]
Lct_193_%=:
Lcl_195_%=:
s_cmp_eq_u32 s82, 0
s_cbranch_scc1 Lcx_196_%=
s_swappc_b64 s[86:87], s[84:85]
s_branch Lcl_195_%=
Lcx_196_%=:
s_nop 7
v_add_f32_dpp v10, v9, v9 row_ror:4 row_mask:0xf bank_mask:0xf
s_nop 1
v_add_f32_dpp v10, v10, v10 row_ror:8 row_mask:0xf bank_mask:0xf
s_nop 0
v_readlane_b32 s50, v10, 0
v_readlane_b32 s51, v10, 16
v_readlane_b32 s52, v10, 32
v_readlane_b32 s53, v10, 48
s_nop 1
v_mov_b32 v9, s50
v_add_f32 v9, s51, v9
v_add_f32 v9, s52, v9
v_add_f32 v9, s53, v9
v_lshrrev_b32 v12, 8, v1
v_lshlrev_b32 v12, 7, v12
v_lshrrev_b32 v13, 2, v1
v_and_b32 v13, 60, v13
v_add3_u32 v12, v12, v13, s45
s_waitcnt lgkmcnt(0)
s_mov_b32 exec_lo, 0x00ff00ff
s_mov_b32 exec_hi, 0x00ff00ff
ds_write_b32 v12, v18 offset:0
ds_write_b32 v12, v19 offset:32
ds_write_b32 v12, v20 offset:64
ds_write_b32 v12, v21 offset:96
s_mov_b64 exec, -1
v_mov_b32 v13, s45
ds_write_b64 v13, v[8:9] offset:512
s_waitcnt lgkmcnt(0)
s_barrier
v_mov_b32 v12, s55
s_add_u32 s50, s55, 81920
v_mov_b32 v13, s50
ds_read_b64 v[18:19], v12 offset:512
ds_read_b64 v[20:21], v12 offset:41472
ds_read_b64 v[30:31], v13 offset:512
ds_read_b64 v[32:33], v13 offset:41472
s_lshl_b32 s50, s46, 7
v_lshrrev_b32 v7, 1, v1
v_add3_u32 v12, v12, v7, s50
v_add3_u32 v13, v13, v7, s50
ds_read_b64 v[22:23], v12
ds_read_b64 v[24:25], v12 offset:40960
ds_read_b64 v[26:27], v13
ds_read_b64 v[28:29], v13 offset:40960
s_waitcnt lgkmcnt(0)
v_min3_f32 v7, v18, v20, v30
v_min_f32 v7, v7, v32
v_sub_f32 v18, v7, v18
v_sub_f32 v20, v7, v20
v_sub_f32 v30, v7, v30
v_sub_f32 v32, v7, v32
v_exp_f32 v18, v18
v_exp_f32 v20, v20
v_exp_f32 v30, v30
v_exp_f32 v32, v32
s_nop 0
v_mul_f32 v10, v18, v19
v_fmac_f32 v10, v20, v21
v_fmac_f32 v10, v30, v31
v_fmac_f32 v10, v32, v33
v_mul_f32 v34, v18, v22
v_mul_f32 v36, v18, v23
v_fmac_f32 v34, v20, v24
v_fmac_f32 v36, v20, v25
v_fmac_f32 v34, v30, v26
v_fmac_f32 v36, v30, v27
v_fmac_f32 v34, v32, v28
v_fmac_f32 v36, v32, v29
v_sub_f32 v7, 0, v7
v_cmp_eq_u32 vcc, 0x100, v1
s_nop 1
v_cndmask_b32 v34, v34, v7, vcc
v_cndmask_b32 v36, v36, v10, vcc
s_add_u32 s31, s30, 4
v_mov_b32 v35, s31
v_mov_b32 v37, s31
s_add_u32 s50, s47, 672
s_lshl_b32 s51, s50, 10
s_lshl_b32 s52, s46, 8
s_add_u32 s51, s51, s52
s_add_u32 s51, s51, 131072
s_lshl_b32 s52, s50, 6
s_lshl_b32 s53, s46, 4
s_add_u32 s52, s52, s53
s_add_u32 s52, s52, 1114112
v_add_u32 v12, s51, v1
v_mov_b32 v13, s52
v_cndmask_b32 v12, v12, v13, vcc
s_mov_b32 exec_lo, 0x1ffff
s_mov_b32 exec_hi, 0
buffer_store_dwordx4 v[34:37], v12, s[24:27], 0 offen sc1
s_mov_b64 exec, -1
s_nop 1
s_waitcnt vmcnt(0)
s_branch LEND_%=
; ==== out-of-line pieces of the code above
Lnc_4_%=:
s_mov_b32 s81, 0
s_swappc_b64 s[66:67], s[64:65]
s_branch Lns_3_%=
Lnc_6_%=:
s_mov_b32 s81, 0
s_swappc_b64 s[66:67], s[64:65]
s_branch Lns_5_%=
Lnc_8_%=:
s_mov_b32 s81, 0
s_swappc_b64 s[66:67], s[64:65]
s_branch Lns_7_%=
Lnc_10_%=:
s_mov_b32 s81, 0
s_swappc_b64 s[66:67], s[64:65]
s_branch Lns_9_%=
Lnc_12_%=:
s_mov_b32 s81, 0
s_swappc_b64 s[66:67], s[64:65]
s_branch Lns_11_%=
Lnc_14_%=:
s_mov_b32 s81, 0
s_swappc_b64 s[66:67], s[64:65]
s_branch Lns_13_%=
Lnc_16_%=:
s_mov_b32 s81, 0
s_swappc_b64 s[66:67], s[64:65]
s_branch Lns_15_%=
Lnc_18_%=:
s_mov_b32 s81, 0
s_swappc_b64 s[66:67], s[64:65]
s_branch Lns_17_%=
Lpo_20_%=:
buffer_load_dwordx4 v[64:67], v1, s[60:63], s70 offen offset:0 
buffer_load_dwordx4 v[68:71], v1, s[60:63], s70 offen offset:1024 
buffer_load_dwordx4 v[72:75], v1, s[60:63], s70 offen offset:2048 
buffer_load_dwordx4 v[76:79], v1, s[60:63], s70 offen offset:3072 
s_branch Lpn_19_%=
Lnc_22_%=:
s_mov_b32 s81, 0
s_swappc_b64 s[66:67], s[64:65]
s_branch Lns_21_%=
Lnc_24_%=:
s_mov_b32 s81, 0
s_swappc_b64 s[66:67], s[64:65]
s_branch Lns_23_%=
Lnc_26_%=:
s_mov_b32 s81, 0
s_swappc_b64 s[66:67], s[64:65]
s_branch Lns_25_%=
Lnc_28_%=:
s_mov_b32 s81, 0
s_swappc_b64 s[66:67], s[64:65]
s_branch Lns_27_%=
Lnc_30_%=:
s_mov_b32 s81, 0
s_swappc_b64 s[66:67], s[64:65]
s_branch Lns_29_%=
Lnc_32_%=:
s_mov_b32 s81, 0
s_swappc_b64 s[66:67], s[64:65]
s_branch Lns_31_%=
Lnc_34_%=:
s_mov_b32 s81, 0
s_swappc_b64 s[66:67], s[64:65]
s_branch Lns_33_%=
Lnc_36_%=:
s_mov_b32 s81, 0
s_swappc_b64 s[66:67], s[64:65]
s_branch Lns_35_%=
Lnc_38_%=:
s_mov_b32 s81, 0
s_swappc_b64 s[66:67], s[64:65]
s_branch Lns_37_%=
Lnc_40_%=:
s_mov_b32 s81, 0
s_swappc_b64 s[66:67], s[64:65]
s_branch Lns_39_%=
Losw_42_%=:
s_getpc_b64 s[90:91]
Lpc_43_%=:
s_add_u32 s90, s90, LSW1_%=-Lpc_43_%=
s_addc_u32 s91, s91, 0
s_swappc_b64 s[86:87], s[90:91]
s_branch Lnsw_41_%=
Lpo_45_%=:
buffer_load_dwordx4 v[48:51], v1, s[60:63], s71 offen offset:0 
buffer_load_dwordx4 v[52:55], v1, s[60:63], s71 offen offset:1024 
buffer_load_dwordx4 v[56:59], v1, s[60:63], s71 offen offset:2048 
buffer_load_dwordx4 v[60:63], v1, s[60:63], s71 offen offset:3072 
s_branch Lpn_44_%=
Lso_47_%=:
s_cmp_eq_u32 s79, 1
s_cbranch_scc0 Lq2_48_%=
buffer_load_dwordx4 v[40:43], v39, s[24:27], 0 offen sc1
buffer_load_dwordx4 v[44:47], v39, s[24:27], 0 offen offset:16 sc1
Lq2_48_%=:
s_branch Lsb_46_%=
Lso_50_%=:
s_cmp_eq_u32 s79, 2
s_cbranch_scc1 Lq3_51_%=
v_and_b32 v12, 0xff, v0
v_lshlrev_b32 v12, 1, v12
s_lshl_b32 s50, s49, 9
s_add_u32 s50, s50, 36864
v_add_u32 v12, s50, v12
v_mov_b32 v39, v12
buffer_load_dwordx4 v[40:43], v39, s[24:27], 0 offen sc1
buffer_load_dwordx4 v[44:47], v39, s[24:27], 0 offen offset:16 sc1
Lq3_51_%=:
s_branch Lsb_49_%=
Lnc_53_%=:
s_mov_b32 s81, 0
s_swappc_b64 s[66:67], s[64:65]
s_branch Lns_52_%=
Losw_55_%=:
s_getpc_b64 s[90:91]
Lpc_56_%=:
s_add_u32 s90, s90, LSW1_%=-Lpc_56_%=
s_addc_u32 s91, s91, 0
s_swappc_b64 s[86:87], s[90:91]
s_branch Lnsw_54_%=
Lpo_58_%=:
buffer_load_dwordx4 v[80:83], v1, s[60:63], s72 offen offset:0 
buffer_load_dwordx4 v[84:87], v1, s[60:63], s72 offen offset:1024 
buffer_load_dwordx4 v[88:91], v1, s[60:63], s72 offen offset:2048 
buffer_load_dwordx4 v[92:95], v1, s[60:63], s72 offen offset:3072 
s_branch Lpn_57_%=
Lso_60_%=:
s_cmp_eq_u32 s77, 1
s_cbranch_scc0 Lq2_61_%=
buffer_load_dwordx4 v[40:43], v39, s[24:27], 0 offen sc1
buffer_load_dwordx4 v[44:47], v39, s[24:27], 0 offen offset:16 sc1
Lq2_61_%=:
s_branch Lsb_59_%=
Lso_63_%=:
s_cmp_eq_u32 s77, 2
s_cbranch_scc1 Lq3_64_%=
v_and_b32 v12, 0xff, v0
v_lshlrev_b32 v12, 1, v12
s_lshl_b32 s50, s49, 9
s_add_u32 s50, s50, 36864
v_add_u32 v12, s50, v12
v_mov_b32 v39, v12
buffer_load_dwordx4 v[40:43], v39, s[24:27], 0 offen sc1
buffer_load_dwordx4 v[44:47], v39, s[24:27], 0 offen offset:16 sc1
Lq3_64_%=:
s_branch Lsb_62_%=
Lnc_66_%=:
s_mov_b32 s81, 0
s_swappc_b64 s[66:67], s[64:65]
s_branch Lns_65_%=
Losw_68_%=:
s_getpc_b64 s[90:91]
Lpc_69_%=:
s_add_u32 s90, s90, LSW1_%=-Lpc_69_%=
s_addc_u32 s91, s91, 0
s_swappc_b64 s[86:87], s[90:91]
s_branch Lnsw_67_%=
Lpo_71_%=:
buffer_load_dwordx4 v[64:67], v1, s[60:63], s70 offen offset:0 
buffer_load_dwordx4 v[68:71], v1, s[60:63], s70 offen offset:1024 
buffer_load_dwordx4 v[72:75], v1, s[60:63], s70 offen offset:2048 
buffer_load_dwordx4 v[76:79], v1, s[60:63], s70 offen offset:3072 
s_branch Lpn_70_%=
Lso_73_%=:
s_cmp_eq_u32 s78, 1
s_cbranch_scc0 Lq2_74_%=
buffer_load_dwordx4 v[40:43], v39, s[24:27], 0 offen sc1
buffer_load_dwordx4 v[44:47], v39, s[24:27], 0 offen offset:16 sc1
Lq2_74_%=:
s_branch Lsb_72_%=
Lso_76_%=:
s_cmp_eq_u32 s78, 2
s_cbranch_scc1 Lq3_77_%=
v_and_b32 v12, 0xff, v0
v_lshlrev_b32 v12, 1, v12
s_lshl_b32 s50, s49, 9
s_add_u32 s50, s50, 36864
v_add_u32 v12, s50, v12
v_mov_b32 v39, v12
buffer_load_dwordx4 v[40:43], v39, s[24:27], 0 offen sc1
buffer_load_dwordx4 v[44:47], v39, s[24:27], 0 offen offset:16 sc1
Lq3_77_%=:
s_branch Lsb_75_%=
Lnc_79_%=:
s_mov_b32 s81, 0
s_swappc_b64 s[66:67], s[64:65]
s_branch Lns_78_%=
Lnc_86_%=:
s_mov_b32 s81, 0
s_swappc_b64 s[66:67], s[64:65]
s_branch Lns_85_%=
Lnc_88_%=:
s_mov_b32 s81, 0
s_swappc_b64 s[66:67], s[64:65]
s_branch Lns_87_%=
Lnc_90_%=:
s_mov_b32 s81, 0
s_swappc_b64 s[66:67], s[64:65]
s_branch Lns_89_%=
Lnc_92_%=:
s_mov_b32 s81, 0
s_swappc_b64 s[66:67], s[64:65]
s_branch Lns_91_%=
Lnc_94_%=:
s_mov_b32 s81, 0
s_swappc_b64 s[66:67], s[64:65]
s_branch Lns_93_%=
Lnc_96_%=:
s_mov_b32 s81, 0
s_swappc_b64 s[66:67], s[64:65]
s_branch Lns_95_%=
Lnc_99_%=:
s_mov_b32 s81, 0
s_swappc_b64 s[66:67], s[64:65]
s_branch Lns_98_%=
Lnc_101_%=:
s_mov_b32 s81, 0
s_swappc_b64 s[66:67], s[64:65]
s_branch Lns_100_%=
Lnc_103_%=:
s_mov_b32 s81, 0
s_swappc_b64 s[66:67], s[64:65]
s_branch Lns_102_%=
Lnc_105_%=:
s_mov_b32 s81, 0
s_swappc_b64 s[66:67], s[64:65]
s_branch Lns_104_%=
Lnc_107_%=:
s_mov_b32 s81, 0
s_swappc_b64 s[66:67], s[64:65]
s_branch Lns_106_%=
Lnc_109_%=:
s_mov_b32 s81, 0
s_swappc_b64 s[66:67], s[64:65]
s_branch Lns_108_%=
Lnc_111_%=:
s_mov_b32 s81, 0
s_swappc_b64 s[66:67], s[64:65]
s_branch Lns_110_%=
Lnc_113_%=:
s_mov_b32 s81, 0
s_swappc_b64 s[66:67], s[64:65]
s_branch Lns_112_%=
Lnc_115_%=:
s_mov_b32 s81, 0
s_swappc_b64 s[66:67], s[64:65]
s_branch Lns_114_%=
Lnc_117_%=:
s_mov_b32 s81, 0
s_swappc_b64 s[66:67], s[64:65]
s_branch Lns_116_%=
Lnc_119_%=:
s_mov_b32 s81, 0
s_swappc_b64 s[66:67], s[64:65]
s_branch Lns_118_%=
Lnc_121_%=:
s_mov_b32 s81, 0
s_swappc_b64 s[66:67], s[64:65]
s_branch Lns_120_%=
Lnc_123_%=:
s_mov_b32 s81, 0
s_swappc_b64 s[66:67], s[64:65]
s_branch Lns_122_%=
Lnc_125_%=:
s_mov_b32 s81, 0
s_swappc_b64 s[66:67], s[64:65]
s_branch Lns_124_%=
Lnc_127_%=:
s_mov_b32 s81, 0
s_swappc_b64 s[66:67], s[64:65]
s_branch Lns_126_%=
Lnc_129_%=:
s_mov_b32 s81, 0
s_swappc_b64 s[66:67], s[64:65]
s_branch Lns_128_%=
Lnc_131_%=:
s_mov_b32 s81, 0
s_swappc_b64 s[66:67], s[64:65]
s_branch Lns_130_%=
Lnc_133_%=:
s_mov_b32 s81, 0
s_swappc_b64 s[66:67], s[64:65]
s_branch Lns_132_%=
Lnc_135_%=:
s_mov_b32 s81, 0
s_swappc_b64 s[66:67], s[64:65]
s_branch Lns_134_%=
Lnc_137_%=:
s_mov_b32 s81, 0
s_swappc_b64 s[66:67], s[64:65]
s_branch Lns_136_%=
Lnc_139_%=:
s_mov_b32 s81, 0
s_swappc_b64 s[66:67], s[64:65]
s_branch Lns_138_%=
Lnc_141_%=:
s_mov_b32 s81, 0
s_swappc_b64 s[66:67], s[64:65]
s_branch Lns_140_%=
Lnc_143_%=:
s_mov_b32 s81, 0
s_swappc_b64 s[66:67], s[64:65]
s_branch Lns_142_%=
Lnc_145_%=:
s_mov_b32 s81, 0
s_swappc_b64 s[66:67], s[64:65]
s_branch Lns_144_%=
Lnc_147_%=:
s_mov_b32 s81, 0
s_swappc_b64 s[66:67], s[64:65]
s_branch Lns_146_%=
Lnc_149_%=:
s_mov_b32 s81, 0
s_swappc_b64 s[66:67], s[64:65]
s_branch Lns_148_%=
Lnc_151_%=:
s_mov_b32 s81, 0
s_swappc_b64 s[66:67], s[64:65]
s_branch Lns_150_%=
Lnc_153_%=:
s_mov_b32 s81, 0
s_swappc_b64 s[66:67], s[64:65]
s_branch Lns_152_%=
Lnc_155_%=:
s_mov_b32 s81, 0
s_swappc_b64 s[66:67], s[64:65]
s_branch Lns_154_%=
Lnc_157_%=:
s_mov_b32 s81, 0
s_swappc_b64 s[66:67], s[64:65]
s_branch Lns_156_%=
Losw_159_%=:
s_getpc_b64 s[90:91]
Lpc_160_%=:
s_add_u32 s90, s90, LSW3_%=-Lpc_160_%=
s_addc_u32 s91, s91, 0
s_swappc_b64 s[86:87], s[90:91]
s_branch Lnsw_158_%=
Lso_162_%=:
s_cmp_eq_u32 s79, 1
s_cbranch_scc0 Lq2_163_%=
buffer_load_dwordx4 v[40:43], v39, s[24:27], 0 offen sc1
buffer_load_dwordx4 v[44:47], v39, s[24:27], 0 offen offset:16 sc1
Lq2_163_%=:
s_branch Lsb_161_%=
Lnc_165_%=:
s_mov_b32 s81, 0
s_swappc_b64 s[66:67], s[64:65]
s_branch Lns_164_%=
Loco_167_%=:
s_swappc_b64 s[86:87], s[84:85]
s_branch Lnco_166_%=
Losw_169_%=:
s_getpc_b64 s[90:91]
Lpc_170_%=:
s_add_u32 s90, s90, LSW3_%=-Lpc_170_%=
s_addc_u32 s91, s91, 0
s_swappc_b64 s[86:87], s[90:91]
s_branch Lnsw_168_%=
Lso_172_%=:
s_cmp_eq_u32 s77, 1
s_cbranch_scc0 Lq2_173_%=
buffer_load_dwordx4 v[40:43], v39, s[24:27], 0 offen sc1
buffer_load_dwordx4 v[44:47], v39, s[24:27], 0 offen offset:16 sc1
Lq2_173_%=:
s_branch Lsb_171_%=
Lnc_175_%=:
s_mov_b32 s81, 0
s_swappc_b64 s[66:67], s[64:65]
s_branch Lns_174_%=
Loco_177_%=:
s_swappc_b64 s[86:87], s[84:85]
s_branch Lnco_176_%=
Losw_179_%=:
s_getpc_b64 s[90:91]
Lpc_180_%=:
s_add_u32 s90, s90, LSW3_%=-Lpc_180_%=
s_addc_u32 s91, s91, 0
s_swappc_b64 s[86:87], s[90:91]
s_branch Lnsw_178_%=
Lso_182_%=:
s_cmp_eq_u32 s78, 1
s_cbranch_scc0 Lq2_183_%=
buffer_load_dwordx4 v[40:43], v39, s[24:27], 0 offen sc1
buffer_load_dwordx4 v[44:47], v39, s[24:27], 0 offen offset:16 sc1
Lq2_183_%=:
s_branch Lsb_181_%=
Lnc_185_%=:
s_mov_b32 s81, 0
s_swappc_b64 s[66:67], s[64:65]
s_branch Lns_184_%=
Loco_187_%=:
s_swappc_b64 s[86:87], s[84:85]
s_branch Lnco_186_%=
; ==== subroutines: context switch (publish the partial result, take the next q), pending tile, retained slots, running-max update
LSW1_%=:
s_nop 7
v_add_f32_dpp v10, v9, v9 row_ror:4 row_mask:0xf bank_mask:0xf
ds_read_b128 v[14:17], v11
s_nop 1
v_add_f32_dpp v10, v10, v10 row_ror:8 row_mask:0xf bank_mask:0xf
s_nop 0
v_readlane_b32 s50, v10, 0
v_readlane_b32 s51, v10, 16
v_readlane_b32 s52, v10, 32
v_readlane_b32 s53, v10, 48
s_nop 1
v_mov_b32 v9, s50
v_add_f32 v9, s51, v9
v_add_f32 v9, s52, v9
v_add_f32 v9, s53, v9
v_lshrrev_b32 v12, 8, v1
v_lshlrev_b32 v12, 7, v12
v_lshrrev_b32 v13, 2, v1
v_and_b32 v13, 60, v13
v_add3_u32 v12, v12, v13, s45
s_waitcnt lgkmcnt(0)
s_mov_b32 exec_lo, 0x00ff00ff
s_mov_b32 exec_hi, 0x00ff00ff
ds_write_b32 v12, v18 offset:0
ds_write_b32 v12, v19 offset:32
ds_write_b32 v12, v20 offset:64
ds_write_b32 v12, v21 offset:96
s_mov_b64 exec, -1
v_mov_b32 v13, s45
ds_write_b64 v13, v[8:9] offset:512
s_waitcnt lgkmcnt(0)
s_barrier
v_mov_b32 v12, s55
s_add_u32 s50, s55, 81920
v_mov_b32 v13, s50
ds_read_b64 v[18:19], v12 offset:512
ds_read_b64 v[20:21], v12 offset:41472
ds_read_b64 v[30:31], v13 offset:512
ds_read_b64 v[32:33], v13 offset:41472
s_lshl_b32 s50, s46, 7
v_lshrrev_b32 v7, 1, v1
v_add3_u32 v12, v12, v7, s50
v_add3_u32 v13, v13, v7, s50
ds_read_b64 v[22:23], v12
ds_read_b64 v[24:25], v12 offset:40960
ds_read_b64 v[26:27], v13
ds_read_b64 v[28:29], v13 offset:40960
s_waitcnt lgkmcnt(0)
s_barrier
ds_write_b128 v11, v[14:17]
v_min3_f32 v7, v18, v20, v30
v_min_f32 v7, v7, v32
v_sub_f32 v18, v7, v18
v_sub_f32 v20, v7, v20
v_sub_f32 v30, v7, v30
v_sub_f32 v32, v7, v32
v_exp_f32 v18, v18
v_exp_f32 v20, v20
v_exp_f32 v30, v30
v_exp_f32 v32, v32
s_nop 0
v_mul_f32 v10, v18, v19
v_fmac_f32 v10, v20, v21
v_fmac_f32 v10, v30, v31
v_fmac_f32 v10, v32, v33
v_mul_f32 v34, v18, v22
v_mul_f32 v36, v18, v23
v_fmac_f32 v34, v20, v24
v_fmac_f32 v36, v20, v25
v_fmac_f32 v34, v30, v26
v_fmac_f32 v36, v30, v27
v_fmac_f32 v34, v32, v28
v_fmac_f32 v36, v32, v29
v_sub_f32 v7, 0, v7
v_cmp_eq_u32 vcc, 0x100, v1
s_nop 1
v_cndmask_b32 v34, v34, v7, vcc
v_cndmask_b32 v36, v36, v10, vcc
s_add_u32 s31, s30, 1
v_mov_b32 v35, s31
v_mov_b32 v37, s31
s_add_u32 s50, s47, 0
s_lshl_b32 s51, s50, 10
s_lshl_b32 s52, s46, 8
s_add_u32 s51, s51, s52
s_add_u32 s51, s51, 131072
s_lshl_b32 s52, s50, 6
s_lshl_b32 s53, s46, 4
s_add_u32 s52, s52, s53
s_add_u32 s52, s52, 1114112
v_add_u32 v12, s51, v1
v_mov_b32 v13, s52
v_cndmask_b32 v12, v12, v13, vcc
s_mov_b32 exec_lo, 0x1ffff
s_mov_b32 exec_hi, 0
buffer_store_dwordx4 v[34:37], v12, s[24:27], 0 offen sc1
s_mov_b64 exec, -1
s_waitcnt lgkmcnt(0)
s_nop 1
s_add_u32 s31, s30, 2
v_cmp_eq_u32_e64 s[50:51], s31, v41
v_cmp_eq_u32_e64 s[52:53], s31, v43
s_and_b64 s[50:51], s[50:51], s[52:53]
v_cmp_eq_u32_e64 s[52:53], s31, v45
s_and_b64 s[50:51], s[50:51], s[52:53]
v_cmp_eq_u32_e64 s[52:53], s31, v47
s_and_b64 s[50:51], s[50:51], s[52:53]
s_cmp_eq_u64 s[50:51], -1
s_cbranch_scc1 Lqok_197_%=
s_mov_b32 s48, 0
Lqrt_198_%=:
s_sleep 8
v_and_b32 v12, 0xff, v0
v_lshlrev_b32 v12, 1, v12
s_lshl_b32 s50, s49, 9
s_add_u32 s50, s50, 20480
v_add_u32 v12, s50, v12
buffer_load_dwordx4 v[40:43], v12, s[24:27], 0 offen sc1
buffer_load_dwordx4 v[44:47], v12, s[24:27], 0 offen offset:16 sc1
s_waitcnt vmcnt(0)
v_cmp_eq_u32_e64 s[50:51], s31, v41
v_cmp_eq_u32_e64 s[52:53], s31, v43
s_and_b64 s[50:51], s[50:51], s[52:53]
v_cmp_eq_u32_e64 s[52:53], s31, v45
s_and_b64 s[50:51], s[50:51], s[52:53]
v_cmp_eq_u32_e64 s[52:53], s31, v47
s_and_b64 s[50:51], s[50:51], s[52:53]
s_cmp_eq_u64 s[50:51], -1
s_cbranch_scc1 Lqok_197_%=
s_add_u32 s48, s48, 1
s_cmp_lt_u32 s48, 0x8000
s_cbranch_scc1 Lqrt_198_%=
s_branch LEND_%=
Lqok_197_%=:
v_mov_b32 v14, v40
v_mov_b32 v15, v42
v_mov_b32 v16, v44
v_mov_b32 v17, v46
v_cndmask_b32_e64 v22, 0, v14, s[36:37]
v_cndmask_b32_e64 v23, 0, v15, s[36:37]
v_cndmask_b32_e64 v24, 0, v16, s[36:37]
v_cndmask_b32_e64 v25, 0, v17, s[36:37]
v_cndmask_b32_e64 v26, 0, v14, s[38:39]
v_cndmask_b32_e64 v27, 0, v15, s[38:39]
v_cndmask_b32_e64 v28, 0, v16, s[38:39]
v_cndmask_b32_e64 v29, 0, v17, s[38:39]
v_cndmask_b32_e64 v30, 0, v14, s[40:41]
v_cndmask_b32_e64 v31, 0, v15, s[40:41]
v_cndmask_b32_e64 v32, 0, v16, s[40:41]
v_cndmask_b32_e64 v33, 0, v17, s[40:41]
v_cndmask_b32_e64 v34, 0, v14, s[42:43]
v_cndmask_b32_e64 v35, 0, v15, s[42:43]
v_cndmask_b32_e64 v36, 0, v16, s[42:43]
v_cndmask_b32_e64 v37, 0, v17, s[42:43]
s_nop 1
s_nop 7
v_mov_b32 v18, 0
v_mov_b32 v19, 0
v_mov_b32 v20, 0
v_mov_b32 v21, 0
v_mov_b32 v9, 0
v_mov_b32 v8, 0
s_mov_b32 s81, 1
s_nop 1
s_mov_b32 s80, 1
s_setpc_b64 s[86:87]
LSW3_%=:
s_nop 7
v_add_f32_dpp v10, v9, v9 row_ror:4 row_mask:0xf bank_mask:0xf
ds_read_b128 v[14:17], v11
s_nop 1
v_add_f32_dpp v10, v10, v10 row_ror:8 row_mask:0xf bank_mask:0xf
s_nop 0
v_readlane_b32 s50, v10, 0
v_readlane_b32 s51, v10, 16
v_readlane_b32 s52, v10, 32
v_readlane_b32 s53, v10, 48
s_nop 1
v_mov_b32 v9, s50
v_add_f32 v9, s51, v9
v_add_f32 v9, s52, v9
v_add_f32 v9, s53, v9
v_lshrrev_b32 v12, 8, v1
v_lshlrev_b32 v12, 7, v12
v_lshrrev_b32 v13, 2, v1
v_and_b32 v13, 60, v13
v_add3_u32 v12, v12, v13, s45
s_waitcnt lgkmcnt(0)
s_mov_b32 exec_lo, 0x00ff00ff
s_mov_b32 exec_hi, 0x00ff00ff
ds_write_b32 v12, v18 offset:0
ds_write_b32 v12, v19 offset:32
ds_write_b32 v12, v20 offset:64
ds_write_b32 v12, v21 offset:96
s_mov_b64 exec, -1
v_mov_b32 v13, s45
ds_write_b64 v13, v[8:9] offset:512
s_waitcnt lgkmcnt(0)
s_barrier
v_mov_b32 v12, s55
s_add_u32 s50, s55, 81920
v_mov_b32 v13, s50
ds_read_b64 v[18:19], v12 offset:512
ds_read_b64 v[20:21], v12 offset:41472
ds_read_b64 v[30:31], v13 offset:512
ds_read_b64 v[32:33], v13 offset:41472
s_lshl_b32 s50, s46, 7
v_lshrrev_b32 v7, 1, v1
v_add3_u32 v12, v12, v7, s50
v_add3_u32 v13, v13, v7, s50
ds_read_b64 v[22:23], v12
ds_read_b64 v[24:25], v12 offset:40960
ds_read_b64 v[26:27], v13
ds_read_b64 v[28:29], v13 offset:40960
s_waitcnt lgkmcnt(0)
s_barrier
ds_write_b128 v11, v[14:17]
v_min3_f32 v7, v18, v20, v30
v_min_f32 v7, v7, v32
v_sub_f32 v18, v7, v18
v_sub_f32 v20, v7, v20
v_sub_f32 v30, v7, v30
v_sub_f32 v32, v7, v32
v_exp_f32 v18, v18
v_exp_f32 v20, v20
v_exp_f32 v30, v30
v_exp_f32 v32, v32
s_nop 0
v_mul_f32 v10, v18, v19
v_fmac_f32 v10, v20, v21
v_fmac_f32 v10, v30, v31
v_fmac_f32 v10, v32, v33
v_mul_f32 v34, v18, v22
v_mul_f32 v36, v18, v23
v_fmac_f32 v34, v20, v24
v_fmac_f32 v36, v20, v25
v_fmac_f32 v34, v30, v26
v_fmac_f32 v36, v30, v27
v_fmac_f32 v34, v32, v28
v_fmac_f32 v36, v32, v29
v_sub_f32 v7, 0, v7
v_cmp_eq_u32 vcc, 0x100, v1
s_nop 1
v_cndmask_b32 v34, v34, v7, vcc
v_cndmask_b32 v36, v36, v10, vcc
s_add_u32 s31, s30, 3
v_mov_b32 v35, s31
v_mov_b32 v37, s31
s_add_u32 s50, s47, 448
s_lshl_b32 s51, s50, 10
s_lshl_b32 s52, s46, 8
s_add_u32 s51, s51, s52
s_add_u32 s51, s51, 131072
s_lshl_b32 s52, s50, 6
s_lshl_b32 s53, s46, 4
s_add_u32 s52, s52, s53
s_add_u32 s52, s52, 1114112
v_add_u32 v12, s51, v1
v_mov_b32 v13, s52
v_cndmask_b32 v12, v12, v13, vcc
s_mov_b32 exec_lo, 0x1ffff
s_mov_b32 exec_hi, 0
buffer_store_dwordx4 v[34:37], v12, s[24:27], 0 offen sc1
s_mov_b64 exec, -1
s_waitcnt lgkmcnt(0)
s_nop 1
s_add_u32 s31, s30, 4
v_cmp_eq_u32_e64 s[50:51], s31, v41
v_cmp_eq_u32_e64 s[52:53], s31, v43
s_and_b64 s[50:51], s[50:51], s[52:53]
v_cmp_eq_u32_e64 s[52:53], s31, v45
s_and_b64 s[50:51], s[50:51], s[52:53]
v_cmp_eq_u32_e64 s[52:53], s31, v47
s_and_b64 s[50:51], s[50:51], s[52:53]
s_cmp_eq_u64 s[50:51], -1
s_cbranch_scc1 Lqok_199_%=
s_mov_b32 s48, 0
Lqrt_200_%=:
s_sleep 8
v_and_b32 v12, 0xff, v0
v_lshlrev_b32 v12, 1, v12
s_lshl_b32 s50, s49, 9
s_add_u32 s50, s50, 53248
v_add_u32 v12, s50, v12
buffer_load_dwordx4 v[40:43], v12, s[24:27], 0 offen sc1
buffer_load_dwordx4 v[44:47], v12, s[24:27], 0 offen offset:16 sc1
s_waitcnt vmcnt(0)
v_cmp_eq_u32_e64 s[50:51], s31, v41
v_cmp_eq_u32_e64 s[52:53], s31, v43
s_and_b64 s[50:51], s[50:51], s[52:53]
v_cmp_eq_u32_e64 s[52:53], s31, v45
s_and_b64 s[50:51], s[50:51], s[52:53]
v_cmp_eq_u32_e64 s[52:53], s31, v47
s_and_b64 s[50:51], s[50:51], s[52:53]
s_cmp_eq_u64 s[50:51], -1
s_cbranch_scc1 Lqok_199_%=
s_add_u32 s48, s48, 1
s_cmp_lt_u32 s48, 0x8000
s_cbranch_scc1 Lqrt_200_%=
s_branch LEND_%=
Lqok_199_%=:
v_mov_b32 v14, v40
v_mov_b32 v15, v42
v_mov_b32 v16, v44
v_mov_b32 v17, v46
v_cndmask_b32_e64 v22, 0, v14, s[36:37]
v_cndmask_b32_e64 v23, 0, v15, s[36:37]
v_cndmask_b32_e64 v24, 0, v16, s[36:37]
v_cndmask_b32_e64 v25, 0, v17, s[36:37]
v_cndmask_b32_e64 v26, 0, v14, s[38:39]
v_cndmask_b32_e64 v27, 0, v15, s[38:39]
v_cndmask_b32_e64 v28, 0, v16, s[38:39]
v_cndmask_b32_e64 v29, 0, v17, s[38:39]
v_cndmask_b32_e64 v30, 0, v14, s[40:41]
v_cndmask_b32_e64 v31, 0, v15, s[40:41]
v_cndmask_b32_e64 v32, 0, v16, s[40:41]
v_cndmask_b32_e64 v33, 0, v17, s[40:41]
v_cndmask_b32_e64 v34, 0, v14, s[42:43]
v_cndmask_b32_e64 v35, 0, v15, s[42:43]
v_cndmask_b32_e64 v36, 0, v16, s[42:43]
v_cndmask_b32_e64 v37, 0, v17, s[42:43]
s_nop 1
s_nop 7
v_mov_b32 v18, 0
v_mov_b32 v19, 0
v_mov_b32 v20, 0
v_mov_b32 v21, 0
v_mov_b32 v9, 0
v_mov_b32 v8, 0
s_mov_b32 s81, 1
s_nop 1
s_mov_b32 s80, 1
s_mov_b32 s82, 18
s_getpc_b64 s[84:85]
Lpc_201_%=:
s_add_u32 s84, s84, LCONS0_%=-Lpc_201_%=
s_addc_u32 s85, s85, 0
s_setpc_b64 s[86:87]
LPEND0_%=:
v_mfma_f32_16x16x32_bf16 v[14:17], v[48:51], v[22:25], 0
v_mfma_f32_16x16x32_bf16 v[14:17], v[52:55], v[26:29], v[14:17]
v_mfma_f32_16x16x32_bf16 v[14:17], v[56:59], v[30:33], v[14:17]
v_mfma_f32_16x16x32_bf16 v[14:17], v[60:63], v[34:37], v[14:17]
s_nop 7
v_mul_f32_dpp v14, v14, v2 quad_perm:[0,0,0,0] row_mask:0xf bank_mask:0xf
v_mul_f32_dpp v15, v15, v2 quad_perm:[1,1,1,1] row_mask:0xf bank_mask:0xf
v_mul_f32_dpp v16, v16, v2 quad_perm:[2,2,2,2] row_mask:0xf bank_mask:0xf
v_mul_f32_dpp v17, v17, v2 quad_perm:[3,3,3,3] row_mask:0xf bank_mask:0xf
v_add_f32 v14, v14, v15
v_add_f32 v16, v16, v17
v_add_f32 v14, v14, v8
v_add_f32 v7, v14, v16
v_cmp_lt_f32 vcc, 0, v7
s_cbranch_vccnz Lnc_203_%=
s_cmp_lg_u32 s81, 0
s_cbranch_scc1 Lnc_203_%=
Lns_202_%=:
v_exp_f32 v7, v7
s_nop 0
v_add_f32 v9, v9, v7
v_cvt_pk_bf16_f32 v10, v7, v7
s_nop 1
v_and_b32_dpp v14, v10, v3 row_newbcast:0 row_mask:0xf bank_mask:0xf
v_and_b32_dpp v15, v10, v4 row_newbcast:0 row_mask:0xf bank_mask:0xf
v_and_b32_dpp v16, v10, v5 row_newbcast:0 row_mask:0xf bank_mask:0xf
v_and_b32_dpp v17, v10, v6 row_newbcast:0 row_mask:0xf bank_mask:0xf
s_nop 1
v_mfma_f32_16x16x32_bf16 v[18:21], v[64:67], v[14:17], v[18:21]
v_and_b32_dpp v14, v10, v3 row_newbcast:4 row_mask:0xf bank_mask:0xf
v_and_b32_dpp v15, v10, v4 row_newbcast:4 row_mask:0xf bank_mask:0xf
v_and_b32_dpp v16, v10, v5 row_newbcast:4 row_mask:0xf bank_mask:0xf
v_and_b32_dpp v17, v10, v6 row_newbcast:4 row_mask:0xf bank_mask:0xf
s_nop 1
v_mfma_f32_16x16x32_bf16 v[18:21], v[68:71], v[14:17], v[18:21]
v_and_b32_dpp v14, v10, v3 row_newbcast:8 row_mask:0xf bank_mask:0xf
v_and_b32_dpp v15, v10, v4 row_newbcast:8 row_mask:0xf bank_mask:0xf
v_and_b32_dpp v16, v10, v5 row_newbcast:8 row_mask:0xf bank_mask:0xf
v_and_b32_dpp v17, v10, v6 row_newbcast:8 row_mask:0xf bank_mask:0xf
s_nop 1
v_mfma_f32_16x16x32_bf16 v[18:21], v[72:75], v[14:17], v[18:21]
v_and_b32_dpp v14, v10, v3 row_newbcast:12 row_mask:0xf bank_mask:0xf
v_and_b32_dpp v15, v10, v4 row_newbcast:12 row_mask:0xf bank_mask:0xf
v_and_b32_dpp v16, v10, v5 row_newbcast:12 row_mask:0xf bank_mask:0xf
v_and_b32_dpp v17, v10, v6 row_newbcast:12 row_mask:0xf bank_mask:0xf
s_nop 1
v_mfma_f32_16x16x32_bf16 v[18:21], v[76:79], v[14:17], v[18:21]
s_setpc_b64 s[86:87]
LPEND1_%=:
v_mfma_f32_16x16x32_bf16 v[14:17], v[80:83], v[22:25], 0
v_mfma_f32_16x16x32_bf16 v[14:17], v[84:87], v[26:29], v[14:17]
v_mfma_f32_16x16x32_bf16 v[14:17], v[88:91], v[30:33], v[14:17]
v_mfma_f32_16x16x32_bf16 v[14:17], v[92:95], v[34:37], v[14:17]
s_nop 7
v_mul_f32_dpp v14, v14, v2 quad_perm:[0,0,0,0] row_mask:0xf bank_mask:0xf
v_mul_f32_dpp v15, v15, v2 quad_perm:[1,1,1,1] row_mask:0xf bank_mask:0xf
v_mul_f32_dpp v16, v16, v2 quad_perm:[2,2,2,2] row_mask:0xf bank_mask:0xf
v_mul_f32_dpp v17, v17, v2 quad_perm:[3,3,3,3] row_mask:0xf bank_mask:0xf
v_add_f32 v14, v14, v15
v_add_f32 v16, v16, v17
v_add_f32 v14, v14, v8
v_add_f32 v7, v14, v16
v_cmp_lt_f32 vcc, 0, v7
s_cbranch_vccnz Lnc_205_%=
s_cmp_lg_u32 s81, 0
s_cbranch_scc1 Lnc_205_%=
Lns_204_%=:
v_exp_f32 v7, v7
s_nop 0
v_add_f32 v9, v9, v7
v_cvt_pk_bf16_f32 v10, v7, v7
s_nop 1
v_and_b32_dpp v14, v10, v3 row_newbcast:0 row_mask:0xf bank_mask:0xf
v_and_b32_dpp v15, v10, v4 row_newbcast:0 row_mask:0xf bank_mask:0xf
v_and_b32_dpp v16, v10, v5 row_newbcast:0 row_mask:0xf bank_mask:0xf
v_and_b32_dpp v17, v10, v6 row_newbcast:0 row_mask:0xf bank_mask:0xf
s_nop 1
v_mfma_f32_16x16x32_bf16 v[18:21], v[48:51], v[14:17], v[18:21]
v_and_b32_dpp v14, v10, v3 row_newbcast:4 row_mask:0xf bank_mask:0xf
v_and_b32_dpp v15, v10, v4 row_newbcast:4 row_mask:0xf bank_mask:0xf
v_and_b32_dpp v16, v10, v5 row_newbcast:4 row_mask:0xf bank_mask:0xf
v_and_b32_dpp v17, v10, v6 row_newbcast:4 row_mask:0xf bank_mask:0xf
s_nop 1
v_mfma_f32_16x16x32_bf16 v[18:21], v[52:55], v[14:17], v[18:21]
v_and_b32_dpp v14, v10, v3 row_newbcast:8 row_mask:0xf bank_mask:0xf
v_and_b32_dpp v15, v10, v4 row_newbcast:8 row_mask:0xf bank_mask:0xf
v_and_b32_dpp v16, v10, v5 row_newbcast:8 row_mask:0xf bank_mask:0xf
v_and_b32_dpp v17, v10, v6 row_newbcast:8 row_mask:0xf bank_mask:0xf
s_nop 1
v_mfma_f32_16x16x32_bf16 v[18:21], v[56:59], v[14:17], v[18:21]
v_and_b32_dpp v14, v10, v3 row_newbcast:12 row_mask:0xf bank_mask:0xf
v_and_b32_dpp v15, v10, v4 row_newbcast:12 row_mask:0xf bank_mask:0xf
v_and_b32_dpp v16, v10, v5 row_newbcast:12 row_mask:0xf bank_mask:0xf
v_and_b32_dpp v17, v10, v6 row_newbcast:12 row_mask:0xf bank_mask:0xf
s_nop 1
v_mfma_f32_16x16x32_bf16 v[18:21], v[60:63], v[14:17], v[18:21]
s_setpc_b64 s[86:87]
LPEND2_%=:
v_mfma_f32_16x16x32_bf16 v[14:17], v[64:67], v[22:25], 0
v_mfma_f32_16x16x32_bf16 v[14:17], v[68:71], v[26:29], v[14:17]
v_mfma_f32_16x16x32_bf16 v[14:17], v[72:75], v[30:33], v[14:17]
v_mfma_f32_16x16x32_bf16 v[14:17], v[76:79], v[34:37], v[14:17]
s_nop 7
v_mul_f32_dpp v14, v14, v2 quad_perm:[0,0,0,0] row_mask:0xf bank_mask:0xf
v_mul_f32_dpp v15, v15, v2 quad_perm:[1,1,1,1] row_mask:0xf bank_mask:0xf
v_mul_f32_dpp v16, v16, v2 quad_perm:[2,2,2,2] row_mask:0xf bank_mask:0xf
v_mul_f32_dpp v17, v17, v2 quad_perm:[3,3,3,3] row_mask:0xf bank_mask:0xf
v_add_f32 v14, v14, v15
v_add_f32 v16, v16, v17
v_add_f32 v14, v14, v8
v_add_f32 v7, v14, v16
v_cmp_lt_f32 vcc, 0, v7
s_cbranch_vccnz Lnc_207_%=
s_cmp_lg_u32 s81, 0
s_cbranch_scc1 Lnc_207_%=
Lns_206_%=:
v_exp_f32 v7, v7
s_nop 0
v_add_f32 v9, v9, v7
v_cvt_pk_bf16_f32 v10, v7, v7
s_nop 1
v_and_b32_dpp v14, v10, v3 row_newbcast:0 row_mask:0xf bank_mask:0xf
v_and_b32_dpp v15, v10, v4 row_newbcast:0 row_mask:0xf bank_mask:0xf
v_and_b32_dpp v16, v10, v5 row_newbcast:0 row_mask:0xf bank_mask:0xf
v_and_b32_dpp v17, v10, v6 row_newbcast:0 row_mask:0xf bank_mask:0xf
s_nop 1
v_mfma_f32_16x16x32_bf16 v[18:21], v[80:83], v[14:17], v[18:21]
v_and_b32_dpp v14, v10, v3 row_newbcast:4 row_mask:0xf bank_mask:0xf
v_and_b32_dpp v15, v10, v4 row_newbcast:4 row_mask:0xf bank_mask:0xf
v_and_b32_dpp v16, v10, v5 row_newbcast:4 row_mask:0xf bank_mask:0xf
v_and_b32_dpp v17, v10, v6 row_newbcast:4 row_mask:0xf bank_mask:0xf
s_nop 1
v_mfma_f32_16x16x32_bf16 v[18:21], v[84:87], v[14:17], v[18:21]
v_and_b32_dpp v14, v10, v3 row_newbcast:8 row_mask:0xf bank_mask:0xf
v_and_b32_dpp v15, v10, v4 row_newbcast:8 row_mask:0xf bank_mask:0xf
v_and_b32_dpp v16, v10, v5 row_newbcast:8 row_mask:0xf bank_mask:0xf
v_and_b32_dpp v17, v10, v6 row_newbcast:8 row_mask:0xf bank_mask:0xf
s_nop 1
v_mfma_f32_16x16x32_bf16 v[18:21], v[88:91], v[14:17], v[18:21]
v_and_b32_dpp v14, v10, v3 row_newbcast:12 row_mask:0xf bank_mask:0xf
v_and_b32_dpp v15, v10, v4 row_newbcast:12 row_mask:0xf bank_mask:0xf
v_and_b32_dpp v16, v10, v5 row_newbcast:12 row_mask:0xf bank_mask:0xf
v_and_b32_dpp v17, v10, v6 row_newbcast:12 row_mask:0xf bank_mask:0xf
s_nop 1
v_mfma_f32_16x16x32_bf16 v[18:21], v[92:95], v[14:17], v[18:21]
s_setpc_b64 s[86:87]
LCONS0_%=:
v_mfma_f32_16x16x32_bf16 v[14:17], a[0:3], v[22:25], 0
v_mfma_f32_16x16x32_bf16 v[14:17], a[4:7], v[26:29], v[14:17]
v_mfma_f32_16x16x32_bf16 v[14:17], a[8:11], v[30:33], v[14:17]
v_mfma_f32_16x16x32_bf16 v[14:17], a[12:15], v[34:37], v[14:17]
s_nop 7
v_mul_f32_dpp v14, v14, v2 quad_perm:[0,0,0,0] row_mask:0xf bank_mask:0xf
v_mul_f32_dpp v15, v15, v2 quad_perm:[1,1,1,1] row_mask:0xf bank_mask:0xf
v_mul_f32_dpp v16, v16, v2 quad_perm:[2,2,2,2] row_mask:0xf bank_mask:0xf
v_mul_f32_dpp v17, v17, v2 quad_perm:[3,3,3,3] row_mask:0xf bank_mask:0xf
v_add_f32 v14, v14, v15
v_add_f32 v16, v16, v17
v_add_f32 v14, v14, v8
v_add_f32 v7, v14, v16
v_cmp_lt_f32 vcc, 0, v7
s_cbranch_vccnz Lnc_209_%=
s_cmp_lg_u32 s81, 0
s_cbranch_scc1 Lnc_209_%=
Lns_208_%=:
v_exp_f32 v7, v7
s_nop 0
v_add_f32 v9, v9, v7
v_cvt_pk_bf16_f32 v10, v7, v7
s_nop 1
v_and_b32_dpp v14, v10, v3 row_newbcast:0 row_mask:0xf bank_mask:0xf
v_and_b32_dpp v15, v10, v4 row_newbcast:0 row_mask:0xf bank_mask:0xf
v_and_b32_dpp v16, v10, v5 row_newbcast:0 row_mask:0xf bank_mask:0xf
v_and_b32_dpp v17, v10, v6 row_newbcast:0 row_mask:0xf bank_mask:0xf
s_nop 1
v_mfma_f32_16x16x32_bf16 v[18:21], a[16:19], v[14:17], v[18:21]
v_and_b32_dpp v14, v10, v3 row_newbcast:4 row_mask:0xf bank_mask:0xf
v_and_b32_dpp v15, v10, v4 row_newbcast:4 row_mask:0xf bank_mask:0xf
v_and_b32_dpp v16, v10, v5 row_newbcast:4 row_mask:0xf bank_mask:0xf
v_and_b32_dpp v17, v10, v6 row_newbcast:4 row_mask:0xf bank_mask:0xf
s_nop 1
v_mfma_f32_16x16x32_bf16 v[18:21], a[20:23], v[14:17], v[18:21]
v_and_b32_dpp v14, v10, v3 row_newbcast:8 row_mask:0xf bank_mask:0xf
v_and_b32_dpp v15, v10, v4 row_newbcast:8 row_mask:0xf bank_mask:0xf
v_and_b32_dpp v16, v10, v5 row_newbcast:8 row_mask:0xf bank_mask:0xf
v_and_b32_dpp v17, v10, v6 row_newbcast:8 row_mask:0xf bank_mask:0xf
s_nop 1
v_mfma_f32_16x16x32_bf16 v[18:21], a[24:27], v[14:17], v[18:21]
v_and_b32_dpp v14, v10, v3 row_newbcast:12 row_mask:0xf bank_mask:0xf
v_and_b32_dpp v15, v10, v4 row_newbcast:12 row_mask:0xf bank_mask:0xf
v_and_b32_dpp v16, v10, v5 row_newbcast:12 row_mask:0xf bank_mask:0xf
v_and_b32_dpp v17, v10, v6 row_newbcast:12 row_mask:0xf bank_mask:0xf
s_nop 1
v_mfma_f32_16x16x32_bf16 v[18:21], a[28:31], v[14:17], v[18:21]
s_add_u32 s84, s84, LCONS1_%=-LCONS0_%=
s_addc_u32 s85, s85, 0
s_sub_u32 s82, s82, 1
s_setpc_b64 s[86:87]
LCONS1_%=:
v_mfma_f32_16x16x32_bf16 v[14:17], a[32:35], v[22:25], 0
v_mfma_f32_16x16x32_bf16 v[14:17], a[36:39], v[26:29], v[14:17]
v_mfma_f32_16x16x32_bf16 v[14:17], a[40:43], v[30:33], v[14:17]
v_mfma_f32_16x16x32_bf16 v[14:17], a[44:47], v[34:37], v[14:17]
s_nop 7
v_mul_f32_dpp v14, v14, v2 quad_perm:[0,0,0,0] row_mask:0xf bank_mask:0xf
v_mul_f32_dpp v15, v15, v2 quad_perm:[1,1,1,1] row_mask:0xf bank_mask:0xf
v_mul_f32_dpp v16, v16, v2 quad_perm:[2,2,2,2] row_mask:0xf bank_mask:0xf
v_mul_f32_dpp v17, v17, v2 quad_perm:[3,3,3,3] row_mask:0xf bank_mask:0xf
v_add_f32 v14, v14, v15
v_add_f32 v16, v16, v17
v_add_f32 v14, v14, v8
v_add_f32 v7, v14, v16
v_cmp_lt_f32 vcc, 0, v7
s_cbranch_vccnz Lnc_211_%=
s_cmp_lg_u32 s81, 0
s_cbranch_scc1 Lnc_211_%=
Lns_210_%=:
v_exp_f32 v7, v7
s_nop 0
v_add_f32 v9, v9, v7
v_cvt_pk_bf16_f32 v10, v7, v7
s_nop 1
v_and_b32_dpp v14, v10, v3 row_newbcast:0 row_mask:0xf bank_mask:0xf
v_and_b32_dpp v15, v10, v4 row_newbcast:0 row_mask:0xf bank_mask:0xf
v_and_b32_dpp v16, v10, v5 row_newbcast:0 row_mask:0xf bank_mask:0xf
v_and_b32_dpp v17, v10, v6 row_newbcast:0 row_mask:0xf bank_mask:0xf
s_nop 1
v_mfma_f32_16x16x32_bf16 v[18:21], a[48:51], v[14:17], v[18:21]
v_and_b32_dpp v14, v10, v3 row_newbcast:4 row_mask:0xf bank_mask:0xf
v_and_b32_dpp v15, v10, v4 row_newbcast:4 row_mask:0xf bank_mask:0xf
v_and_b32_dpp v16, v10, v5 row_newbcast:4 row_mask:0xf bank_mask:0xf
v_and_b32_dpp v17, v10, v6 row_newbcast:4 row_mask:0xf bank_mask:0xf
s_nop 1
v_mfma_f32_16x16x32_bf16 v[18:21], a[52:55], v[14:17], v[18:21]
v_and_b32_dpp v14, v10, v3 row_newbcast:8 row_mask:0xf bank_mask:0xf
v_and_b32_dpp v15, v10, v4 row_newbcast:8 row_mask:0xf bank_mask:0xf
v_and_b32_dpp v16, v10, v5 row_newbcast:8 row_mask:0xf bank_mask:0xf
v_and_b32_dpp v17, v10, v6 row_newbcast:8 row_mask:0xf bank_mask:0xf
s_nop 1
v_mfma_f32_16x16x32_bf16 v[18:21], a[56:59], v[14:17], v[18:21]
v_and_b32_dpp v14, v10, v3 row_newbcast:12 row_mask:0xf bank_mask:0xf
v_and_b32_dpp v15, v10, v4 row_newbcast:12 row_mask:0xf bank_mask:0xf
v_and_b32_dpp v16, v10, v5 row_newbcast:12 row_mask:0xf bank_mask:0xf
v_and_b32_dpp v17, v10, v6 row_newbcast:12 row_mask:0xf bank_mask:0xf
s_nop 1
v_mfma_f32_16x16x32_bf16 v[18:21], a[60:63], v[14:17], v[18:21]
s_add_u32 s84, s84, LCONS2_%=-LCONS1_%=
s_addc_u32 s85, s85, 0
s_sub_u32 s82, s82, 1
s_setpc_b64 s[86:87]
LCONS2_%=:
v_mfma_f32_16x16x32_bf16 v[14:17], a[64:67], v[22:25], 0
v_mfma_f32_16x16x32_bf16 v[14:17], a[68:71], v[26:29], v[14:17]
v_mfma_f32_16x16x32_bf16 v[14:17], a[72:75], v[30:33], v[14:17]
v_mfma_f32_16x16x32_bf16 v[14:17], a[76:79], v[34:37], v[14:17]
s_nop 7
v_mul_f32_dpp v14, v14, v2 quad_perm:[0,0,0,0] row_mask:0xf bank_mask:0xf
v_mul_f32_dpp v15, v15, v2 quad_perm:[1,1,1,1] row_mask:0xf bank_mask:0xf
v_mul_f32_dpp v16, v16, v2 quad_perm:[2,2,2,2] row_mask:0xf bank_mask:0xf
v_mul_f32_dpp v17, v17, v2 quad_perm:[3,3,3,3] row_mask:0xf bank_mask:0xf
v_add_f32 v14, v14, v15
v_add_f32 v16, v16, v17
v_add_f32 v14, v14, v8
v_add_f32 v7, v14, v16
v_cmp_lt_f32 vcc, 0, v7
s_cbranch_vccnz Lnc_213_%=
s_cmp_lg_u32 s81, 0
s_cbranch_scc1 Lnc_213_%=
Lns_212_%=:
v_exp_f32 v7, v7
s_nop 0
v_add_f32 v9, v9, v7
v_cvt_pk_bf16_f32 v10, v7, v7
s_nop 1
v_and_b32_dpp v14, v10, v3 row_newbcast:0 row_mask:0xf bank_mask:0xf
v_and_b32_dpp v15, v10, v4 row_newbcast:0 row_mask:0xf bank_mask:0xf
v_and_b32_dpp v16, v10, v5 row_newbcast:0 row_mask:0xf bank_mask:0xf
v_and_b32_dpp v17, v10, v6 row_newbcast:0 row_mask:0xf bank_mask:0xf
s_nop 1
v_mfma_f32_16x16x32_bf16 v[18:21], a[80:83], v[14:17], v[18:21]
v_and_b32_dpp v14, v10, v3 row_newbcast:4 row_mask:0xf bank_mask:0xf
v_and_b32_dpp v15, v10, v4 row_newbcast:4 row_mask:0xf bank_mask:0xf
v_and_b32_dpp v16, v10, v5 row_newbcast:4 row_mask:0xf bank_mask:0xf
v_and_b32_dpp v17, v10, v6 row_newbcast:4 row_mask:0xf bank_mask:0xf
s_nop 1
v_mfma_f32_16x16x32_bf16 v[18:21], a[84:87], v[14:17], v[18:21]
v_and_b32_dpp v14, v10, v3 row_newbcast:8 row_mask:0xf bank_mask:0xf
v_and_b32_dpp v15, v10, v4 row_newbcast:8 row_mask:0xf bank_mask:0xf
v_and_b32_dpp v16, v10, v5 row_newbcast:8 row_mask:0xf bank_mask:0xf
v_and_b32_dpp v17, v10, v6 row_newbcast:8 row_mask:0xf bank_mask:0xf
s_nop 1
v_mfma_f32_16x16x32_bf16 v[18:21], a[88:91], v[14:17], v[18:21]
v_and_b32_dpp v14, v10, v3 row_newbcast:12 row_mask:0xf bank_mask:0xf
v_and_b32_dpp v15, v10, v4 row_newbcast:12 row_mask:0xf bank_mask:0xf
v_and_b32_dpp v16, v10, v5 row_newbcast:12 row_mask:0xf bank_mask:0xf
v_and_b32_dpp v17, v10, v6 row_newbcast:12 row_mask:0xf bank_mask:0xf
s_nop 1
v_mfma_f32_16x16x32_bf16 v[18:21], a[92:95], v[14:17], v[18:21]
s_add_u32 s84, s84, LCONS3_%=-LCONS2_%=
s_addc_u32 s85, s85, 0
s_sub_u32 s82, s82, 1
s_setpc_b64 s[86:87]
LCONS3_%=:
v_mfma_f32_16x16x32_bf16 v[14:17], a[96:99], v[22:25], 0
v_mfma_f32_16x16x32_bf16 v[14:17], a[100:103], v[26:29], v[14:17]
v_mfma_f32_16x16x32_bf16 v[14:17], a[104:107], v[30:33], v[14:17]
v_mfma_f32_16x16x32_bf16 v[14:17], a[108:111], v[34:37], v[14:17]
s_nop 7
v_mul_f32_dpp v14, v14, v2 quad_perm:[0,0,0,0] row_mask:0xf bank_mask:0xf
v_mul_f32_dpp v15, v15, v2 quad_perm:[1,1,1,1] row_mask:0xf bank_mask:0xf
v_mul_f32_dpp v16, v16, v2 quad_perm:[2,2,2,2] row_mask:0xf bank_mask:0xf
v_mul_f32_dpp v17, v17, v2 quad_perm:[3,3,3,3] row_mask:0xf bank_mask:0xf
v_add_f32 v14, v14, v15
v_add_f32 v16, v16, v17
v_add_f32 v14, v14, v8
v_add_f32 v7, v14, v16
v_cmp_lt_f32 vcc, 0, v7
s_cbranch_vccnz Lnc_215_%=
s_cmp_lg_u32 s81, 0
s_cbranch_scc1 Lnc_215_%=
Lns_214_%=:
v_exp_f32 v7, v7
s_nop 0
v_add_f32 v9, v9, v7
v_cvt_pk_bf16_f32 v10, v7, v7
s_nop 1
v_and_b32_dpp v14, v10, v3 row_newbcast:0 row_mask:0xf bank_mask:0xf
v_and_b32_dpp v15, v10, v4 row_newbcast:0 row_mask:0xf bank_mask:0xf
v_and_b32_dpp v16, v10, v5 row_newbcast:0 row_mask:0xf bank_mask:0xf
v_and_b32_dpp v17, v10, v6 row_newbcast:0 row_mask:0xf bank_mask:0xf
s_nop 1
v_mfma_f32_16x16x32_bf16 v[18:21], a[112:115], v[14:17], v[18:21]
v_and_b32_dpp v14, v10, v3 row_newbcast:4 row_mask:0xf bank_mask:0xf
v_and_b32_dpp v15, v10, v4 row_newbcast:4 row_mask:0xf bank_mask:0xf
v_and_b32_dpp v16, v10, v5 row_newbcast:4 row_mask:0xf bank_mask:0xf
v_and_b32_dpp v17, v10, v6 row_newbcast:4 row_mask:0xf bank_mask:0xf
s_nop 1
v_mfma_f32_16x16x32_bf16 v[18:21], a[116:119], v[14:17], v[18:21]
v_and_b32_dpp v14, v10, v3 row_newbcast:8 row_mask:0xf bank_mask:0xf
v_and_b32_dpp v15, v10, v4 row_newbcast:8 row_mask:0xf bank_mask:0xf
v_and_b32_dpp v16, v10, v5 row_newbcast:8 row_mask:0xf bank_mask:0xf
v_and_b32_dpp v17, v10, v6 row_newbcast:8 row_mask:0xf bank_mask:0xf
s_nop 1
v_mfma_f32_16x16x32_bf16 v[18:21], a[120:123], v[14:17], v[18:21]
v_and_b32_dpp v14, v10, v3 row_newbcast:12 row_mask:0xf bank_mask:0xf
v_and_b32_dpp v15, v10, v4 row_newbcast:12 row_mask:0xf bank_mask:0xf
v_and_b32_dpp v16, v10, v5 row_newbcast:12 row_mask:0xf bank_mask:0xf
v_and_b32_dpp v17, v10, v6 row_newbcast:12 row_mask:0xf bank_mask:0xf
s_nop 1
v_mfma_f32_16x16x32_bf16 v[18:21], a[124:127], v[14:17], v[18:21]
s_add_u32 s84, s84, LCONS4_%=-LCONS3_%=
s_addc_u32 s85, s85, 0
s_sub_u32 s82, s82, 1
s_setpc_b64 s[86:87]
LCONS4_%=:
v_mfma_f32_16x16x32_bf16 v[14:17], a[128:131], v[22:25], 0
v_mfma_f32_16x16x32_bf16 v[14:17], a[132:135], v[26:29], v[14:17]
v_mfma_f32_16x16x32_bf16 v[14:17], a[136:139], v[30:33], v[14:17]
v_mfma_f32_16x16x32_bf16 v[14:17], a[140:143], v[34:37], v[14:17]
s_nop 7
v_mul_f32_dpp v14, v14, v2 quad_perm:[0,0,0,0] row_mask:0xf bank_mask:0xf
v_mul_f32_dpp v15, v15, v2 quad_perm:[1,1,1,1] row_mask:0xf bank_mask:0xf
v_mul_f32_dpp v16, v16, v2 quad_perm:[2,2,2,2] row_mask:0xf bank_mask:0xf
v_mul_f32_dpp v17, v17, v2 quad_perm:[3,3,3,3] row_mask:0xf bank_mask:0xf
v_add_f32 v14, v14, v15
v_add_f32 v16, v16, v17
v_add_f32 v14, v14, v8
v_add_f32 v7, v14, v16
v_cmp_lt_f32 vcc, 0, v7
s_cbranch_vccnz Lnc_217_%=
s_cmp_lg_u32 s81, 0
s_cbranch_scc1 Lnc_217_%=
Lns_216_%=:
v_exp_f32 v7, v7
s_nop 0
v_add_f32 v9, v9, v7
v_cvt_pk_bf16_f32 v10, v7, v7
s_nop 1
v_and_b32_dpp v14, v10, v3 row_newbcast:0 row_mask:0xf bank_mask:0xf
v_and_b32_dpp v15, v10, v4 row_newbcast:0 row_mask:0xf bank_mask:0xf
v_and_b32_dpp v16, v10, v5 row_newbcast:0 row_mask:0xf bank_mask:0xf
v_and_b32_dpp v17, v10, v6 row_newbcast:0 row_mask:0xf bank_mask:0xf
s_nop 1
v_mfma_f32_16x16x32_bf16 v[18:21], a[144:147], v[14:17], v[18:21]
v_and_b32_dpp v14, v10, v3 row_newbcast:4 row_mask:0xf bank_mask:0xf
v_and_b32_dpp v15, v10, v4 row_newbcast:4 row_mask:0xf bank_mask:0xf
v_and_b32_dpp v16, v10, v5 row_newbcast:4 row_mask:0xf bank_mask:0xf
v_and_b32_dpp v17, v10, v6 row_newbcast:4 row_mask:0xf bank_mask:0xf
s_nop 1
v_mfma_f32_16x16x32_bf16 v[18:21], a[148:151], v[14:17], v[18:21]
v_and_b32_dpp v14, v10, v3 row_newbcast:8 row_mask:0xf bank_mask:0xf
v_and_b32_dpp v15, v10, v4 row_newbcast:8 row_mask:0xf bank_mask:0xf
v_and_b32_dpp v16, v10, v5 row_newbcast:8 row_mask:0xf bank_mask:0xf
v_and_b32_dpp v17, v10, v6 row_newbcast:8 row_mask:0xf bank_mask:0xf
s_nop 1
v_mfma_f32_16x16x32_bf16 v[18:21], a[152:155], v[14:17], v[18:21]
v_and_b32_dpp v14, v10, v3 row_newbcast:12 row_mask:0xf bank_mask:0xf
v_and_b32_dpp v15, v10, v4 row_newbcast:12 row_mask:0xf bank_mask:0xf
v_and_b32_dpp v16, v10, v5 row_newbcast:12 row_mask:0xf bank_mask:0xf
v_and_b32_dpp v17, v10, v6 row_newbcast:12 row_mask:0xf bank_mask:0xf
s_nop 1
v_mfma_f32_16x16x32_bf16 v[18:21], a[156:159], v[14:17], v[18:21]
s_add_u32 s84, s84, LCONS5_%=-LCONS4_%=
s_addc_u32 s85, s85, 0
s_sub_u32 s82, s82, 1
s_setpc_b64 s[86:87]
LCONS5_%=:
v_mfma_f32_16x16x32_bf16 v[14:17], a[160:163], v[22:25], 0
v_mfma_f32_16x16x32_bf16 v[14:17], a[164:167], v[26:29], v[14:17]
v_mfma_f32_16x16x32_bf16 v[14:17], a[168:171], v[30:33], v[14:17]
v_mfma_f32_16x16x32_bf16 v[14:17], a[172:175], v[34:37], v[14:17]
s_nop 7
v_mul_f32_dpp v14, v14, v2 quad_perm:[0,0,0,0] row_mask:0xf bank_mask:0xf
v_mul_f32_dpp v15, v15, v2 quad_perm:[1,1,1,1] row_mask:0xf bank_mask:0xf
v_mul_f32_dpp v16, v16, v2 quad_perm:[2,2,2,2] row_mask:0xf bank_mask:0xf
v_mul_f32_dpp v17, v17, v2 quad_perm:[3,3,3,3] row_mask:0xf bank_mask:0xf
v_add_f32 v14, v14, v15
v_add_f32 v16, v16, v17
v_add_f32 v14, v14, v8
v_add_f32 v7, v14, v16
v_cmp_lt_f32 vcc, 0, v7
s_cbranch_vccnz Lnc_219_%=
s_cmp_lg_u32 s81, 0
s_cbranch_scc1 Lnc_219_%=
Lns_218_%=:
v_exp_f32 v7, v7
s_nop 0
v_add_f32 v9, v9, v7
v_cvt_pk_bf16_f32 v10, v7, v7
s_nop 1
v_and_b32_dpp v14, v10, v3 row_newbcast:0 row_mask:0xf bank_mask:0xf
v_and_b32_dpp v15, v10, v4 row_newbcast:0 row_mask:0xf bank_mask:0xf
v_and_b32_dpp v16, v10, v5 row_newbcast:0 row_mask:0xf bank_mask:0xf
v_and_b32_dpp v17, v10, v6 row_newbcast:0 row_mask:0xf bank_mask:0xf
s_nop 1
v_mfma_f32_16x16x32_bf16 v[18:21], a[176:179], v[14:17], v[18:21]
v_and_b32_dpp v14, v10, v3 row_newbcast:4 row_mask:0xf bank_mask:0xf
v_and_b32_dpp v15, v10, v4 row_newbcast:4 row_mask:0xf bank_mask:0xf
v_and_b32_dpp v16, v10, v5 row_newbcast:4 row_mask:0xf bank_mask:0xf
v_and_b32_dpp v17, v10, v6 row_newbcast:4 row_mask:0xf bank_mask:0xf
s_nop 1
v_mfma_f32_16x16x32_bf16 v[18:21], a[180:183], v[14:17], v[18:21]
v_and_b32_dpp v14, v10, v3 row_newbcast:8 row_mask:0xf bank_mask:0xf
v_and_b32_dpp v15, v10, v4 row_newbcast:8 row_mask:0xf bank_mask:0xf
v_and_b32_dpp v16, v10, v5 row_newbcast:8 row_mask:0xf bank_mask:0xf
v_and_b32_dpp v17, v10, v6 row_newbcast:8 row_mask:0xf bank_mask:0xf
s_nop 1
v_mfma_f32_16x16x32_bf16 v[18:21], a[184:187], v[14:17], v[18:21]
v_and_b32_dpp v14, v10, v3 row_newbcast:12 row_mask:0xf bank_mask:0xf
v_and_b32_dpp v15, v10, v4 row_newbcast:12 row_mask:0xf bank_mask:0xf
v_and_b32_dpp v16, v10, v5 row_newbcast:12 row_mask:0xf bank_mask:0xf
v_and_b32_dpp v17, v10, v6 row_newbcast:12 row_mask:0xf bank_mask:0xf
s_nop 1
v_mfma_f32_16x16x32_bf16 v[18:21], a[188:191], v[14:17], v[18:21]
s_add_u32 s84, s84, LCONS6_%=-LCONS5_%=
s_addc_u32 s85, s85, 0
s_sub_u32 s82, s82, 1
s_setpc_b64 s[86:87]
LCONS6_%=:
v_mfma_f32_16x16x32_bf16 v[14:17], a[192:195], v[22:25], 0
v_mfma_f32_16x16x32_bf16 v[14:17], a[196:199], v[26:29], v[14:17]
v_mfma_f32_16x16x32_bf16 v[14:17], a[200:203], v[30:33], v[14:17]
v_mfma_f32_16x16x32_bf16 v[14:17], a[204:207], v[34:37], v[14:17]
s_nop 7
v_mul_f32_dpp v14, v14, v2 quad_perm:[0,0,0,0] row_mask:0xf bank_mask:0xf
v_mul_f32_dpp v15, v15, v2 quad_perm:[1,1,1,1] row_mask:0xf bank_mask:0xf
v_mul_f32_dpp v16, v16, v2 quad_perm:[2,2,2,2] row_mask:0xf bank_mask:0xf
v_mul_f32_dpp v17, v17, v2 quad_perm:[3,3,3,3] row_mask:0xf bank_mask:0xf
v_add_f32 v14, v14, v15
v_add_f32 v16, v16, v17
v_add_f32 v14, v14, v8
v_add_f32 v7, v14, v16
v_cmp_lt_f32 vcc, 0, v7
s_cbranch_vccnz Lnc_221_%=
s_cmp_lg_u32 s81, 0
s_cbranch_scc1 Lnc_221_%=
Lns_220_%=:
v_exp_f32 v7, v7
s_nop 0
v_add_f32 v9, v9, v7
v_cvt_pk_bf16_f32 v10, v7, v7
s_nop 1
v_and_b32_dpp v14, v10, v3 row_newbcast:0 row_mask:0xf bank_mask:0xf
v_and_b32_dpp v15, v10, v4 row_newbcast:0 row_mask:0xf bank_mask:0xf
v_and_b32_dpp v16, v10, v5 row_newbcast:0 row_mask:0xf bank_mask:0xf
v_and_b32_dpp v17, v10, v6 row_newbcast:0 row_mask:0xf bank_mask:0xf
s_nop 1
v_mfma_f32_16x16x32_bf16 v[18:21], a[208:211], v[14:17], v[18:21]
v_and_b32_dpp v14, v10, v3 row_newbcast:4 row_mask:0xf bank_mask:0xf
v_and_b32_dpp v15, v10, v4 row_newbcast:4 row_mask:0xf bank_mask:0xf
v_and_b32_dpp v16, v10, v5 row_newbcast:4 row_mask:0xf bank_mask:0xf
v_and_b32_dpp v17, v10, v6 row_newbcast:4 row_mask:0xf bank_mask:0xf
s_nop 1
v_mfma_f32_16x16x32_bf16 v[18:21], a[212:215], v[14:17], v[18:21]
v_and_b32_dpp v14, v10, v3 row_newbcast:8 row_mask:0xf bank_mask:0xf
v_and_b32_dpp v15, v10, v4 row_newbcast:8 row_mask:0xf bank_mask:0xf
v_and_b32_dpp v16, v10, v5 row_newbcast:8 row_mask:0xf bank_mask:0xf
v_and_b32_dpp v17, v10, v6 row_newbcast:8 row_mask:0xf bank_mask:0xf
s_nop 1
v_mfma_f32_16x16x32_bf16 v[18:21], a[216:219], v[14:17], v[18:21]
v_and_b32_dpp v14, v10, v3 row_newbcast:12 row_mask:0xf bank_mask:0xf
v_and_b32_dpp v15, v10, v4 row_newbcast:12 row_mask:0xf bank_mask:0xf
v_and_b32_dpp v16, v10, v5 row_newbcast:12 row_mask:0xf bank_mask:0xf
v_and_b32_dpp v17, v10, v6 row_newbcast:12 row_mask:0xf bank_mask:0xf
s_nop 1
v_mfma_f32_16x16x32_bf16 v[18:21], a[220:223], v[14:17], v[18:21]
s_add_u32 s84, s84, LCONS7_%=-LCONS6_%=
s_addc_u32 s85, s85, 0
s_sub_u32 s82, s82, 1
s_setpc_b64 s[86:87]
LCONS7_%=:
v_mfma_f32_16x16x32_bf16 v[14:17], a[224:227], v[22:25], 0
v_mfma_f32_16x16x32_bf16 v[14:17], a[228:231], v[26:29], v[14:17]
v_mfma_f32_16x16x32_bf16 v[14:17], a[232:235], v[30:33], v[14:17]
v_mfma_f32_16x16x32_bf16 v[14:17], a[236:239], v[34:37], v[14:17]
s_nop 7
v_mul_f32_dpp v14, v14, v2 quad_perm:[0,0,0,0] row_mask:0xf bank_mask:0xf
v_mul_f32_dpp v15, v15, v2 quad_perm:[1,1,1,1] row_mask:0xf bank_mask:0xf
v_mul_f32_dpp v16, v16, v2 quad_perm:[2,2,2,2] row_mask:0xf bank_mask:0xf
v_mul_f32_dpp v17, v17, v2 quad_perm:[3,3,3,3] row_mask:0xf bank_mask:0xf
v_add_f32 v14, v14, v15
v_add_f32 v16, v16, v17
v_add_f32 v14, v14, v8
v_add_f32 v7, v14, v16
v_cmp_lt_f32 vcc, 0, v7
s_cbranch_vccnz Lnc_223_%=
s_cmp_lg_u32 s81, 0
s_cbranch_scc1 Lnc_223_%=
Lns_222_%=:
v_exp_f32 v7, v7
s_nop 0
v_add_f32 v9, v9, v7
v_cvt_pk_bf16_f32 v10, v7, v7
s_nop 1
v_and_b32_dpp v14, v10, v3 row_newbcast:0 row_mask:0xf bank_mask:0xf
v_and_b32_dpp v15, v10, v4 row_newbcast:0 row_mask:0xf bank_mask:0xf
v_and_b32_dpp v16, v10, v5 row_newbcast:0 row_mask:0xf bank_mask:0xf
v_and_b32_dpp v17, v10, v6 row_newbcast:0 row_mask:0xf bank_mask:0xf
s_nop 1
v_mfma_f32_16x16x32_bf16 v[18:21], a[240:243], v[14:17], v[18:21]
v_and_b32_dpp v14, v10, v3 row_newbcast:4 row_mask:0xf bank_mask:0xf
v_and_b32_dpp v15, v10, v4 row_newbcast:4 row_mask:0xf bank_mask:0xf
v_and_b32_dpp v16, v10, v5 row_newbcast:4 row_mask:0xf bank_mask:0xf
v_and_b32_dpp v17, v10, v6 row_newbcast:4 row_mask:0xf bank_mask:0xf
s_nop 1
v_mfma_f32_16x16x32_bf16 v[18:21], a[244:247], v[14:17], v[18:21]
v_and_b32_dpp v14, v10, v3 row_newbcast:8 row_mask:0xf bank_mask:0xf
v_and_b32_dpp v15, v10, v4 row_newbcast:8 row_mask:0xf bank_mask:0xf
v_and_b32_dpp v16, v10, v5 row_newbcast:8 row_mask:0xf bank_mask:0xf
v_and_b32_dpp v17, v10, v6 row_newbcast:8 row_mask:0xf bank_mask:0xf
s_nop 1
v_mfma_f32_16x16x32_bf16 v[18:21], a[248:251], v[14:17], v[18:21]
v_and_b32_dpp v14, v10, v3 row_newbcast:12 row_mask:0xf bank_mask:0xf
v_and_b32_dpp v15, v10, v4 row_newbcast:12 row_mask:0xf bank_mask:0xf
v_and_b32_dpp v16, v10, v5 row_newbcast:12 row_mask:0xf bank_mask:0xf
v_and_b32_dpp v17, v10, v6 row_newbcast:12 row_mask:0xf bank_mask:0xf
s_nop 1
v_mfma_f32_16x16x32_bf16 v[18:21], a[252:255], v[14:17], v[18:21]
s_add_u32 s84, s84, LCONS8_%=-LCONS7_%=
s_addc_u32 s85, s85, 0
s_sub_u32 s82, s82, 1
s_setpc_b64 s[86:87]
LCONS8_%=:
v_mfma_f32_16x16x32_bf16 v[14:17], v[96:99], v[22:25], 0
v_mfma_f32_16x16x32_bf16 v[14:17], v[100:103], v[26:29], v[14:17]
v_mfma_f32_16x16x32_bf16 v[14:17], v[104:107], v[30:33], v[14:17]
v_mfma_f32_16x16x32_bf16 v[14:17], v[108:111], v[34:37], v[14:17]
s_nop 7
v_mul_f32_dpp v14, v14, v2 quad_perm:[0,0,0,0] row_mask:0xf bank_mask:0xf
v_mul_f32_dpp v15, v15, v2 quad_perm:[1,1,1,1] row_mask:0xf bank_mask:0xf
v_mul_f32_dpp v16, v16, v2 quad_perm:[2,2,2,2] row_mask:0xf bank_mask:0xf
v_mul_f32_dpp v17, v17, v2 quad_perm:[3,3,3,3] row_mask:0xf bank_mask:0xf
v_add_f32 v14, v14, v15
v_add_f32 v16, v16, v17
v_add_f32 v14, v14, v8
v_add_f32 v7, v14, v16
v_cmp_lt_f32 vcc, 0, v7
s_cbranch_vccnz Lnc_225_%=
s_cmp_lg_u32 s81, 0
s_cbranch_scc1 Lnc_225_%=
Lns_224_%=:
v_exp_f32 v7, v7
s_nop 0
v_add_f32 v9, v9, v7
v_cvt_pk_bf16_f32 v10, v7, v7
s_nop 1
v_and_b32_dpp v14, v10, v3 row_newbcast:0 row_mask:0xf bank_mask:0xf
v_and_b32_dpp v15, v10, v4 row_newbcast:0 row_mask:0xf bank_mask:0xf
v_and_b32_dpp v16, v10, v5 row_newbcast:0 row_mask:0xf bank_mask:0xf
v_and_b32_dpp v17, v10, v6 row_newbcast:0 row_mask:0xf bank_mask:0xf
s_nop 1
v_mfma_f32_16x16x32_bf16 v[18:21], v[112:115], v[14:17], v[18:21]
v_and_b32_dpp v14, v10, v3 row_newbcast:4 row_mask:0xf bank_mask:0xf
v_and_b32_dpp v15, v10, v4 row_newbcast:4 row_mask:0xf bank_mask:0xf
v_and_b32_dpp v16, v10, v5 row_newbcast:4 row_mask:0xf bank_mask:0xf
v_and_b32_dpp v17, v10, v6 row_newbcast:4 row_mask:0xf bank_mask:0xf
s_nop 1
v_mfma_f32_16x16x32_bf16 v[18:21], v[116:119], v[14:17], v[18:21]
v_and_b32_dpp v14, v10, v3 row_newbcast:8 row_mask:0xf bank_mask:0xf
v_and_b32_dpp v15, v10, v4 row_newbcast:8 row_mask:0xf bank_mask:0xf
v_and_b32_dpp v16, v10, v5 row_newbcast:8 row_mask:0xf bank_mask:0xf
v_and_b32_dpp v17, v10, v6 row_newbcast:8 row_mask:0xf bank_mask:0xf
s_nop 1
v_mfma_f32_16x16x32_bf16 v[18:21], v[120:123], v[14:17], v[18:21]
v_and_b32_dpp v14, v10, v3 row_newbcast:12 row_mask:0xf bank_mask:0xf
v_and_b32_dpp v15, v10, v4 row_newbcast:12 row_mask:0xf bank_mask:0xf
v_and_b32_dpp v16, v10, v5 row_newbcast:12 row_mask:0xf bank_mask:0xf
v_and_b32_dpp v17, v10, v6 row_newbcast:12 row_mask:0xf bank_mask:0xf
s_nop 1
v_mfma_f32_16x16x32_bf16 v[18:21], v[124:127], v[14:17], v[18:21]
s_add_u32 s84, s84, LCONS9_%=-LCONS8_%=
s_addc_u32 s85, s85, 0
s_sub_u32 s82, s82, 1
s_setpc_b64 s[86:87]
LCONS9_%=:
v_mfma_f32_16x16x32_bf16 v[14:17], v[128:131], v[22:25], 0
v_mfma_f32_16x16x32_bf16 v[14:17], v[132:135], v[26:29], v[14:17]
v_mfma_f32_16x16x32_bf16 v[14:17], v[136:139], v[30:33], v[14:17]
v_mfma_f32_16x16x32_bf16 v[14:17], v[140:143], v[34:37], v[14:17]
s_nop 7
v_mul_f32_dpp v14, v14, v2 quad_perm:[0,0,0,0] row_mask:0xf bank_mask:0xf
v_mul_f32_dpp v15, v15, v2 quad_perm:[1,1,1,1] row_mask:0xf bank_mask:0xf
v_mul_f32_dpp v16, v16, v2 quad_perm:[2,2,2,2] row_mask:0xf bank_mask:0xf
v_mul_f32_dpp v17, v17, v2 quad_perm:[3,3,3,3] row_mask:0xf bank_mask:0xf
v_add_f32 v14, v14, v15
v_add_f32 v16, v16, v17
v_add_f32 v14, v14, v8
v_add_f32 v7, v14, v16
v_cmp_lt_f32 vcc, 0, v7
s_cbranch_vccnz Lnc_227_%=
s_cmp_lg_u32 s81, 0
s_cbranch_scc1 Lnc_227_%=
Lns_226_%=:
v_exp_f32 v7, v7
s_nop 0
v_add_f32 v9, v9, v7
v_cvt_pk_bf16_f32 v10, v7, v7
s_nop 1
v_and_b32_dpp v14, v10, v3 row_newbcast:0 row_mask:0xf bank_mask:0xf
v_and_b32_dpp v15, v10, v4 row_newbcast:0 row_mask:0xf bank_mask:0xf
v_and_b32_dpp v16, v10, v5 row_newbcast:0 row_mask:0xf bank_mask:0xf
v_and_b32_dpp v17, v10, v6 row_newbcast:0 row_mask:0xf bank_mask:0xf
s_nop 1
v_mfma_f32_16x16x32_bf16 v[18:21], v[144:147], v[14:17], v[18:21]
v_and_b32_dpp v14, v10, v3 row_newbcast:4 row_mask:0xf bank_mask:0xf
v_and_b32_dpp v15, v10, v4 row_newbcast:4 row_mask:0xf bank_mask:0xf
v_and_b32_dpp v16, v10, v5 row_newbcast:4 row_mask:0xf bank_mask:0xf
v_and_b32_dpp v17, v10, v6 row_newbcast:4 row_mask:0xf bank_mask:0xf
s_nop 1
v_mfma_f32_16x16x32_bf16 v[18:21], v[148:151], v[14:17], v[18:21]
v_and_b32_dpp v14, v10, v3 row_newbcast:8 row_mask:0xf bank_mask:0xf
v_and_b32_dpp v15, v10, v4 row_newbcast:8 row_mask:0xf bank_mask:0xf
v_and_b32_dpp v16, v10, v5 row_newbcast:8 row_mask:0xf bank_mask:0xf
v_and_b32_dpp v17, v10, v6 row_newbcast:8 row_mask:0xf bank_mask:0xf
s_nop 1
v_mfma_f32_16x16x32_bf16 v[18:21], v[152:155], v[14:17], v[18:21]
v_and_b32_dpp v14, v10, v3 row_newbcast:12 row_mask:0xf bank_mask:0xf
v_and_b32_dpp v15, v10, v4 row_newbcast:12 row_mask:0xf bank_mask:0xf
v_and_b32_dpp v16, v10, v5 row_newbcast:12 row_mask:0xf bank_mask:0xf
v_and_b32_dpp v17, v10, v6 row_newbcast:12 row_mask:0xf bank_mask:0xf
s_nop 1
v_mfma_f32_16x16x32_bf16 v[18:21], v[156:159], v[14:17], v[18:21]
s_add_u32 s84, s84, LCONS10_%=-LCONS9_%=
s_addc_u32 s85, s85, 0
s_sub_u32 s82, s82, 1
s_setpc_b64 s[86:87]
LCONS10_%=:
v_mfma_f32_16x16x32_bf16 v[14:17], v[160:163], v[22:25], 0
v_mfma_f32_16x16x32_bf16 v[14:17], v[164:167], v[26:29], v[14:17]
v_mfma_f32_16x16x32_bf16 v[14:17], v[168:171], v[30:33], v[14:17]
v_mfma_f32_16x16x32_bf16 v[14:17], v[172:175], v[34:37], v[14:17]
s_nop 7
v_mul_f32_dpp v14, v14, v2 quad_perm:[0,0,0,0] row_mask:0xf bank_mask:0xf
v_mul_f32_dpp v15, v15, v2 quad_perm:[1,1,1,1] row_mask:0xf bank_mask:0xf
v_mul_f32_dpp v16, v16, v2 quad_perm:[2,2,2,2] row_mask:0xf bank_mask:0xf
v_mul_f32_dpp v17, v17, v2 quad_perm:[3,3,3,3] row_mask:0xf bank_mask:0xf
v_add_f32 v14, v14, v15
v_add_f32 v16, v16, v17
v_add_f32 v14, v14, v8
v_add_f32 v7, v14, v16
v_cmp_lt_f32 vcc, 0, v7
s_cbranch_vccnz Lnc_229_%=
s_cmp_lg_u32 s81, 0
s_cbranch_scc1 Lnc_229_%=
Lns_228_%=:
v_exp_f32 v7, v7
s_nop 0
v_add_f32 v9, v9, v7
v_cvt_pk_bf16_f32 v10, v7, v7
s_nop 1
v_and_b32_dpp v14, v10, v3 row_newbcast:0 row_mask:0xf bank_mask:0xf
v_and_b32_dpp v15, v10, v4 row_newbcast:0 row_mask:0xf bank_mask:0xf
v_and_b32_dpp v16, v10, v5 row_newbcast:0 row_mask:0xf bank_mask:0xf
v_and_b32_dpp v17, v10, v6 row_newbcast:0 row_mask:0xf bank_mask:0xf
s_nop 1
v_mfma_f32_16x16x32_bf16 v[18:21], v[176:179], v[14:17], v[18:21]
v_and_b32_dpp v14, v10, v3 row_newbcast:4 row_mask:0xf bank_mask:0xf
v_and_b32_dpp v15, v10, v4 row_newbcast:4 row_mask:0xf bank_mask:0xf
v_and_b32_dpp v16, v10, v5 row_newbcast:4 row_mask:0xf bank_mask:0xf
v_and_b32_dpp v17, v10, v6 row_newbcast:4 row_mask:0xf bank_mask:0xf
s_nop 1
v_mfma_f32_16x16x32_bf16 v[18:21], v[180:183], v[14:17], v[18:21]
v_and_b32_dpp v14, v10, v3 row_newbcast:8 row_mask:0xf bank_mask:0xf
v_and_b32_dpp v15, v10, v4 row_newbcast:8 row_mask:0xf bank_mask:0xf
v_and_b32_dpp v16, v10, v5 row_newbcast:8 row_mask:0xf bank_mask:0xf
v_and_b32_dpp v17, v10, v6 row_newbcast:8 row_mask:0xf bank_mask:0xf
s_nop 1
v_mfma_f32_16x16x32_bf16 v[18:21], v[184:187], v[14:17], v[18:21]
v_and_b32_dpp v14, v10, v3 row_newbcast:12 row_mask:0xf bank_mask:0xf
v_and_b32_dpp v15, v10, v4 row_newbcast:12 row_mask:0xf bank_mask:0xf
v_and_b32_dpp v16, v10, v5 row_newbcast:12 row_mask:0xf bank_mask:0xf
v_and_b32_dpp v17, v10, v6 row_newbcast:12 row_mask:0xf bank_mask:0xf
s_nop 1
v_mfma_f32_16x16x32_bf16 v[18:21], v[188:191], v[14:17], v[18:21]
s_add_u32 s84, s84, LCONS11_%=-LCONS10_%=
s_addc_u32 s85, s85, 0
s_sub_u32 s82, s82, 1
s_setpc_b64 s[86:87]
LCONS11_%=:
v_mfma_f32_16x16x32_bf16 v[14:17], v[192:195], v[22:25], 0
v_mfma_f32_16x16x32_bf16 v[14:17], v[196:199], v[26:29], v[14:17]
v_mfma_f32_16x16x32_bf16 v[14:17], v[200:203], v[30:33], v[14:17]
v_mfma_f32_16x16x32_bf16 v[14:17], v[204:207], v[34:37], v[14:17]
s_nop 7
v_mul_f32_dpp v14, v14, v2 quad_perm:[0,0,0,0] row_mask:0xf bank_mask:0xf
v_mul_f32_dpp v15, v15, v2 quad_perm:[1,1,1,1] row_mask:0xf bank_mask:0xf
v_mul_f32_dpp v16, v16, v2 quad_perm:[2,2,2,2] row_mask:0xf bank_mask:0xf
v_mul_f32_dpp v17, v17, v2 quad_perm:[3,3,3,3] row_mask:0xf bank_mask:0xf
v_add_f32 v14, v14, v15
v_add_f32 v16, v16, v17
v_add_f32 v14, v14, v8
v_add_f32 v7, v14, v16
v_cmp_lt_f32 vcc, 0, v7
s_cbranch_vccnz Lnc_231_%=
s_cmp_lg_u32 s81, 0
s_cbranch_scc1 Lnc_231_%=
Lns_230_%=:
v_exp_f32 v7, v7
s_nop 0
v_add_f32 v9, v9, v7
v_cvt_pk_bf16_f32 v10, v7, v7
s_nop 1
v_and_b32_dpp v14, v10, v3 row_newbcast:0 row_mask:0xf bank_mask:0xf
v_and_b32_dpp v15, v10, v4 row_newbcast:0 row_mask:0xf bank_mask:0xf
v_and_b32_dpp v16, v10, v5 row_newbcast:0 row_mask:0xf bank_mask:0xf
v_and_b32_dpp v17, v10, v6 row_newbcast:0 row_mask:0xf bank_mask:0xf
s_nop 1
v_mfma_f32_16x16x32_bf16 v[18:21], v[208:211], v[14:17], v[18:21]
v_and_b32_dpp v14, v10, v3 row_newbcast:4 row_mask:0xf bank_mask:0xf
v_and_b32_dpp v15, v10, v4 row_newbcast:4 row_mask:0xf bank_mask:0xf
v_and_b32_dpp v16, v10, v5 row_newbcast:4 row_mask:0xf bank_mask:0xf
v_and_b32_dpp v17, v10, v6 row_newbcast:4 row_mask:0xf bank_mask:0xf
s_nop 1
v_mfma_f32_16x16x32_bf16 v[18:21], v[212:215], v[14:17], v[18:21]
v_and_b32_dpp v14, v10, v3 row_newbcast:8 row_mask:0xf bank_mask:0xf
v_and_b32_dpp v15, v10, v4 row_newbcast:8 row_mask:0xf bank_mask:0xf
v_and_b32_dpp v16, v10, v5 row_newbcast:8 row_mask:0xf bank_mask:0xf
v_and_b32_dpp v17, v10, v6 row_newbcast:8 row_mask:0xf bank_mask:0xf
s_nop 1
v_mfma_f32_16x16x32_bf16 v[18:21], v[216:219], v[14:17], v[18:21]
v_and_b32_dpp v14, v10, v3 row_newbcast:12 row_mask:0xf bank_mask:0xf
v_and_b32_dpp v15, v10, v4 row_newbcast:12 row_mask:0xf bank_mask:0xf
v_and_b32_dpp v16, v10, v5 row_newbcast:12 row_mask:0xf bank_mask:0xf
v_and_b32_dpp v17, v10, v6 row_newbcast:12 row_mask:0xf bank_mask:0xf
s_nop 1
v_mfma_f32_16x16x32_bf16 v[18:21], v[220:223], v[14:17], v[18:21]
s_add_u32 s84, s84, LCONS12_%=-LCONS11_%=
s_addc_u32 s85, s85, 0
s_sub_u32 s82, s82, 1
s_setpc_b64 s[86:87]
LCONS12_%=:
v_mfma_f32_16x16x32_bf16 v[14:17], v[224:227], v[22:25], 0
v_mfma_f32_16x16x32_bf16 v[14:17], v[228:231], v[26:29], v[14:17]
v_mfma_f32_16x16x32_bf16 v[14:17], v[232:235], v[30:33], v[14:17]
v_mfma_f32_16x16x32_bf16 v[14:17], v[236:239], v[34:37], v[14:17]
s_nop 7
v_mul_f32_dpp v14, v14, v2 quad_perm:[0,0,0,0] row_mask:0xf bank_mask:0xf
v_mul_f32_dpp v15, v15, v2 quad_perm:[1,1,1,1] row_mask:0xf bank_mask:0xf
v_mul_f32_dpp v16, v16, v2 quad_perm:[2,2,2,2] row_mask:0xf bank_mask:0xf
v_mul_f32_dpp v17, v17, v2 quad_perm:[3,3,3,3] row_mask:0xf bank_mask:0xf
v_add_f32 v14, v14, v15
v_add_f32 v16, v16, v17
v_add_f32 v14, v14, v8
v_add_f32 v7, v14, v16
v_cmp_lt_f32 vcc, 0, v7
s_cbranch_vccnz Lnc_233_%=
s_cmp_lg_u32 s81, 0
s_cbranch_scc1 Lnc_233_%=
Lns_232_%=:
v_exp_f32 v7, v7
s_nop 0
v_add_f32 v9, v9, v7
v_cvt_pk_bf16_f32 v10, v7, v7
s_nop 1
v_and_b32_dpp v14, v10, v3 row_newbcast:0 row_mask:0xf bank_mask:0xf
v_and_b32_dpp v15, v10, v4 row_newbcast:0 row_mask:0xf bank_mask:0xf
v_and_b32_dpp v16, v10, v5 row_newbcast:0 row_mask:0xf bank_mask:0xf
v_and_b32_dpp v17, v10, v6 row_newbcast:0 row_mask:0xf bank_mask:0xf
s_nop 1
v_mfma_f32_16x16x32_bf16 v[18:21], v[240:243], v[14:17], v[18:21]
v_and_b32_dpp v14, v10, v3 row_newbcast:4 row_mask:0xf bank_mask:0xf
v_and_b32_dpp v15, v10, v4 row_newbcast:4 row_mask:0xf bank_mask:0xf
v_and_b32_dpp v16, v10, v5 row_newbcast:4 row_mask:0xf bank_mask:0xf
v_and_b32_dpp v17, v10, v6 row_newbcast:4 row_mask:0xf bank_mask:0xf
s_nop 1
v_mfma_f32_16x16x32_bf16 v[18:21], v[244:247], v[14:17], v[18:21]
v_and_b32_dpp v14, v10, v3 row_newbcast:8 row_mask:0xf bank_mask:0xf
v_and_b32_dpp v15, v10, v4 row_newbcast:8 row_mask:0xf bank_mask:0xf
v_and_b32_dpp v16, v10, v5 row_newbcast:8 row_mask:0xf bank_mask:0xf
v_and_b32_dpp v17, v10, v6 row_newbcast:8 row_mask:0xf bank_mask:0xf
s_nop 1
v_mfma_f32_16x16x32_bf16 v[18:21], v[248:251], v[14:17], v[18:21]
v_and_b32_dpp v14, v10, v3 row_newbcast:12 row_mask:0xf bank_mask:0xf
v_and_b32_dpp v15, v10, v4 row_newbcast:12 row_mask:0xf bank_mask:0xf
v_and_b32_dpp v16, v10, v5 row_newbcast:12 row_mask:0xf bank_mask:0xf
v_and_b32_dpp v17, v10, v6 row_newbcast:12 row_mask:0xf bank_mask:0xf
s_nop 1
v_mfma_f32_16x16x32_bf16 v[18:21], v[252:255], v[14:17], v[18:21]
s_add_u32 s84, s84, LCONS13_%=-LCONS12_%=
s_addc_u32 s85, s85, 0
s_sub_u32 s82, s82, 1
s_setpc_b64 s[86:87]
LCONS13_%=:
ds_read_b128 a[0:3], v11 offset:0
ds_read_b128 a[4:7], v11 offset:1024
ds_read_b128 a[8:11], v11 offset:2048
ds_read_b128 a[12:15], v11 offset:3072
ds_read_b128 a[16:19], v11 offset:4096
ds_read_b128 a[20:23], v11 offset:5120
ds_read_b128 a[24:27], v11 offset:6144
ds_read_b128 a[28:31], v11 offset:7168
s_waitcnt lgkmcnt(0)
v_mfma_f32_16x16x32_bf16 v[14:17], a[0:3], v[22:25], 0
v_mfma_f32_16x16x32_bf16 v[14:17], a[4:7], v[26:29], v[14:17]
v_mfma_f32_16x16x32_bf16 v[14:17], a[8:11], v[30:33], v[14:17]
v_mfma_f32_16x16x32_bf16 v[14:17], a[12:15], v[34:37], v[14:17]
s_nop 7
v_mul_f32_dpp v14, v14, v2 quad_perm:[0,0,0,0] row_mask:0xf bank_mask:0xf
v_mul_f32_dpp v15, v15, v2 quad_perm:[1,1,1,1] row_mask:0xf bank_mask:0xf
v_mul_f32_dpp v16, v16, v2 quad_perm:[2,2,2,2] row_mask:0xf bank_mask:0xf
v_mul_f32_dpp v17, v17, v2 quad_perm:[3,3,3,3] row_mask:0xf bank_mask:0xf
v_add_f32 v14, v14, v15
v_add_f32 v16, v16, v17
v_add_f32 v14, v14, v8
v_add_f32 v7, v14, v16
v_cmp_lt_f32 vcc, 0, v7
s_cbranch_vccnz Lnc_235_%=
s_cmp_lg_u32 s81, 0
s_cbranch_scc1 Lnc_235_%=
Lns_234_%=:
v_exp_f32 v7, v7
s_nop 0
v_add_f32 v9, v9, v7
v_cvt_pk_bf16_f32 v10, v7, v7
s_nop 1
v_and_b32_dpp v14, v10, v3 row_newbcast:0 row_mask:0xf bank_mask:0xf
v_and_b32_dpp v15, v10, v4 row_newbcast:0 row_mask:0xf bank_mask:0xf
v_and_b32_dpp v16, v10, v5 row_newbcast:0 row_mask:0xf bank_mask:0xf
v_and_b32_dpp v17, v10, v6 row_newbcast:0 row_mask:0xf bank_mask:0xf
s_nop 1
v_mfma_f32_16x16x32_bf16 v[18:21], a[16:19], v[14:17], v[18:21]
v_and_b32_dpp v14, v10, v3 row_newbcast:4 row_mask:0xf bank_mask:0xf
v_and_b32_dpp v15, v10, v4 row_newbcast:4 row_mask:0xf bank_mask:0xf
v_and_b32_dpp v16, v10, v5 row_newbcast:4 row_mask:0xf bank_mask:0xf
v_and_b32_dpp v17, v10, v6 row_newbcast:4 row_mask:0xf bank_mask:0xf
s_nop 1
v_mfma_f32_16x16x32_bf16 v[18:21], a[20:23], v[14:17], v[18:21]
v_and_b32_dpp v14, v10, v3 row_newbcast:8 row_mask:0xf bank_mask:0xf
v_and_b32_dpp v15, v10, v4 row_newbcast:8 row_mask:0xf bank_mask:0xf
v_and_b32_dpp v16, v10, v5 row_newbcast:8 row_mask:0xf bank_mask:0xf
v_and_b32_dpp v17, v10, v6 row_newbcast:8 row_mask:0xf bank_mask:0xf
s_nop 1
v_mfma_f32_16x16x32_bf16 v[18:21], a[24:27], v[14:17], v[18:21]
v_and_b32_dpp v14, v10, v3 row_newbcast:12 row_mask:0xf bank_mask:0xf
v_and_b32_dpp v15, v10, v4 row_newbcast:12 row_mask:0xf bank_mask:0xf
v_and_b32_dpp v16, v10, v5 row_newbcast:12 row_mask:0xf bank_mask:0xf
v_and_b32_dpp v17, v10, v6 row_newbcast:12 row_mask:0xf bank_mask:0xf
s_nop 1
v_mfma_f32_16x16x32_bf16 v[18:21], a[28:31], v[14:17], v[18:21]
s_add_u32 s84, s84, LCONS14_%=-LCONS13_%=
s_addc_u32 s85, s85, 0
s_sub_u32 s82, s82, 1
s_setpc_b64 s[86:87]
LCONS14_%=:
ds_read_b128 a[0:3], v11 offset:8192
ds_read_b128 a[4:7], v11 offset:9216
ds_read_b128 a[8:11], v11 offset:10240
ds_read_b128 a[12:15], v11 offset:11264
ds_read_b128 a[16:19], v11 offset:12288
ds_read_b128 a[20:23], v11 offset:13312
ds_read_b128 a[24:27], v11 offset:14336
ds_read_b128 a[28:31], v11 offset:15360
s_waitcnt lgkmcnt(0)
v_mfma_f32_16x16x32_bf16 v[14:17], a[0:3], v[22:25], 0
v_mfma_f32_16x16x32_bf16 v[14:17], a[4:7], v[26:29], v[14:17]
v_mfma_f32_16x16x32_bf16 v[14:17], a[8:11], v[30:33], v[14:17]
v_mfma_f32_16x16x32_bf16 v[14:17], a[12:15], v[34:37], v[14:17]
s_nop 7
v_mul_f32_dpp v14, v14, v2 quad_perm:[0,0,0,0] row_mask:0xf bank_mask:0xf
v_mul_f32_dpp v15, v15, v2 quad_perm:[1,1,1,1] row_mask:0xf bank_mask:0xf
v_mul_f32_dpp v16, v16, v2 quad_perm:[2,2,2,2] row_mask:0xf bank_mask:0xf
v_mul_f32_dpp v17, v17, v2 quad_perm:[3,3,3,3] row_mask:0xf bank_mask:0xf
v_add_f32 v14, v14, v15
v_add_f32 v16, v16, v17
v_add_f32 v14, v14, v8
v_add_f32 v7, v14, v16
v_cmp_lt_f32 vcc, 0, v7
s_cbranch_vccnz Lnc_237_%=
s_cmp_lg_u32 s81, 0
s_cbranch_scc1 Lnc_237_%=
Lns_236_%=:
v_exp_f32 v7, v7
s_nop 0
v_add_f32 v9, v9, v7
v_cvt_pk_bf16_f32 v10, v7, v7
s_nop 1
v_and_b32_dpp v14, v10, v3 row_newbcast:0 row_mask:0xf bank_mask:0xf
v_and_b32_dpp v15, v10, v4 row_newbcast:0 row_mask:0xf bank_mask:0xf
v_and_b32_dpp v16, v10, v5 row_newbcast:0 row_mask:0xf bank_mask:0xf
v_and_b32_dpp v17, v10, v6 row_newbcast:0 row_mask:0xf bank_mask:0xf
s_nop 1
v_mfma_f32_16x16x32_bf16 v[18:21], a[16:19], v[14:17], v[18:21]
v_and_b32_dpp v14, v10, v3 row_newbcast:4 row_mask:0xf bank_mask:0xf
v_and_b32_dpp v15, v10, v4 row_newbcast:4 row_mask:0xf bank_mask:0xf
v_and_b32_dpp v16, v10, v5 row_newbcast:4 row_mask:0xf bank_mask:0xf
v_and_b32_dpp v17, v10, v6 row_newbcast:4 row_mask:0xf bank_mask:0xf
s_nop 1
v_mfma_f32_16x16x32_bf16 v[18:21], a[20:23], v[14:17], v[18:21]
v_and_b32_dpp v14, v10, v3 row_newbcast:8 row_mask:0xf bank_mask:0xf
v_and_b32_dpp v15, v10, v4 row_newbcast:8 row_mask:0xf bank_mask:0xf
v_and_b32_dpp v16, v10, v5 row_newbcast:8 row_mask:0xf bank_mask:0xf
v_and_b32_dpp v17, v10, v6 row_newbcast:8 row_mask:0xf bank_mask:0xf
s_nop 1
v_mfma_f32_16x16x32_bf16 v[18:21], a[24:27], v[14:17], v[18:21]
v_and_b32_dpp v14, v10, v3 row_newbcast:12 row_mask:0xf bank_mask:0xf
v_and_b32_dpp v15, v10, v4 row_newbcast:12 row_mask:0xf bank_mask:0xf
v_and_b32_dpp v16, v10, v5 row_newbcast:12 row_mask:0xf bank_mask:0xf
v_and_b32_dpp v17, v10, v6 row_newbcast:12 row_mask:0xf bank_mask:0xf
s_nop 1
v_mfma_f32_16x16x32_bf16 v[18:21], a[28:31], v[14:17], v[18:21]
s_add_u32 s84, s84, LCONS15_%=-LCONS14_%=
s_addc_u32 s85, s85, 0
s_sub_u32 s82, s82, 1
s_setpc_b64 s[86:87]
LCONS15_%=:
ds_read_b128 a[0:3], v11 offset:16384
ds_read_b128 a[4:7], v11 offset:17408
ds_read_b128 a[8:11], v11 offset:18432
ds_read_b128 a[12:15], v11 offset:19456
ds_read_b128 a[16:19], v11 offset:20480
ds_read_b128 a[20:23], v11 offset:21504
ds_read_b128 a[24:27], v11 offset:22528
ds_read_b128 a[28:31], v11 offset:23552
s_waitcnt lgkmcnt(0)
v_mfma_f32_16x16x32_bf16 v[14:17], a[0:3], v[22:25], 0
v_mfma_f32_16x16x32_bf16 v[14:17], a[4:7], v[26:29], v[14:17]
v_mfma_f32_16x16x32_bf16 v[14:17], a[8:11], v[30:33], v[14:17]
v_mfma_f32_16x16x32_bf16 v[14:17], a[12:15], v[34:37], v[14:17]
s_nop 7
v_mul_f32_dpp v14, v14, v2 quad_perm:[0,0,0,0] row_mask:0xf bank_mask:0xf
v_mul_f32_dpp v15, v15, v2 quad_perm:[1,1,1,1] row_mask:0xf bank_mask:0xf
v_mul_f32_dpp v16, v16, v2 quad_perm:[2,2,2,2] row_mask:0xf bank_mask:0xf
v_mul_f32_dpp v17, v17, v2 quad_perm:[3,3,3,3] row_mask:0xf bank_mask:0xf
v_add_f32 v14, v14, v15
v_add_f32 v16, v16, v17
v_add_f32 v14, v14, v8
v_add_f32 v7, v14, v16
v_cmp_lt_f32 vcc, 0, v7
s_cbranch_vccnz Lnc_239_%=
s_cmp_lg_u32 s81, 0
s_cbranch_scc1 Lnc_239_%=
Lns_238_%=:
v_exp_f32 v7, v7
s_nop 0
v_add_f32 v9, v9, v7
v_cvt_pk_bf16_f32 v10, v7, v7
s_nop 1
v_and_b32_dpp v14, v10, v3 row_newbcast:0 row_mask:0xf bank_mask:0xf
v_and_b32_dpp v15, v10, v4 row_newbcast:0 row_mask:0xf bank_mask:0xf
v_and_b32_dpp v16, v10, v5 row_newbcast:0 row_mask:0xf bank_mask:0xf
v_and_b32_dpp v17, v10, v6 row_newbcast:0 row_mask:0xf bank_mask:0xf
s_nop 1
v_mfma_f32_16x16x32_bf16 v[18:21], a[16:19], v[14:17], v[18:21]
v_and_b32_dpp v14, v10, v3 row_newbcast:4 row_mask:0xf bank_mask:0xf
v_and_b32_dpp v15, v10, v4 row_newbcast:4 row_mask:0xf bank_mask:0xf
v_and_b32_dpp v16, v10, v5 row_newbcast:4 row_mask:0xf bank_mask:0xf
v_and_b32_dpp v17, v10, v6 row_newbcast:4 row_mask:0xf bank_mask:0xf
s_nop 1
v_mfma_f32_16x16x32_bf16 v[18:21], a[20:23], v[14:17], v[18:21]
v_and_b32_dpp v14, v10, v3 row_newbcast:8 row_mask:0xf bank_mask:0xf
v_and_b32_dpp v15, v10, v4 row_newbcast:8 row_mask:0xf bank_mask:0xf
v_and_b32_dpp v16, v10, v5 row_newbcast:8 row_mask:0xf bank_mask:0xf
v_and_b32_dpp v17, v10, v6 row_newbcast:8 row_mask:0xf bank_mask:0xf
s_nop 1
v_mfma_f32_16x16x32_bf16 v[18:21], a[24:27], v[14:17], v[18:21]
v_and_b32_dpp v14, v10, v3 row_newbcast:12 row_mask:0xf bank_mask:0xf
v_and_b32_dpp v15, v10, v4 row_newbcast:12 row_mask:0xf bank_mask:0xf
v_and_b32_dpp v16, v10, v5 row_newbcast:12 row_mask:0xf bank_mask:0xf
v_and_b32_dpp v17, v10, v6 row_newbcast:12 row_mask:0xf bank_mask:0xf
s_nop 1
v_mfma_f32_16x16x32_bf16 v[18:21], a[28:31], v[14:17], v[18:21]
s_add_u32 s84, s84, LCONS16_%=-LCONS15_%=
s_addc_u32 s85, s85, 0
s_sub_u32 s82, s82, 1
s_setpc_b64 s[86:87]
LCONS16_%=:
ds_read_b128 a[0:3], v11 offset:24576
ds_read_b128 a[4:7], v11 offset:25600
ds_read_b128 a[8:11], v11 offset:26624
ds_read_b128 a[12:15], v11 offset:27648
ds_read_b128 a[16:19], v11 offset:28672
ds_read_b128 a[20:23], v11 offset:29696
ds_read_b128 a[24:27], v11 offset:30720
ds_read_b128 a[28:31], v11 offset:31744
s_waitcnt lgkmcnt(0)
v_mfma_f32_16x16x32_bf16 v[14:17], a[0:3], v[22:25], 0
v_mfma_f32_16x16x32_bf16 v[14:17], a[4:7], v[26:29], v[14:17]
v_mfma_f32_16x16x32_bf16 v[14:17], a[8:11], v[30:33], v[14:17]
v_mfma_f32_16x16x32_bf16 v[14:17], a[12:15], v[34:37], v[14:17]
s_nop 7
v_mul_f32_dpp v14, v14, v2 quad_perm:[0,0,0,0] row_mask:0xf bank_mask:0xf
v_mul_f32_dpp v15, v15, v2 quad_perm:[1,1,1,1] row_mask:0xf bank_mask:0xf
v_mul_f32_dpp v16, v16, v2 quad_perm:[2,2,2,2] row_mask:0xf bank_mask:0xf
v_mul_f32_dpp v17, v17, v2 quad_perm:[3,3,3,3] row_mask:0xf bank_mask:0xf
v_add_f32 v14, v14, v15
v_add_f32 v16, v16, v17
v_add_f32 v14, v14, v8
v_add_f32 v7, v14, v16
v_cmp_lt_f32 vcc, 0, v7
s_cbranch_vccnz Lnc_241_%=
s_cmp_lg_u32 s81, 0
s_cbranch_scc1 Lnc_241_%=
Lns_240_%=:
v_exp_f32 v7, v7
s_nop 0
v_add_f32 v9, v9, v7
v_cvt_pk_bf16_f32 v10, v7, v7
s_nop 1
v_and_b32_dpp v14, v10, v3 row_newbcast:0 row_mask:0xf bank_mask:0xf
v_and_b32_dpp v15, v10, v4 row_newbcast:0 row_mask:0xf bank_mask:0xf
v_and_b32_dpp v16, v10, v5 row_newbcast:0 row_mask:0xf bank_mask:0xf
v_and_b32_dpp v17, v10, v6 row_newbcast:0 row_mask:0xf bank_mask:0xf
s_nop 1
v_mfma_f32_16x16x32_bf16 v[18:21], a[16:19], v[14:17], v[18:21]
v_and_b32_dpp v14, v10, v3 row_newbcast:4 row_mask:0xf bank_mask:0xf
v_and_b32_dpp v15, v10, v4 row_newbcast:4 row_mask:0xf bank_mask:0xf
v_and_b32_dpp v16, v10, v5 row_newbcast:4 row_mask:0xf bank_mask:0xf
v_and_b32_dpp v17, v10, v6 row_newbcast:4 row_mask:0xf bank_mask:0xf
s_nop 1
v_mfma_f32_16x16x32_bf16 v[18:21], a[20:23], v[14:17], v[18:21]
v_and_b32_dpp v14, v10, v3 row_newbcast:8 row_mask:0xf bank_mask:0xf
v_and_b32_dpp v15, v10, v4 row_newbcast:8 row_mask:0xf bank_mask:0xf
v_and_b32_dpp v16, v10, v5 row_newbcast:8 row_mask:0xf bank_mask:0xf
v_and_b32_dpp v17, v10, v6 row_newbcast:8 row_mask:0xf bank_mask:0xf
s_nop 1
v_mfma_f32_16x16x32_bf16 v[18:21], a[24:27], v[14:17], v[18:21]
v_and_b32_dpp v14, v10, v3 row_newbcast:12 row_mask:0xf bank_mask:0xf
v_and_b32_dpp v15, v10, v4 row_newbcast:12 row_mask:0xf bank_mask:0xf
v_and_b32_dpp v16, v10, v5 row_newbcast:12 row_mask:0xf bank_mask:0xf
v_and_b32_dpp v17, v10, v6 row_newbcast:12 row_mask:0xf bank_mask:0xf
s_nop 1
v_mfma_f32_16x16x32_bf16 v[18:21], a[28:31], v[14:17], v[18:21]
s_add_u32 s84, s84, LCONS17_%=-LCONS16_%=
s_addc_u32 s85, s85, 0
s_sub_u32 s82, s82, 1
s_setpc_b64 s[86:87]
LCONS17_%=:
ds_read_b128 a[0:3], v11 offset:32768
ds_read_b128 a[4:7], v11 offset:33792
ds_read_b128 a[8:11], v11 offset:34816
ds_read_b128 a[12:15], v11 offset:35840
ds_read_b128 a[16:19], v11 offset:36864
ds_read_b128 a[20:23], v11 offset:37888
ds_read_b128 a[24:27], v11 offset:38912
ds_read_b128 a[28:31], v11 offset:39936
s_waitcnt lgkmcnt(0)
v_mfma_f32_16x16x32_bf16 v[14:17], a[0:3], v[22:25], 0
v_mfma_f32_16x16x32_bf16 v[14:17], a[4:7], v[26:29], v[14:17]
v_mfma_f32_16x16x32_bf16 v[14:17], a[8:11], v[30:33], v[14:17]
v_mfma_f32_16x16x32_bf16 v[14:17], a[12:15], v[34:37], v[14:17]
s_nop 7
v_mul_f32_dpp v14, v14, v2 quad_perm:[0,0,0,0] row_mask:0xf bank_mask:0xf
v_mul_f32_dpp v15, v15, v2 quad_perm:[1,1,1,1] row_mask:0xf bank_mask:0xf
v_mul_f32_dpp v16, v16, v2 quad_perm:[2,2,2,2] row_mask:0xf bank_mask:0xf
v_mul_f32_dpp v17, v17, v2 quad_perm:[3,3,3,3] row_mask:0xf bank_mask:0xf
v_add_f32 v14, v14, v15
v_add_f32 v16, v16, v17
v_add_f32 v14, v14, v8
v_add_f32 v7, v14, v16
v_cmp_lt_f32 vcc, 0, v7
s_cbranch_vccnz Lnc_243_%=
s_cmp_lg_u32 s81, 0
s_cbranch_scc1 Lnc_243_%=
Lns_242_%=:
v_exp_f32 v7, v7
s_nop 0
v_add_f32 v9, v9, v7
v_cvt_pk_bf16_f32 v10, v7, v7
s_nop 1
v_and_b32_dpp v14, v10, v3 row_newbcast:0 row_mask:0xf bank_mask:0xf
v_and_b32_dpp v15, v10, v4 row_newbcast:0 row_mask:0xf bank_mask:0xf
v_and_b32_dpp v16, v10, v5 row_newbcast:0 row_mask:0xf bank_mask:0xf
v_and_b32_dpp v17, v10, v6 row_newbcast:0 row_mask:0xf bank_mask:0xf
s_nop 1
v_mfma_f32_16x16x32_bf16 v[18:21], a[16:19], v[14:17], v[18:21]
v_and_b32_dpp v14, v10, v3 row_newbcast:4 row_mask:0xf bank_mask:0xf
v_and_b32_dpp v15, v10, v4 row_newbcast:4 row_mask:0xf bank_mask:0xf
v_and_b32_dpp v16, v10, v5 row_newbcast:4 row_mask:0xf bank_mask:0xf
v_and_b32_dpp v17, v10, v6 row_newbcast:4 row_mask:0xf bank_mask:0xf
s_nop 1
v_mfma_f32_16x16x32_bf16 v[18:21], a[20:23], v[14:17], v[18:21]
v_and_b32_dpp v14, v10, v3 row_newbcast:8 row_mask:0xf bank_mask:0xf
v_and_b32_dpp v15, v10, v4 row_newbcast:8 row_mask:0xf bank_mask:0xf
v_and_b32_dpp v16, v10, v5 row_newbcast:8 row_mask:0xf bank_mask:0xf
v_and_b32_dpp v17, v10, v6 row_newbcast:8 row_mask:0xf bank_mask:0xf
s_nop 1
v_mfma_f32_16x16x32_bf16 v[18:21], a[24:27], v[14:17], v[18:21]
v_and_b32_dpp v14, v10, v3 row_newbcast:12 row_mask:0xf bank_mask:0xf
v_and_b32_dpp v15, v10, v4 row_newbcast:12 row_mask:0xf bank_mask:0xf
v_and_b32_dpp v16, v10, v5 row_newbcast:12 row_mask:0xf bank_mask:0xf
v_and_b32_dpp v17, v10, v6 row_newbcast:12 row_mask:0xf bank_mask:0xf
s_nop 1
v_mfma_f32_16x16x32_bf16 v[18:21], a[28:31], v[14:17], v[18:21]
s_sub_u32 s82, s82, 1
s_setpc_b64 s[86:87]
Lnc_203_%=:
s_mov_b32 s81, 0
s_swappc_b64 s[66:67], s[64:65]
s_branch Lns_202_%=
Lnc_205_%=:
s_mov_b32 s81, 0
s_swappc_b64 s[66:67], s[64:65]
s_branch Lns_204_%=
Lnc_207_%=:
s_mov_b32 s81, 0
s_swappc_b64 s[66:67], s[64:65]
s_branch Lns_206_%=
Lnc_209_%=:
s_mov_b32 s81, 0
s_swappc_b64 s[66:67], s[64:65]
s_branch Lns_208_%=
Lnc_211_%=:
s_mov_b32 s81, 0
s_swappc_b64 s[66:67], s[64:65]
s_branch Lns_210_%=
Lnc_213_%=:
s_mov_b32 s81, 0
s_swappc_b64 s[66:67], s[64:65]
s_branch Lns_212_%=
Lnc_215_%=:
s_mov_b32 s81, 0
s_swappc_b64 s[66:67], s[64:65]
s_branch Lns_214_%=
Lnc_217_%=:
s_mov_b32 s81, 0
s_swappc_b64 s[66:67], s[64:65]
s_branch Lns_216_%=
Lnc_219_%=:
s_mov_b32 s81, 0
s_swappc_b64 s[66:67], s[64:65]
s_branch Lns_218_%=
Lnc_221_%=:
s_mov_b32 s81, 0
s_swappc_b64 s[66:67], s[64:65]
s_branch Lns_220_%=
Lnc_223_%=:
s_mov_b32 s81, 0
s_swappc_b64 s[66:67], s[64:65]
s_branch Lns_222_%=
Lnc_225_%=:
s_mov_b32 s81, 0
s_swappc_b64 s[66:67], s[64:65]
s_branch Lns_224_%=
Lnc_227_%=:
s_mov_b32 s81, 0
s_swappc_b64 s[66:67], s[64:65]
s_branch Lns_226_%=
Lnc_229_%=:
s_mov_b32 s81, 0
s_swappc_b64 s[66:67], s[64:65]
s_branch Lns_228_%=
Lnc_231_%=:
s_mov_b32 s81, 0
s_swappc_b64 s[66:67], s[64:65]
s_branch Lns_230_%=
Lnc_233_%=:
s_mov_b32 s81, 0
s_swappc_b64 s[66:67], s[64:65]
s_branch Lns_232_%=
Lnc_235_%=:
s_mov_b32 s81, 0
s_swappc_b64 s[66:67], s[64:65]
s_branch Lns_234_%=
Lnc_237_%=:
s_mov_b32 s81, 0
s_swappc_b64 s[66:67], s[64:65]
s_branch Lns_236_%=
Lnc_239_%=:
s_mov_b32 s81, 0
s_swappc_b64 s[66:67], s[64:65]
s_branch Lns_238_%=
Lnc_241_%=:
s_mov_b32 s81, 0
s_swappc_b64 s[66:67], s[64:65]
s_branch Lns_240_%=
Lnc_243_%=:
s_mov_b32 s81, 0
s_swappc_b64 s[66:67], s[64:65]
s_branch Lns_242_%=
LSLOW_%=:
v_max_f32_dpp v12, v7, v7 row_ror:4 row_mask:0xf bank_mask:0xf
s_nop 1
v_max_f32_dpp v12, v12, v12 row_ror:8 row_mask:0xf bank_mask:0xf
s_nop 0
v_readlane_b32 s50, v12, 0
v_readlane_b32 s51, v12, 16
v_readlane_b32 s52, v12, 32
v_readlane_b32 s53, v12, 48
s_nop 1
v_mov_b32 v12, s50
v_max_f32 v12, s51, v12
v_max_f32 v12, s52, v12
v_max_f32 v12, s53, v12
v_sub_f32 v7, v7, v12
v_sub_f32 v8, v8, v12
v_exp_f32_e64 v10, -v12
s_nop 0
v_min_f32 v10, 1.0, v10
v_mul_f32 v9, v9, v10
v_mul_f32 v18, v18, v10
v_mul_f32 v19, v19, v10
v_mul_f32 v20, v20, v10
v_mul_f32 v21, v21, v10
s_nop 1
s_setpc_b64 s[66:67]
LEND_%=:
)ASM"
      :
      : "s"((unsigned)kb), "s"((unsigned)(kb >> 32)), "s"((unsigned)vb), "s"((unsigned)(vb >> 32)), "s"((unsigned)wb), "s"((unsigned)(wb >> 32)),
        "s"(h), "s"(s), "s"(w), "s"(ldsb)
      : "v0","v1","v2","v3","v4","v5","v6","v7","v8","v9","v10","v11","v12","v13","v14","v15","v16","v17","v18","v19","v20","v21","v22","v23","v24","v25","v26","v27","v28","v29","v30","v31","v32","v33","v34","v35","v36","v37","v38","v39","v40","v41","v42","v43","v44","v45","v46","v47","v48","v49","v50","v51","v52","v53","v54","v55","v56","v57","v58","v59","v60","v61","v62","v63","v64","v65","v66","v67","v68","v69","v70","v71","v72","v73","v74","v75","v76","v77","v78","v79","v80","v81","v82","v83","v84","v85","v86","v87","v88","v89","v90","v91","v92","v93","v94","v95","v96","v97","v98","v99","v100","v101","v102","v103","v104","v105","v106","v107","v108","v109","v110","v111","v112","v113","v114","v115","v116","v117","v118","v119","v120","v121","v122","v123","v124","v125","v126","v127","v128","v129","v130","v131","v132","v133","v134","v135","v136","v137","v138","v139","v140","v141","v142","v143","v144","v145","v146","v147","v148","v149","v150","v151","v152","v153","v154","v155","v156","v157","v158","v159","v160","v161","v162","v163","v164","v165","v166","v167","v168","v169","v170","v171","v172","v173","v174","v175","v176","v177","v178","v179","v180","v181","v182","v183","v184","v185","v186","v187","v188","v189","v190","v191","v192","v193","v194","v195","v196","v197","v198","v199","v200","v201","v202","v203","v204","v205","v206","v207","v208","v209","v210","v211","v212","v213","v214","v215","v216","v217","v218","v219","v220","v221","v222","v223","v224","v225","v226","v227","v228","v229","v230","v231","v232","v233","v234","v235","v236","v237","v238","v239","v240","v241","v242","v243","v244","v245","v246","v247","v248","v249","v250","v251","v252","v253","v254","v255","a0","a1","a2","a3","a4","a5","a6","a7","a8","a9","a10","a11","a12","a13","a14","a15","a16","a17","a18","a19","a20","a21","a22","a23","a24","a25","a26","a27","a28","a29","a30","a31","a32","a33","a34","a35","a36","a37","a38","a39","a40","a41","a42","a43","a44","a45","a46","a47","a48","a49","a50","a51","a52","a53","a54","a55","a56","a57","a58","a59","a60","a61","a62","a63","a64","a65","a66","a67","a68","a69","a70","a71","a72","a73","a74","a75","a76","a77","a78","a79","a80","a81","a82","a83","a84","a85","a86","a87","a88","a89","a90","a91","a92","a93","a94","a95","a96","a97","a98","a99","a100","a101","a102","a103","a104","a105","a106","a107","a108","a109","a110","a111","a112","a113","a114","a115","a116","a117","a118","a119","a120","a121","a122","a123","a124","a125","a126","a127","a128","a129","a130","a131","a132","a133","a134","a135","a136","a137","a138","a139","a140","a141","a142","a143","a144","a145","a146","a147","a148","a149","a150","a151","a152","a153","a154","a155","a156","a157","a158","a159","a160","a161","a162","a163","a164","a165","a166","a167","a168","a169","a170","a171","a172","a173","a174","a175","a176","a177","a178","a179","a180","a181","a182","a183","a184","a185","a186","a187","a188","a189","a190","a191","a192","a193","a194","a195","a196","a197","a198","a199","a200","a201","a202","a203","a204","a205","a206","a207","a208","a209","a210","a211","a212","a213","a214","a215","a216","a217","a218","a219","a220","a221","a222","a223","a224","a225","a226","a227","a228","a229","a230","a231","a232","a233","a234","a235","a236","a237","a238","a239","a240","a241","a242","a243","a244","a245","a246","a247","a248","a249","a250","a251","a252","a253","a254","a255","s8","s9","s10","s11","s12","s13","s14","s15","s16","s17","s18","s19","s20","s21","s22","s23","s24","s25","s26","s27","s28","s29","s30","s31","s34","s35","s36","s37","s38","s39","s40","s41","s42","s43","s44","s45","s46","s47","s48","s49","s50","s51","s52","s53","s54","s55","s56","s57","s58","s59","s60","s61","s62","s63","s64","s65","s66","s67","s68","s69","s70","s71","s72","s73","s74","s75","s76","s77","s78","s79","s80","s81","s82","s83","s84","s85","s86","s87","s88","s89","s90","s91","s92","s93","s95","vcc","scc","memory");
  }
}

extern "C" void kernel_launch(void* const* d_in, const int* in_sizes, int n_in, void* d_out, int out_size, void* d_ws, size_t ws_size, hipStream_t stream) {
  mega_kernel<<<256, 256, 0, stream>>>((const u16*)d_in[0], (const u16*)d_in[1], (const u16*)d_in[2], (const u16*)d_in[3], (const u16*)d_in[4],
                                       (const u16*)d_in[6], (const u16*)d_in[7], (const u16*)d_in[8], (u16*)d_out, (char*)d_ws);
}
```
